```python
import torch

def _softmax(x, axis=-1):
    return torch.softmax(x, dim=axis)
def _gelu(x):
    return torch.nn.functional.gelu(x, approximate="tanh")
def _silu(x):
    return torch.nn.functional.silu(x)
def _sigmoid(x):
    return torch.sigmoid(x)
def _layernorm(x, axis=-1, eps=1e-5):
    m = x.mean(dim=axis, keepdim=True)
    v = x.var(dim=axis, keepdim=True, unbiased=False)
    return (x - m) / torch.sqrt(v + eps)
def _scatter_add(tgt, idx, src, axis=0):
    return tgt.clone().index_add_(axis, idx, src)
def _segment_sum(src, seg, n):
    out = torch.zeros((n,) + src.shape[1:], dtype=src.dtype)
    return out.index_add_(0, seg, src)
def _pad(x, pw):
    flat = tuple(v for pair in reversed(pw) for v in pair)
    return torch.nn.functional.pad(x, flat)

kernel_name = 'parallel_sum__spectral_fnet__dense_activation_relu__prompt'

def setup_inputs():

    _w = (lambda a: (lambda t: t.to(torch.bfloat16) if t.is_floating_point() else t)(torch.as_tensor(a)))
    def _A_setup():
        B, S, D = 1, 2048, 448
        ns = torch.arange(S)[:, None] * torch.arange(S)[None, :]
        nd = torch.arange(D)[:, None] * torch.arange(D)[None, :]
        Ws_re = torch.cos(-2.0 * torch.pi * ns / S).to(float)
        Ws_im = torch.sin(-2.0 * torch.pi * ns / S).to(float)
        Wd_re = torch.cos(-2.0 * torch.pi * nd / D).to(float)
        Wd_im = torch.sin(-2.0 * torch.pi * nd / D).to(float)
        return {
            "x": _w((lambda *s: torch.randn(*s, dtype=torch.bfloat16) * 0.05)(B, S, D)),
            "dft_seq_re": _w(Ws_re),
            "dft_seq_im": _w(Ws_im),
            "dft_hid_re": _w(Wd_re),
            "dft_hid_im": _w(Wd_im),
        }
    _A_in = _A_setup()

    def _B_setup():
        B, S, D, Dff = 1, 2048, 448, 1792
        return {
            "x": _w((lambda *s: torch.randn(*s, dtype=torch.bfloat16) * 0.05)(B, S, D)),
            "w1": _w((lambda *s: torch.randn(*s, dtype=torch.bfloat16) * 0.4)(D, Dff)),
            "w2": _w((lambda *s: torch.randn(*s, dtype=torch.bfloat16) * 0.4)(Dff, D)),
        }
    _B_in = _B_setup()

    return {
        "A_x": _A_in["x"],
        "A_dft_seq_re": _A_in["dft_seq_re"],
        "A_dft_seq_im": _A_in["dft_seq_im"],
        "A_dft_hid_re": _A_in["dft_hid_re"],
        "A_dft_hid_im": _A_in["dft_hid_im"],
        "B_x": _B_in["x"],
        "B_w1": _B_in["w1"],
        "B_w2": _B_in["w2"],
    }

def reference(A_x, A_dft_seq_re, A_dft_seq_im, A_dft_hid_re, A_dft_hid_im, B_x, B_w1, B_w2):
    def _A_ref(x, dft_seq_re, dft_seq_im, dft_hid_re, dft_hid_im):
        a = torch.einsum("bsd,ed->bse", x, dft_hid_re)
        b = torch.einsum("bsd,ed->bse", x, dft_hid_im)
        y = torch.einsum("ts,bse->bte", dft_seq_re, a) - torch.einsum("ts,bse->bte", dft_seq_im, b)
        return y

    def _B_ref(x, w1, w2):
        h = torch.einsum("bsd,df->bsf", x, w1)
        zero = torch.zeros_like(h)
        h = torch.maximum(h, zero)
        return torch.einsum("bsf,fd->bsd", h, w2)

    x = A_x
    xn = _layernorm(x, axis=-1)
    return x + _A_ref(xn, A_dft_seq_re, A_dft_seq_im, A_dft_hid_re, A_dft_hid_im) + _B_ref(xn, B_w1, B_w2)
```

```cpp
#include <hip/hip_runtime.h>
#include <hip/hip_bf16.h>
typedef __attribute__((ext_vector_type(8))) short bf16x8;
typedef __attribute__((ext_vector_type(4))) float f32x4;
typedef unsigned short u16;
constexpr int S=2048, D=448, FF=1792;
__device__ __forceinline__ float bf2f(u16 v){ return __uint_as_float(((unsigned)v)<<16); }
__device__ __forceinline__ u16 f2bf(float f){ __hip_bfloat16 b = __float2bfloat16(f); return *(u16*)&b; }

__global__ void k_ln(const u16* x, u16* xn){
  int row = blockIdx.x*4 + (threadIdx.x>>6); int lane = threadIdx.x&63;
  float v[7]; float s=0;
  for(int i=0;i<7;i++){ v[i]=bf2f(x[row*D+i*64+lane]); s+=v[i]; }
  for(int o=32;o;o>>=1) s+=__shfl_xor(s,o);
  float m=s/D; float q=0;
  for(int i=0;i<7;i++){ float d=v[i]-m; q+=d*d; }
  for(int o=32;o;o>>=1) q+=__shfl_xor(q,o);
  float r=rsqrtf(q/D+1e-5f);
  for(int i=0;i<7;i++) xn[row*D+i*64+lane]=f2bf((v[i]-m)*r);
}
__global__ void k_s1(const u16* xn, const u16* wre, const u16* wim, const u16* w1, u16* abT, u16* h){
  int wid = blockIdx.x*4 + (threadIdx.x>>6); int lane=threadIdx.x&63;
  int tn = wid % 168, tm = wid / 168; int r=lane&15, g=lane>>4;
  f32x4 acc={0,0,0,0};
  int n0=tn*16;
  for(int k=0;k<D;k+=32){
    bf16x8 a=*(const bf16x8*)(xn+(tm*16+r)*D+k+g*8);
    bf16x8 b;
    if(n0<448) b=*(const bf16x8*)(wre+(n0+r)*D+k+g*8);
    else if(n0<896) b=*(const bf16x8*)(wim+(n0-448+r)*D+k+g*8);
    else { for(int j=0;j<8;j++) b[j]=w1[(k+g*8+j)*FF+(n0-896)+r]; }
    acc=__builtin_amdgcn_mfma_f32_16x16x32_bf16(a,b,acc,0,0,0);
  }
  for(int j=0;j<4;j++){
    int row=tm*16+g*4+j, col=n0+r;
    if(n0<448) abT[col*S+row]=f2bf(acc[j]);
    else if(n0<896) abT[col*S+row]=f2bf(-acc[j]);
    else h[row*FF+col-896]=f2bf(fmaxf(acc[j],0.f));
  }
}
__global__ void k_s2(const u16* x, const u16* cr, const u16* ci, const u16* abT, const u16* h, const u16* w2, u16* out){
  int wid = blockIdx.x*4 + (threadIdx.x>>6); int lane=threadIdx.x&63;
  int tn = wid % 28, tm = wid / 28; int r=lane&15, g=lane>>4;
  f32x4 acc={0,0,0,0};
  for(int k=0;k<S;k+=32){
    bf16x8 a=*(const bf16x8*)(cr+(tm*16+r)*S+k+g*8);
    bf16x8 b=*(const bf16x8*)(abT+(tn*16+r)*S+k+g*8);
    acc=__builtin_amdgcn_mfma_f32_16x16x32_bf16(a,b,acc,0,0,0);
    a=*(const bf16x8*)(ci+(tm*16+r)*S+k+g*8);
    b=*(const bf16x8*)(abT+(448+tn*16+r)*S+k+g*8);
    acc=__builtin_amdgcn_mfma_f32_16x16x32_bf16(a,b,acc,0,0,0);
  }
  for(int k=0;k<FF;k+=32){
    bf16x8 a=*(const bf16x8*)(h+(tm*16+r)*FF+k+g*8);
    bf16x8 b; for(int j=0;j<8;j++) b[j]=w2[(k+g*8+j)*D+tn*16+r];
    acc=__builtin_amdgcn_mfma_f32_16x16x32_bf16(a,b,acc,0,0,0);
  }
  for(int j=0;j<4;j++){ int row=tm*16+g*4+j, col=tn*16+r; out[row*D+col]=f2bf(acc[j]+bf2f(x[row*D+col])); }
}
extern "C" void kernel_launch(void* const* d_in, const int* in_sizes, int n_in, void* d_out, int out_size, void* d_ws, size_t ws_size, hipStream_t stream) {
  const u16* x=(const u16*)d_in[0]; const u16* cr=(const u16*)d_in[1]; const u16* ci=(const u16*)d_in[2];
  const u16* wre=(const u16*)d_in[3]; const u16* wim=(const u16*)d_in[4]; const u16* w1=(const u16*)d_in[6]; const u16* w2=(const u16*)d_in[7];
  u16* xn=(u16*)d_ws; u16* abT=xn+S*D; u16* h=abT+896*S;
  k_ln<<<S/4,256,0,stream>>>(x,xn);
  k_s1<<<128*168/4,256,0,stream>>>(xn,wre,wim,w1,abT,h);
  k_s2<<<128*28/4,256,0,stream>>>(x,cr,ci,abT,h,w2,(u16*)d_out);
}
```

```cpp
#include <hip/hip_runtime.h>
__global__ __launch_bounds__(256,1) __attribute__((aligned(65536))) void k_fused(const void* x,const void* cr,const void* ci,const void* wre,const void* wim,const void* w1,const void* w2,void* out,void* ws){
  asm volatile(
  "s_load_dwordx16 s[80:95], %0, 0x0\n"
  "s_load_dwordx2 s[96:97], %0, 0x40\n"
  "v_lshrrev_b32 v24, 6, %2\n"
  "v_and_b32 v1, 63, %2\n"
  "s_and_b32 s21, %1, 3\n"
  "s_bfe_u32 s28, %1, 0x10003\n"
  "v_readfirstlane_b32 s24, v24\n"
  "s_lshl1_add_u32 s21, s21, s28\n"
  "s_lshr_b32 s23, %1, 5\n"
  "s_bfe_u32 s28, %1, 0x10002\n"
  "s_lshl1_add_u32 s23, s23, s28\n"
  "s_bfe_u32 s22, %1, 0x10004\n"
  "s_lshl1_add_u32 s20, s23, s22\n"
  "s_lshl3_add_u32 s20, s20, s21\n"
  "s_lshr_b32 s79, %1, 4\n"
  "s_lshr_b32 s75, s20, 3\n"
  "s_lshl2_add_u32 s75, s75, s24\n"
  "s_lshl2_add_u32 s25, s79, s24\n"
  "s_getpc_b64 s[26:27]\n"
  "Lpc%=:\n"
  "s_cmp_eq_u32 s21, 7\n"
  "s_cselect_b32 s28, Lnon%=-Lpc%=, 0\n"
  "s_lshl_b32 s29, s25, 7\n"
  "s_add_u32 s28, s28, s29\n"
  "v_lshlrev_b32 v22, 13, v1\n"
  "v_add_u32 v22, s28, v22\n"
  "v_min_u32 v22, Lend%=-Lpc%=, v22\n"
  "s_mov_b64 exec, 31\n"
  "global_load_dword a163, v22, s[26:27]\n"
  "s_mov_b64 exec, -1\n"
  "s_mul_i32 s25, s25, 28\n"
  "v_add_u32 v28, s25, v1\n"
  "v_lshrrev_b32 v24, 3, v1\n"
  "v_and_b32 v25, 7, v1\n"
  "v_xor_b32 v25, v25, v24\n"
  "v_lshlrev_b32 v25, 4, v25\n"
  "v_and_b32 v26, 4, v24\n"
  "v_add_u32 v26, v26, v24\n"
  "s_movk_i32 s26, 896\n"
  "v_mad_u32_u24 v2, v26, s26, v25\n"
  "s_lshl2_add_u32 s27, s23, s24\n"
  "s_mul_i32 s64, s27, 28672\n"
  "s_lshl_b32 s34, s24, 14\n"
  "s_mov_b32 s38, 28672\n"
  "s_mov_b32 s39, 0x20000\n"
  "v_mad_u32_u24 v4, v24, s26, v25\n"
  "v_lshl_add_u32 v3, v26, 12, v25\n"
  "s_lshl_b32 s28, s22, 8\n"
  "v_or_b32 v3, s28, v3\n"
  "v_and_b32 v24, 15, v1\n"
  "v_lshrrev_b32 v25, 4, v1\n"
  "v_and_b32 v26, 7, v1\n"
  "v_xor_b32 v26, v26, v25\n"
  "v_lshlrev_b32 v26, 4, v26\n"
  "v_lshl_add_u32 v26, v24, 7, v26\n"
  "v_add_u32 v9, s34, v26\n"
  "v_xor_b32 v10, 64, v9\n"
  "v_add_u32 v11, 65536, v26\n"
  "v_xor_b32 v12, 64, v11\n"
  "s_mul_i32 s35, s24, 3584\n"
  "s_add_u32 s35, s35, 65536\n"
  "s_lshl_b32 s68, s24, 12\n"
  "s_add_u32 s68, s68, 65536\n"
  "s_lshl_b32 s69, s24, 11\n"
  "s_add_u32 s69, s69, 65536\n"
  "v_add_u32 v30, s68, v26\n"
  "v_add_u32 v30, 81920, v30\n"
  "v_xor_b32 v31, 64, v30\n"
  "v_lshrrev_b32 v26, 2, v24\n"
  "v_lshl_add_u32 v26, v25, 3, v26\n"
  "s_movk_i32 s28, 448\n"
  "v_and_b32 v27, 3, v1\n"
  "v_lshlrev_b32 v27, 3, v27\n"
  "v_mad_u32_u24 v26, v26, s28, v27\n"
  "v_add_u32 v26, 65536, v26\n"
  "v_and_b32 v25, 1, v25\n"
  "v_lshlrev_b32 v25, 5, v25\n"
  "v_add_u32 v13, v26, v25\n"
  "v_sub_u32 v14, v26, v25\n"
  "v_add_u32 v15, 0x10000, v13\n"
  "v_add_u32 v16, 0x10000, v14\n"
  "v_lshlrev_b32 v17, 4, v1\n"
  "v_add_u32 v18, 65536, v17\n"
  "v_add_u32 v19, 131072, v17\n"
  "v_mov_b32 v24, 0x3f803f80\n"
  "v_accvgpr_write_b32 a180, v24\n"
  "v_accvgpr_write_b32 a181, v24\n"
  "v_accvgpr_write_b32 a182, v24\n"
  "v_accvgpr_write_b32 a183, v24\n"
  "s_lshl_b32 s28, s27, 17\n"
  "s_and_b32 s29, s21, 3\n"
  "s_lshl_b32 s29, s29, 10\n"
  "s_sub_u32 s54, 0x20000, s29\n"
  "s_add_u32 s52, s28, s29\n"
  "s_mov_b32 s55, 0x20000\n"
  "s_min_u32 s28, s21, 6\n"
  "s_lshl2_add_u32 s28, s28, s24\n"
  "s_mul_i32 s40, s28, 14336\n"
  "s_mov_b32 s42, 14336\n"
  "s_mov_b32 s43, 0x20000\n"
  "s_mul_i32 s44, s21, 448\n"
  "s_sub_u32 s46, 1605632, s44\n"
  "s_mov_b32 s47, 0x20000\n"
  "s_mul_i32 s48, s21, 200704\n"
  "s_mov_b32 s50, 200704\n"
  "s_waitcnt lgkmcnt(0)\n"
  "s_add_u32 s36, s80, s64\n"
  "s_addc_u32 s37, s81, 0\n"
  "s_add_u32 m0, s34, 0\n"
  "s_mov_b32 s30, 0\n"
  "buffer_load_dwordx4 v2, s[36:39], s30 offen offset:0 lds\n"
  "s_load_dword s25, s[96:97], 0x0\n"
  "s_cmp_eq_u32 s22, 1\n"
  "s_cselect_b32 s32, s88, s86\n"
  "s_cselect_b32 s41, s89, s87\n"
  "s_add_u32 s40, s40, s32\n"
  "s_addc_u32 s41, s41, 0\n"
  "s_mov_b32 s30, 13312\n"
  "buffer_load_dwordx4 v2, s[36:39], s30 offen offset:1024 lds\n"
  "s_mov_b32 s51, 0x20000\n"
  "s_mov_b32 s62, 0x2000000\n"
  "s_mov_b32 s63, 0x20000\n"
  "s_mov_b32 s66, 28672\n"
  "s_mov_b32 s67, 0x20000\n"
  "s_lshr_b32 s28, s21, 2\n"
  "s_lshl1_add_u32 s28, s28, s22\n"
  "s_mov_b32 s30, 1536\n"
  "buffer_load_dwordx4 v2, s[36:39], s30 offen offset:2048 lds\n"
  "s_mul_i32 s28, s28, 917504\n"
  "s_and_b32 s29, s21, 3\n"
  "s_lshl2_add_u32 s29, s29, s24\n"
  "s_xor_b32 s29, s29, s22\n"
  "s_lshl_b32 s29, s29, 12\n"
  "s_add_u32 s28, s28, s29\n"
  "s_add_u32 s56, s28, 1048576\n"
  "s_mov_b32 s30, 14848\n"
  "buffer_load_dwordx4 v2, s[36:39], s30 offen offset:3072 lds\n"
  "s_mov_b32 s58, 917504\n"
  "s_mov_b32 s59, 0x20000\n"
  "s_add_u32 s64, s94, s64\n"
  "s_addc_u32 s65, s95, 0\n"
  "s_mov_b32 s60, s96\n"
  "s_mov_b32 s61, s97\n"
  "s_add_u32 s56, s96, s56\n"
  "s_addc_u32 s57, s97, 0\n"
  "s_cmp_eq_u32 s21, 7\n"
  "s_cbranch_scc1 Lw0%=\n"
  "s_add_u32 m0, s69, 0\n"
  "s_mov_b32 s30, 0\n"
  "buffer_load_dwordx4 v4, s[40:43], s30 offen offset:0 lds\n"
  "s_mov_b32 s30, 6144\n"
  "buffer_load_dwordx4 v4, s[40:43], s30 offen offset:1024 lds\n"
  "Lw0%=:\n"
  "s_cmp_ge_u32 s21, 4\n"
  "s_cselect_b32 s28, s84, s82\n"
  "s_cselect_b32 s53, s85, s83\n"
  "s_add_u32 s52, s52, s28\n"
  "s_addc_u32 s53, s53, 0\n"
  "s_add_u32 s44, s90, s44\n"
  "s_addc_u32 s45, s91, 0\n"
  "s_add_u32 s48, s92, s48\n"
  "s_addc_u32 s49, s93, 0\n"
  "s_lshl_b32 s28, s24, 11\n"
  "v_add_u32 v169, s28, v11\n"
  "v_add_u32 v170, s28, v12\n"
  "s_mul_i32 s28, s22, 7\n"
  "s_add_u32 s28, s28, s21\n"
  "s_add_u32 m0, s34, 4096\n"
  "s_mov_b32 s30, 128\n"
  "buffer_load_dwordx4 v2, s[36:39], s30 offen offset:0 lds\n"
  "s_lshl_b32 s28, s28, 18\n"
  "s_lshl_b32 s29, s27, 10\n"
  "s_add_u32 s28, s28, s29\n"
  "s_add_u32 s71, s28, 1048576\n"
  "s_lshl4_add_u32 s28, s22, s23\n"
  "s_lshl3_add_u32 s28, s28, s21\n"
  "s_lshl2_add_u32 s28, s28, s24\n"
  "s_mov_b32 s30, 13440\n"
  "buffer_load_dwordx4 v2, s[36:39], s30 offen offset:1024 lds\n"
  "s_lshl2_add_u32 s72, s28, 8192\n"
  "s_lshr_b32 s28, s20, 3\n"
  "s_mul_i32 s29, s28, 57344\n"
  "s_mul_i32 s73, s24, 2048\n"
  "s_add_u32 s73, s73, s29\n"
  "s_add_u32 s73, s73, 5242880\n"
  "s_lshl_b32 s28, s75, 7\n"
  "s_mov_b32 s30, 1664\n"
  "buffer_load_dwordx4 v2, s[36:39], s30 offen offset:2048 lds\n"
  "s_add_u32 s33, s28, 65536\n"
  "s_lshl_b32 s28, s21, 2\n"
  "s_lshr_b32 s28, 0x6543210, s28\n"
  "s_and_b32 s79, s28, 7\n"
  "s_add_u32 s98, s79, 1\n"
  "s_cmp_ge_u32 s98, 7\n"
  "s_cselect_b32 s28, 7, 0\n"
  "s_mov_b32 s30, 14976\n"
  "buffer_load_dwordx4 v2, s[36:39], s30 offen offset:3072 lds\n"
  "s_sub_u32 s98, s98, s28\n"
  "s_and_b32 s28, s24, 1\n"
  "s_lshl_b32 s28, s28, 1\n"
  "s_lshl_b32 s29, s98, 2\n"
  "s_movk_i32 s99, 3584\n"
  "s_lshl_b32 s31, s24, 2\n"
  "s_lshl_b32 s27, s24, 3\n"
  "s_cmp_eq_u32 s21, 7\n"
  "s_cbranch_scc1 Lw1%=\n"
  "s_add_u32 m0, s69, 8192\n"
  "s_mov_b32 s30, 128\n"
  "buffer_load_dwordx4 v4, s[40:43], s30 offen offset:0 lds\n"
  "s_mov_b32 s30, 6272\n"
  "buffer_load_dwordx4 v4, s[40:43], s30 offen offset:1024 lds\n"
  "Lw1%=:\n"
  "s_mul_i32 s70, s22, 448\n"
  "v_add_u32 v24, 0, v1\n"
  "v_mul_u32_u24 v25, 0x925, v24\n"
  "v_lshrrev_b32 v25, 16, v25\n"
  "v_mul_u32_u24 v26, 28, v25\n"
  "v_sub_u32 v24, v24, v26\n"
  "v_xor_b32 v24, s28, v24\n"
  "v_add_u32 v26, s27, v25\n"
  "v_lshlrev_b32 v27, 4, v24\n"
  "v_mad_u32_u24 v26, v26, s99, v27\n"
  "v_subrev_u32 v5, 0, v26\n"
  "v_add_u32 v24, s29, v24\n"
  "v_subrev_u32 v26, 28, v24\n"
  "v_min_u32 v24, v24, v26\n"
  "s_add_u32 m0, s34, 8192\n"
  "s_mov_b32 s30, 256\n"
  "buffer_load_dwordx4 v2, s[36:39], s30 offen offset:0 lds\n"
  "v_lshl_add_u32 v24, v24, 4, s70\n"
  "v_lshrrev_b32 v26, 2, v25\n"
  "v_and_b32 v25, 3, v25\n"
  "v_lshl_add_u32 v25, v26, 4, v25\n"
  "v_add_u32 v25, s31, v25\n"
  "v_mad_u32_u24 v25, v25, s26, v24\n"
  "v_subrev_u32 v25, 0, v25\n"
  "s_mov_b32 s30, 13568\n"
  "buffer_load_dwordx4 v2, s[36:39], s30 offen offset:1024 lds\n"
  "v_accvgpr_write_b32 a176, v25\n"
  "v_add_u32 v24, 64, v1\n"
  "v_mul_u32_u24 v25, 0x925, v24\n"
  "v_lshrrev_b32 v25, 16, v25\n"
  "v_mul_u32_u24 v26, 28, v25\n"
  "v_sub_u32 v24, v24, v26\n"
  "v_xor_b32 v24, s28, v24\n"
  "s_mov_b32 s30, 1792\n"
  "buffer_load_dwordx4 v2, s[36:39], s30 offen offset:2048 lds\n"
  "v_add_u32 v26, s27, v25\n"
  "v_lshlrev_b32 v27, 4, v24\n"
  "v_mad_u32_u24 v26, v26, s99, v27\n"
  "v_subrev_u32 v6, 1024, v26\n"
  "v_add_u32 v24, s29, v24\n"
  "v_subrev_u32 v26, 28, v24\n"
  "v_min_u32 v24, v24, v26\n"
  "s_mov_b32 s30, 15104\n"
  "buffer_load_dwordx4 v2, s[36:39], s30 offen offset:3072 lds\n"
  "v_lshl_add_u32 v24, v24, 4, s70\n"
  "v_lshrrev_b32 v26, 2, v25\n"
  "v_and_b32 v25, 3, v25\n"
  "v_lshl_add_u32 v25, v26, 4, v25\n"
  "v_add_u32 v25, s31, v25\n"
  "v_mad_u32_u24 v25, v25, s26, v24\n"
  "v_subrev_u32 v25, 1024, v25\n"
  "s_cmp_eq_u32 s21, 7\n"
  "s_cbranch_scc1 Lw2%=\n"
  "s_add_u32 m0, s69, 16384\n"
  "s_mov_b32 s30, 256\n"
  "buffer_load_dwordx4 v4, s[40:43], s30 offen offset:0 lds\n"
  "s_mov_b32 s30, 6400\n"
  "buffer_load_dwordx4 v4, s[40:43], s30 offen offset:1024 lds\n"
  "Lw2%=:\n"
  "v_accvgpr_write_b32 a177, v25\n"
  "v_add_u32 v24, 128, v1\n"
  "v_mul_u32_u24 v25, 0x925, v24\n"
  "v_lshrrev_b32 v25, 16, v25\n"
  "v_mul_u32_u24 v26, 28, v25\n"
  "v_sub_u32 v24, v24, v26\n"
  "v_xor_b32 v24, s28, v24\n"
  "v_add_u32 v26, s27, v25\n"
  "v_lshlrev_b32 v27, 4, v24\n"
  "v_mad_u32_u24 v26, v26, s99, v27\n"
  "v_subrev_u32 v7, 2048, v26\n"
  "v_add_u32 v24, s29, v24\n"
  "v_subrev_u32 v26, 28, v24\n"
  "v_min_u32 v24, v24, v26\n"
  "s_cmp_eq_u32 s21, 7\n"
  "s_cbranch_scc1 Lnon%=\n"
  "s_mov_b64 exec, 0\n"
  "s_cmp_eq_u32 s24, 1\n"
  "s_cbranch_scc1 Lto1%=\n"
  "s_cmp_eq_u32 s24, 2\n"
  "s_cbranch_scc1 Lto2%=\n"
  "s_cmp_eq_u32 s24, 3\n"
  "s_cbranch_scc1 Lto3%=\n"
  "s_branch Lto0%=\n"
  "Ltor0%=:\n"
  "s_mov_b64 exec, -1\n"
  "s_waitcnt lgkmcnt(0)\n"
  "s_mul_i32 s25, s25, 0x9E3779B1\n"
  "s_add_u32 s25, s25, 0x7F4A7C15\n"
  "v_mov_b32 v20, s25\n"
  "s_waitcnt vmcnt(12)\n"
  "s_barrier\n"
  "ds_read_b128 v[32:35], v9 offset:0\n"
  "ds_read_b128 v[36:39], v9 offset:2048\n"
  "ds_read_b128 a[192:195], v169 offset:0\n"
  "ds_read_b128 a[196:199], v11 offset:0\n"
  "ds_read_b128 a[200:203], v11 offset:2048\n"
  "ds_read_b128 a[204:207], v11 offset:4096\n"
  "ds_read_b128 a[208:211], v11 offset:6144\n"
  "s_waitcnt lgkmcnt(6)\n"
  "v_mfma_f32_16x16x32_bf16 v[144:147], a[180:183], v[32:35], 0\n"
  "s_add_u32 m0, s34, 12288\n"
  "s_mov_b32 s30, 384\n"
  "buffer_load_dwordx4 v2, s[36:39], s30 offen offset:0 lds\n"
  "v_mfma_f32_16x16x32_bf16 v[152:155], v[32:35], v[32:35], 0\n"
  "ds_read_b128 v[40:43], v10 offset:0\n"
  "s_mov_b32 s30, 13696\n"
  "s_waitcnt lgkmcnt(6)\n"
  "v_mfma_f32_16x16x32_bf16 v[148:151], a[180:183], v[36:39], 0\n"
  "buffer_load_dwordx4 v2, s[36:39], s30 offen offset:1024 lds\n"
  "v_mfma_f32_16x16x32_bf16 v[156:159], v[36:39], v[36:39], 0\n"
  "ds_read_b128 v[44:47], v10 offset:2048\n"
  "s_mov_b32 s30, 1920\n"
  "s_waitcnt lgkmcnt(6)\n"
  "v_mfma_f32_16x16x32_bf16 v[160:163], a[180:183], a[192:195], 0\n"
  "buffer_load_dwordx4 v2, s[36:39], s30 offen offset:2048 lds\n"
  "s_waitcnt lgkmcnt(5)\n"
  "v_mfma_f32_16x16x32_bf16 a[128:131], v[32:35], a[196:199], 0\n"
  "ds_read_b128 a[212:215], v170 offset:0\n"
  "s_mov_b32 s30, 15232\n"
  "v_lshl_add_u32 v24, v24, 4, s70\n"
  "v_mfma_f32_16x16x32_bf16 a[132:135], v[36:39], a[196:199], 0\n"
  "buffer_load_dwordx4 v2, s[36:39], s30 offen offset:3072 lds\n"
  "s_waitcnt lgkmcnt(5)\n"
  "v_mfma_f32_16x16x32_bf16 a[136:139], v[32:35], a[200:203], 0\n"
  "ds_read_b128 a[216:219], v12 offset:0\n"
  "s_add_u32 m0, s69, 24576\n"
  "s_mov_b32 s30, 384\n"
  "v_lshrrev_b32 v26, 2, v25\n"
  "v_mfma_f32_16x16x32_bf16 a[140:143], v[36:39], a[200:203], 0\n"
  "buffer_load_dwordx4 v4, s[40:43], s30 offen offset:0 lds\n"
  "s_waitcnt lgkmcnt(4)\n"
  "v_mfma_f32_16x16x32_bf16 a[144:147], v[32:35], a[204:207], 0\n"
  "ds_read_b128 a[220:223], v12 offset:2048\n"
  "s_mov_b32 s30, 6528\n"
  "v_and_b32 v25, 3, v25\n"
  "v_mfma_f32_16x16x32_bf16 a[148:151], v[36:39], a[204:207], 0\n"
  "buffer_load_dwordx4 v4, s[40:43], s30 offen offset:1024 lds\n"
  "v_mfma_f32_16x16x32_bf16 a[152:155], v[32:35], a[208:211], 0\n"
  "ds_read_b128 a[224:227], v12 offset:4096\n"
  "v_mfma_f32_16x16x32_bf16 a[156:159], v[36:39], a[208:211], 0\n"
  "ds_read_b128 a[228:231], v12 offset:6144\n"
  "s_waitcnt lgkmcnt(5)\n"
  "v_mfma_f32_16x16x32_bf16 v[144:147], a[180:183], v[40:43], v[144:147]\n"
  "s_waitcnt vmcnt(12)\n"
  "s_barrier\n"
  "s_add_u32 m0, s68, 81920\n"
  "s_mov_b32 s30, 512\n"
  "v_mfma_f32_16x16x32_bf16 v[152:155], v[40:43], v[40:43], v[152:155]\n"
  "buffer_load_dwordx4 v2, s[36:39], s30 offen offset:0 lds\n"
  "v_mfma_f32_16x16x32_bf16 v[148:151], a[180:183], v[44:47], v[148:151]\n"
  "ds_read_b128 v[48:51], v9 offset:4096\n"
  "v_lshl_add_u32 v25, v26, 4, v25\n"
  "v_mfma_f32_16x16x32_bf16 v[156:159], v[44:47], v[44:47], v[156:159]\n"
  "ds_read_b128 v[52:55], v9 offset:6144\n"
  "s_waitcnt lgkmcnt(5)\n"
  "v_mfma_f32_16x16x32_bf16 v[160:163], a[180:183], a[212:215], v[160:163]\n"
  "s_mov_b32 s30, 13824\n"
  "v_add_u32 v25, s31, v25\n"
  "v_mfma_f32_16x16x32_bf16 a[128:131], v[40:43], a[216:219], a[128:131]\n"
  "buffer_load_dwordx4 v2, s[36:39], s30 offen offset:1024 lds\n"
  "v_mfma_f32_16x16x32_bf16 a[132:135], v[44:47], a[216:219], a[132:135]\n"
  "ds_read_b128 a[232:235], v169 offset:8192\n"
  "ds_read_b128 a[236:239], v11 offset:8192\n"
  "s_waitcnt lgkmcnt(5)\n"
  "v_mfma_f32_16x16x32_bf16 a[136:139], v[40:43], a[220:223], a[136:139]\n"
  "v_mad_u32_u24 v25, v25, s26, v24\n"
  "v_mfma_f32_16x16x32_bf16 a[140:143], v[44:47], a[220:223], a[140:143]\n"
  "ds_read_b128 a[240:243], v11 offset:10240\n"
  "s_mov_b32 s30, 2048\n"
  "v_mfma_f32_16x16x32_bf16 a[144:147], v[40:43], a[224:227], a[144:147]\n"
  "buffer_load_dwordx4 v2, s[36:39], s30 offen offset:2048 lds\n"
  "v_mfma_f32_16x16x32_bf16 a[148:151], v[44:47], a[224:227], a[148:151]\n"
  "ds_read_b128 a[244:247], v11 offset:12288\n"
  "s_waitcnt lgkmcnt(5)\n"
  "v_mfma_f32_16x16x32_bf16 a[152:155], v[40:43], a[228:231], a[152:155]\n"
  "v_subrev_u32 v25, 2048, v25\n"
  "v_accvgpr_write_b32 a178, v25\n"
  "v_mfma_f32_16x16x32_bf16 a[156:159], v[44:47], a[228:231], a[156:159]\n"
  "ds_read_b128 a[248:251], v11 offset:14336\n"
  "s_mov_b32 s30, 15360\n"
  "v_mfma_f32_16x16x32_bf16 v[144:147], a[180:183], v[48:51], v[144:147]\n"
  "buffer_load_dwordx4 v2, s[36:39], s30 offen offset:3072 lds\n"
  "v_mfma_f32_16x16x32_bf16 v[152:155], v[48:51], v[48:51], v[152:155]\n"
  "ds_read_b128 v[56:59], v10 offset:4096\n"
  "s_waitcnt lgkmcnt(4)\n"
  "v_mfma_f32_16x16x32_bf16 v[148:151], a[180:183], v[52:55], v[148:151]\n"
  "v_add_u32 v24, 192, v1\n"
  "v_mfma_f32_16x16x32_bf16 v[156:159], v[52:55], v[52:55], v[156:159]\n"
  "ds_read_b128 v[60:63], v10 offset:6144\n"
  "s_add_u32 m0, s69, 32768\n"
  "s_mov_b32 s30, 512\n"
  "v_mfma_f32_16x16x32_bf16 v[160:163], a[180:183], a[232:235], v[160:163]\n"
  "buffer_load_dwordx4 v4, s[40:43], s30 offen offset:0 lds\n"
  "v_mfma_f32_16x16x32_bf16 a[128:131], v[48:51], a[236:239], a[128:131]\n"
  "ds_read_b128 a[252:255], v170 offset:8192\n"
  "v_mul_u32_u24 v25, 0x925, v24\n"
  "v_mfma_f32_16x16x32_bf16 a[132:135], v[52:55], a[236:239], a[132:135]\n"
  "ds_read_b128 a[192:195], v12 offset:8192\n"
  "s_waitcnt lgkmcnt(5)\n"
  "v_mfma_f32_16x16x32_bf16 a[136:139], v[48:51], a[240:243], a[136:139]\n"
  "s_mov_b32 s30, 6656\n"
  "v_lshrrev_b32 v25, 16, v25\n"
  "v_mfma_f32_16x16x32_bf16 a[140:143], v[52:55], a[240:243], a[140:143]\n"
  "buffer_load_dwordx4 v4, s[40:43], s30 offen offset:1024 lds\n"
  "v_mfma_f32_16x16x32_bf16 a[144:147], v[48:51], a[244:247], a[144:147]\n"
  "ds_read_b128 a[196:199], v12 offset:10240\n"
  "v_mul_u32_u24 v26, 28, v25\n"
  "v_mfma_f32_16x16x32_bf16 a[148:151], v[52:55], a[244:247], a[148:151]\n"
  "ds_read_b128 a[200:203], v12 offset:12288\n"
  "s_waitcnt lgkmcnt(5)\n"
  "v_mfma_f32_16x16x32_bf16 a[152:155], v[48:51], a[248:251], a[152:155]\n"
  "v_sub_u32 v24, v24, v26\n"
  "v_mfma_f32_16x16x32_bf16 a[156:159], v[52:55], a[248:251], a[156:159]\n"
  "ds_read_b128 a[204:207], v12 offset:14336\n"
  "v_mfma_f32_16x16x32_bf16 v[144:147], a[180:183], v[56:59], v[144:147]\n"
  "s_waitcnt vmcnt(12)\n"
  "s_barrier\n"
  "s_add_u32 m0, s34, 0\n"
  "s_mov_b32 s30, 640\n"
  "v_mfma_f32_16x16x32_bf16 v[152:155], v[56:59], v[56:59], v[152:155]\n"
  "ds_read_b128 v[64:67], v9 offset:8192\n"
  "buffer_load_dwordx4 v2, s[36:39], s30 offen offset:0 lds\n"
  "s_waitcnt lgkmcnt(4)\n"
  "v_mfma_f32_16x16x32_bf16 v[148:151], a[180:183], v[60:63], v[148:151]\n"
  "v_xor_b32 v24, s28, v24\n"
  "v_add_u32 v26, s27, v25\n"
  "v_mfma_f32_16x16x32_bf16 v[156:159], v[60:63], v[60:63], v[156:159]\n"
  "ds_read_b128 v[68:71], v9 offset:10240\n"
  "v_mfma_f32_16x16x32_bf16 v[160:163], a[180:183], a[252:255], v[160:163]\n"
  "s_mov_b32 s30, 13952\n"
  "v_mfma_f32_16x16x32_bf16 a[128:131], v[56:59], a[192:195], a[128:131]\n"
  "buffer_load_dwordx4 v2, s[36:39], s30 offen offset:1024 lds\n"
  "v_mfma_f32_16x16x32_bf16 a[132:135], v[60:63], a[192:195], a[132:135]\n"
  "ds_read_b128 a[208:211], v169 offset:16384\n"
  "ds_read_b128 a[212:215], v11 offset:16384\n"
  "s_waitcnt lgkmcnt(5)\n"
  "v_mfma_f32_16x16x32_bf16 a[136:139], v[56:59], a[196:199], a[136:139]\n"
  "v_lshlrev_b32 v27, 4, v24\n"
  "v_mad_u32_u24 v26, v26, s99, v27\n"
  "v_mfma_f32_16x16x32_bf16 a[140:143], v[60:63], a[196:199], a[140:143]\n"
  "ds_read_b128 a[216:219], v11 offset:18432\n"
  "s_mov_b32 s30, 2176\n"
  "v_mfma_f32_16x16x32_bf16 a[144:147], v[56:59], a[200:203], a[144:147]\n"
  "buffer_load_dwordx4 v2, s[36:39], s30 offen offset:2048 lds\n"
  "v_mfma_f32_16x16x32_bf16 a[148:151], v[60:63], a[200:203], a[148:151]\n"
  "ds_read_b128 a[220:223], v11 offset:20480\n"
  "s_waitcnt lgkmcnt(5)\n"
  "v_mfma_f32_16x16x32_bf16 a[152:155], v[56:59], a[204:207], a[152:155]\n"
  "v_subrev_u32 v8, 3072, v26\n"
  "v_mfma_f32_16x16x32_bf16 a[156:159], v[60:63], a[204:207], a[156:159]\n"
  "ds_read_b128 a[224:227], v11 offset:22528\n"
  "s_mov_b32 s30, 15488\n"
  "v_mfma_f32_16x16x32_bf16 v[144:147], a[180:183], v[64:67], v[144:147]\n"
  "buffer_load_dwordx4 v2, s[36:39], s30 offen offset:3072 lds\n"
  "v_mfma_f32_16x16x32_bf16 v[152:155], v[64:67], v[64:67], v[152:155]\n"
  "ds_read_b128 v[72:75], v10 offset:8192\n"
  "s_waitcnt lgkmcnt(4)\n"
  "v_mfma_f32_16x16x32_bf16 v[148:151], a[180:183], v[68:71], v[148:151]\n"
  "v_add_u32 v24, s29, v24\n"
  "v_subrev_u32 v26, 28, v24\n"
  "v_mfma_f32_16x16x32_bf16 v[156:159], v[68:71], v[68:71], v[156:159]\n"
  "ds_read_b128 v[76:79], v10 offset:10240\n"
  "s_add_u32 m0, s69, 40960\n"
  "s_mov_b32 s30, 640\n"
  "v_mfma_f32_16x16x32_bf16 v[160:163], a[180:183], a[208:211], v[160:163]\n"
  "buffer_load_dwordx4 v4, s[40:43], s30 offen offset:0 lds\n"
  "v_mfma_f32_16x16x32_bf16 a[128:131], v[64:67], a[212:215], a[128:131]\n"
  "ds_read_b128 a[228:231], v170 offset:16384\n"
  "v_mfma_f32_16x16x32_bf16 a[132:135], v[68:71], a[212:215], a[132:135]\n"
  "ds_read_b128 a[232:235], v12 offset:16384\n"
  "s_waitcnt lgkmcnt(5)\n"
  "v_mfma_f32_16x16x32_bf16 a[136:139], v[64:67], a[216:219], a[136:139]\n"
  "s_mov_b32 s30, 6784\n"
  "v_min_u32 v24, v24, v26\n"
  "v_lshl_add_u32 v24, v24, 4, s70\n"
  "v_mfma_f32_16x16x32_bf16 a[140:143], v[68:71], a[216:219], a[140:143]\n"
  "buffer_load_dwordx4 v4, s[40:43], s30 offen offset:1024 lds\n"
  "v_mfma_f32_16x16x32_bf16 a[144:147], v[64:67], a[220:223], a[144:147]\n"
  "ds_read_b128 a[236:239], v12 offset:18432\n"
  "v_mfma_f32_16x16x32_bf16 a[148:151], v[68:71], a[220:223], a[148:151]\n"
  "ds_read_b128 a[240:243], v12 offset:20480\n"
  "s_waitcnt lgkmcnt(5)\n"
  "v_mfma_f32_16x16x32_bf16 a[152:155], v[64:67], a[224:227], a[152:155]\n"
  "v_lshrrev_b32 v26, 2, v25\n"
  "v_and_b32 v25, 3, v25\n"
  "v_mfma_f32_16x16x32_bf16 a[156:159], v[68:71], a[224:227], a[156:159]\n"
  "ds_read_b128 a[244:247], v12 offset:22528\n"
  "v_mfma_f32_16x16x32_bf16 v[144:147], a[180:183], v[72:75], v[144:147]\n"
  "s_waitcnt vmcnt(12)\n"
  "s_barrier\n"
  "s_add_u32 m0, s34, 4096\n"
  "s_mov_b32 s30, 768\n"
  "v_mfma_f32_16x16x32_bf16 v[152:155], v[72:75], v[72:75], v[152:155]\n"
  "ds_read_b128 v[80:83], v9 offset:12288\n"
  "buffer_load_dwordx4 v2, s[36:39], s30 offen offset:0 lds\n"
  "s_waitcnt lgkmcnt(4)\n"
  "v_mfma_f32_16x16x32_bf16 v[148:151], a[180:183], v[76:79], v[148:151]\n"
  "v_lshl_add_u32 v25, v26, 4, v25\n"
  "v_mfma_f32_16x16x32_bf16 v[156:159], v[76:79], v[76:79], v[156:159]\n"
  "ds_read_b128 v[84:87], v9 offset:14336\n"
  "v_mfma_f32_16x16x32_bf16 v[160:163], a[180:183], a[228:231], v[160:163]\n"
  "s_mov_b32 s30, 14080\n"
  "v_add_u32 v25, s31, v25\n"
  "v_mfma_f32_16x16x32_bf16 a[128:131], v[72:75], a[232:235], a[128:131]\n"
  "buffer_load_dwordx4 v2, s[36:39], s30 offen offset:1024 lds\n"
  "v_mfma_f32_16x16x32_bf16 a[132:135], v[76:79], a[232:235], a[132:135]\n"
  "ds_read_b128 a[248:251], v169 offset:24576\n"
  "ds_read_b128 a[252:255], v11 offset:24576\n"
  "s_waitcnt lgkmcnt(5)\n"
  "v_mfma_f32_16x16x32_bf16 a[136:139], v[72:75], a[236:239], a[136:139]\n"
  "v_mad_u32_u24 v25, v25, s26, v24\n"
  "v_mfma_f32_16x16x32_bf16 a[140:143], v[76:79], a[236:239], a[140:143]\n"
  "ds_read_b128 a[192:195], v11 offset:26624\n"
  "s_mov_b32 s30, 2304\n"
  "v_mfma_f32_16x16x32_bf16 a[144:147], v[72:75], a[240:243], a[144:147]\n"
  "buffer_load_dwordx4 v2, s[36:39], s30 offen offset:2048 lds\n"
  "v_mfma_f32_16x16x32_bf16 a[148:151], v[76:79], a[240:243], a[148:151]\n"
  "ds_read_b128 a[196:199], v11 offset:28672\n"
  "s_waitcnt lgkmcnt(5)\n"
  "v_mfma_f32_16x16x32_bf16 a[152:155], v[72:75], a[244:247], a[152:155]\n"
  "v_subrev_u32 v25, 3072, v25\n"
  "v_accvgpr_write_b32 a179, v25\n"
  "v_mfma_f32_16x16x32_bf16 a[156:159], v[76:79], a[244:247], a[156:159]\n"
  "ds_read_b128 a[200:203], v11 offset:30720\n"
  "s_mov_b32 s30, 15616\n"
  "v_mfma_f32_16x16x32_bf16 v[144:147], a[180:183], v[80:83], v[144:147]\n"
  "buffer_load_dwordx4 v2, s[36:39], s30 offen offset:3072 lds\n"
  "v_mfma_f32_16x16x32_bf16 v[152:155], v[80:83], v[80:83], v[152:155]\n"
  "ds_read_b128 v[88:91], v10 offset:12288\n"
  "s_waitcnt lgkmcnt(4)\n"
  "v_mfma_f32_16x16x32_bf16 v[148:151], a[180:183], v[84:87], v[148:151]\n"
  "v_lshrrev_b32 v25, 2, v28\n"
  "v_mfma_f32_16x16x32_bf16 v[156:159], v[84:87], v[84:87], v[156:159]\n"
  "ds_read_b128 v[92:95], v10 offset:14336\n"
  "s_add_u32 m0, s69, 49152\n"
  "s_mov_b32 s30, 768\n"
  "v_mfma_f32_16x16x32_bf16 v[160:163], a[180:183], a[248:251], v[160:163]\n"
  "buffer_load_dwordx4 v4, s[40:43], s30 offen offset:0 lds\n"
  "v_mfma_f32_16x16x32_bf16 a[128:131], v[80:83], a[252:255], a[128:131]\n"
  "ds_read_b128 a[204:207], v170 offset:24576\n"
  "v_lshlrev_b32 v24, 7, v28\n"
  "v_mfma_f32_16x16x32_bf16 a[132:135], v[84:87], a[252:255], a[132:135]\n"
  "ds_read_b128 a[208:211], v12 offset:24576\n"
  "s_waitcnt lgkmcnt(5)\n"
  "v_mfma_f32_16x16x32_bf16 a[136:139], v[80:83], a[192:195], a[136:139]\n"
  "s_mov_b32 s30, 6912\n"
  "v_and_b32 v24, 0x180, v24\n"
  "v_mfma_f32_16x16x32_bf16 a[140:143], v[84:87], a[192:195], a[140:143]\n"
  "buffer_load_dwordx4 v4, s[40:43], s30 offen offset:1024 lds\n"
  "v_mfma_f32_16x16x32_bf16 a[144:147], v[80:83], a[196:199], a[144:147]\n"
  "ds_read_b128 a[212:215], v12 offset:26624\n"
  "v_mad_u32_u24 v27, v25, s99, v24\n"
  "v_mfma_f32_16x16x32_bf16 a[148:151], v[84:87], a[196:199], a[148:151]\n"
  "ds_read_b128 a[216:219], v12 offset:28672\n"
  "s_waitcnt lgkmcnt(5)\n"
  "v_mfma_f32_16x16x32_bf16 a[152:155], v[80:83], a[200:203], a[152:155]\n"
  "v_lshlrev_b32 v23, 7, v28\n"
  "v_mfma_f32_16x16x32_bf16 a[156:159], v[84:87], a[200:203], a[156:159]\n"
  "ds_read_b128 a[220:223], v12 offset:30720\n"
  "v_mfma_f32_16x16x32_bf16 v[144:147], a[180:183], v[88:91], v[144:147]\n"
  "s_waitcnt vmcnt(12)\n"
  "s_barrier\n"
  "s_add_u32 m0, s35, 65536\n"
  "s_mov_b32 s31, 0\n"
  "v_mfma_f32_16x16x32_bf16 v[152:155], v[88:91], v[88:91], v[152:155]\n"
  "buffer_load_dwordx4 v5, s[44:47], s31 offen offset:0 lds\n"
  "s_waitcnt lgkmcnt(3)\n"
  "v_mfma_f32_16x16x32_bf16 v[148:151], a[180:183], v[92:95], v[148:151]\n"
  "ds_read_b128 v[96:99], v30 offset:0\n"
  "s_lshl_b32 s70, s22, 31\n"
  "v_mfma_f32_16x16x32_bf16 v[156:159], v[92:95], v[92:95], v[156:159]\n"
  "ds_read_b128 v[100:103], v30 offset:2048\n"
  "v_mfma_f32_16x16x32_bf16 v[160:163], a[180:183], a[204:207], v[160:163]\n"
  "s_mov_b32 s80, s98\n"
  "v_mfma_f32_16x16x32_bf16 a[128:131], v[88:91], a[208:211], a[128:131]\n"
  "ds_read_b128 a[224:227], v169 offset:32768\n"
  "s_add_u32 s81, s80, 1\n"
  "v_mfma_f32_16x16x32_bf16 a[132:135], v[92:95], a[208:211], a[132:135]\n"
  "ds_read_b128 a[228:231], v11 offset:32768\n"
  "s_waitcnt lgkmcnt(5)\n"
  "v_mfma_f32_16x16x32_bf16 a[136:139], v[88:91], a[212:215], a[136:139]\n"
  "buffer_load_dwordx4 v6, s[44:47], s31 offen offset:1024 lds\n"
  "v_mfma_f32_16x16x32_bf16 a[140:143], v[92:95], a[212:215], a[140:143]\n"
  "ds_read_b128 a[232:235], v11 offset:34816\n"
  "v_mfma_f32_16x16x32_bf16 a[144:147], v[88:91], a[216:219], a[144:147]\n"
  "s_cmp_eq_u32 s81, 7\n"
  "s_cselect_b32 s81, 0, s81\n"
  "s_add_u32 s82, s81, 1\n"
  "v_mfma_f32_16x16x32_bf16 a[148:151], v[92:95], a[216:219], a[148:151]\n"
  "ds_read_b128 a[236:239], v11 offset:36864\n"
  "s_waitcnt lgkmcnt(5)\n"
  "v_mfma_f32_16x16x32_bf16 a[152:155], v[88:91], a[220:223], a[152:155]\n"
  "s_cmp_eq_u32 s82, 7\n"
  "s_cselect_b32 s82, 0, s82\n"
  "s_add_u32 s83, s82, 1\n"
  "v_mfma_f32_16x16x32_bf16 a[156:159], v[92:95], a[220:223], a[156:159]\n"
  "ds_read_b128 a[240:243], v11 offset:38912\n"
  "v_mfma_f32_16x16x32_bf16 v[144:147], a[180:183], v[96:99], v[144:147]\n"
  "buffer_load_dwordx4 v7, s[44:47], s31 offen offset:2048 lds\n"
  "v_mfma_f32_16x16x32_bf16 v[152:155], v[96:99], v[96:99], v[152:155]\n"
  "ds_read_b128 v[104:107], v31 offset:0\n"
  "s_waitcnt lgkmcnt(4)\n"
  "v_mfma_f32_16x16x32_bf16 v[148:151], a[180:183], v[100:103], v[148:151]\n"
  "s_cmp_eq_u32 s83, 7\n"
  "s_cselect_b32 s83, 0, s83\n"
  "s_add_u32 s84, s83, 1\n"
  "v_mfma_f32_16x16x32_bf16 v[156:159], v[100:103], v[100:103], v[156:159]\n"
  "ds_read_b128 v[108:111], v31 offset:2048\n"
  "v_mfma_f32_16x16x32_bf16 v[160:163], a[180:183], a[224:227], v[160:163]\n"
  "s_cmp_eq_u32 s84, 7\n"
  "s_cselect_b32 s84, 0, s84\n"
  "v_mfma_f32_16x16x32_bf16 a[128:131], v[96:99], a[228:231], a[128:131]\n"
  "ds_read_b128 a[244:247], v170 offset:32768\n"
  "s_add_u32 s85, s84, 1\n"
  "v_mfma_f32_16x16x32_bf16 a[132:135], v[100:103], a[228:231], a[132:135]\n"
  "s_mov_b32 exec_hi, 0\n"
  "buffer_load_dwordx4 v8, s[44:47], s31 offen offset:3072 lds\n"
  "s_mov_b32 exec_hi, -1\n"
  "s_waitcnt lgkmcnt(4)\n"
  "v_mfma_f32_16x16x32_bf16 a[136:139], v[96:99], a[232:235], a[136:139]\n"
  "ds_read_b128 a[248:251], v12 offset:32768\n"
  "s_cmp_eq_u32 s85, 7\n"
  "s_cselect_b32 s85, 0, s85\n"
  "v_mfma_f32_16x16x32_bf16 a[140:143], v[100:103], a[232:235], a[140:143]\n"
  "ds_read_b128 a[252:255], v12 offset:34816\n"
  "v_mfma_f32_16x16x32_bf16 a[144:147], v[96:99], a[236:239], a[144:147]\n"
  "s_add_u32 s86, s85, 1\n"
  "s_cmp_eq_u32 s86, 7\n"
  "s_cselect_b32 s86, 0, s86\n"
  "v_mfma_f32_16x16x32_bf16 a[148:151], v[100:103], a[236:239], a[148:151]\n"
  "ds_read_b128 a[192:195], v12 offset:36864\n"
  "s_waitcnt lgkmcnt(5)\n"
  "v_mfma_f32_16x16x32_bf16 a[152:155], v[96:99], a[240:243], a[152:155]\n"
  "v_lshrrev_b32 v24, 4, v1\n"
  "v_bfe_u32 v25, v1, 2, 2\n"
  "v_mfma_f32_16x16x32_bf16 a[156:159], v[100:103], a[240:243], a[156:159]\n"
  "Lto0%=:\n"
  "s_cbranch_execz Ltor0%=\n"
  "ds_read_b128 a[196:199], v12 offset:38912\n"
  "v_mfma_f32_16x16x32_bf16 v[144:147], a[180:183], v[104:107], v[144:147]\n"
  "s_waitcnt vmcnt(10)\n"
  "s_barrier\n"
  "s_add_u32 m0, s35, 0\n"
  "s_mov_b32 s31, 229376\n"
  "v_mfma_f32_16x16x32_bf16 v[152:155], v[104:107], v[104:107], v[152:155]\n"
  "buffer_load_dwordx4 v5, s[44:47], s31 offen offset:0 lds\n"
  "s_waitcnt lgkmcnt(4)\n"
  "v_mfma_f32_16x16x32_bf16 v[148:151], a[180:183], v[108:111], v[148:151]\n"
  "ds_read_b128 v[112:115], v9 offset:0\n"
  "v_xor_b32 v24, v24, v25\n"
  "v_mfma_f32_16x16x32_bf16 v[156:159], v[108:111], v[108:111], v[156:159]\n"
  "ds_read_b128 v[116:119], v9 offset:2048\n"
  "v_mfma_f32_16x16x32_bf16 v[160:163], a[180:183], a[244:247], v[160:163]\n"
  "v_min_u32 v24, 1, v24\n"
  "s_waitcnt lgkmcnt(4)\n"
  "v_mfma_f32_16x16x32_bf16 a[128:131], v[104:107], a[248:251], a[128:131]\n"
  "ds_read_b128 a[200:203], v169 offset:40960\n"
  "v_add_u32 v24, -1, v24\n"
  "v_mfma_f32_16x16x32_bf16 a[132:135], v[108:111], a[248:251], a[132:135]\n"
  "ds_read_b128 a[204:207], v11 offset:40960\n"
  "v_mfma_f32_16x16x32_bf16 a[136:139], v[104:107], a[252:255], a[136:139]\n"
  "buffer_load_dwordx4 v6, s[44:47], s31 offen offset:1024 lds\n"
  "v_mfma_f32_16x16x32_bf16 a[140:143], v[108:111], a[252:255], a[140:143]\n"
  "ds_read_b128 a[208:211], v11 offset:43008\n"
  "s_waitcnt lgkmcnt(5)\n"
  "v_mfma_f32_16x16x32_bf16 a[144:147], v[104:107], a[192:195], a[144:147]\n"
  "v_and_b32 v24, 0x3f80, v24\n"
  "v_and_b32 v25, 1, v1\n"
  "v_mfma_f32_16x16x32_bf16 a[148:151], v[108:111], a[192:195], a[148:151]\n"
  "ds_read_b128 a[212:215], v11 offset:45056\n"
  "v_mfma_f32_16x16x32_bf16 a[152:155], v[104:107], a[196:199], a[152:155]\n"
  "v_lshlrev_b32 v25, 4, v25\n"
  "v_lshlrev_b32 v24, v25, v24\n"
  "v_mfma_f32_16x16x32_bf16 a[156:159], v[108:111], a[196:199], a[156:159]\n"
  "ds_read_b128 a[216:219], v11 offset:47104\n"
  "s_waitcnt lgkmcnt(5)\n"
  "v_mfma_f32_16x16x32_bf16 v[144:147], a[180:183], v[112:115], v[144:147]\n"
  "buffer_load_dwordx4 v7, s[44:47], s31 offen offset:2048 lds\n"
  "v_mfma_f32_16x16x32_bf16 v[152:155], v[112:115], v[112:115], v[152:155]\n"
  "ds_read_b128 v[120:123], v10 offset:0\n"
  "v_mfma_f32_16x16x32_bf16 v[148:151], a[180:183], v[116:119], v[148:151]\n"
  "v_bfe_i32 v25, v1, 1, 1\n"
  "v_and_b32 v25, v25, v24\n"
  "v_mfma_f32_16x16x32_bf16 v[156:159], v[116:119], v[116:119], v[156:159]\n"
  "ds_read_b128 v[124:127], v10 offset:2048\n"
  "s_waitcnt lgkmcnt(4)\n"
  "v_mfma_f32_16x16x32_bf16 v[160:163], a[180:183], a[200:203], v[160:163]\n"
  "v_xor_b32 v24, v25, v24\n"
  "v_mfma_f32_16x16x32_bf16 a[128:131], v[112:115], a[204:207], a[128:131]\n"
  "ds_read_b128 a[220:223], v170 offset:40960\n"
  "v_accvgpr_write_b32 a184, 0\n"
  "v_mfma_f32_16x16x32_bf16 a[132:135], v[116:119], a[204:207], a[132:135]\n"
  "s_mov_b32 exec_hi, 0\n"
  "buffer_load_dwordx4 v8, s[44:47], s31 offen offset:3072 lds\n"
  "s_mov_b32 exec_hi, -1\n"
  "v_mfma_f32_16x16x32_bf16 a[136:139], v[112:115], a[208:211], a[136:139]\n"
  "ds_read_b128 a[224:227], v12 offset:40960\n"
  "v_accvgpr_write_b32 a185, 0\n"
  "v_mfma_f32_16x16x32_bf16 a[140:143], v[116:119], a[208:211], a[140:143]\n"
  "ds_read_b128 a[228:231], v12 offset:43008\n"
  "s_waitcnt lgkmcnt(5)\n"
  "v_mfma_f32_16x16x32_bf16 a[144:147], v[112:115], a[212:215], a[144:147]\n"
  "v_accvgpr_write_b32 a186, v24\n"
  "v_accvgpr_write_b32 a187, v25\n"
  "v_mfma_f32_16x16x32_bf16 a[148:151], v[116:119], a[212:215], a[148:151]\n"
  "ds_read_b128 a[232:235], v12 offset:45056\n"
  "v_mfma_f32_16x16x32_bf16 a[152:155], v[112:115], a[216:219], a[152:155]\n"
  "v_accvgpr_write_b32 a188, 0\n"
  "v_accvgpr_write_b32 a189, 0\n"
  "v_mfma_f32_16x16x32_bf16 a[156:159], v[116:119], a[216:219], a[156:159]\n"
  "ds_read_b128 a[236:239], v12 offset:47104\n"
  "s_waitcnt lgkmcnt(5)\n"
  "v_mfma_f32_16x16x32_bf16 v[144:147], a[180:183], v[120:123], v[144:147]\n"
  "s_waitcnt vmcnt(8)\n"
  "s_barrier\n"
  "s_add_u32 m0, s35, 81920\n"
  "s_mov_b32 s31, 114688\n"
  "v_mfma_f32_16x16x32_bf16 v[152:155], v[120:123], v[120:123], v[152:155]\n"
  "buffer_load_dwordx4 v5, s[44:47], s31 offen offset:0 lds\n"
  "v_mfma_f32_16x16x32_bf16 v[148:151], a[180:183], v[124:127], v[148:151]\n"
  "ds_read_b128 v[128:131], v9 offset:4096\n"
  "s_add_u32 s28, s69, 57344\n"
  "v_mfma_f32_16x16x32_bf16 v[156:159], v[124:127], v[124:127], v[156:159]\n"
  "ds_read_b128 v[132:135], v9 offset:6144\n"
  "s_waitcnt lgkmcnt(4)\n"
  "v_mfma_f32_16x16x32_bf16 v[160:163], a[180:183], a[220:223], v[160:163]\n"
  "v_add_u32 v164, s28, v17\n"
  "v_mfma_f32_16x16x32_bf16 a[128:131], v[120:123], a[224:227], a[128:131]\n"
  "ds_read_b128 a[240:243], v169 offset:49152\n"
  "v_and_b32 v24, 15, v1\n"
  "v_mfma_f32_16x16x32_bf16 a[132:135], v[124:127], a[224:227], a[132:135]\n"
  "ds_read_b128 a[244:247], v11 offset:49152\n"
  "v_mfma_f32_16x16x32_bf16 a[136:139], v[120:123], a[228:231], a[136:139]\n"
  "buffer_load_dwordx4 v6, s[44:47], s31 offen offset:1024 lds\n"
  "v_mfma_f32_16x16x32_bf16 a[140:143], v[124:127], a[228:231], a[140:143]\n"
  "ds_read_b128 a[248:251], v11 offset:51200\n"
  "s_waitcnt lgkmcnt(5)\n"
  "v_mfma_f32_16x16x32_bf16 a[144:147], v[120:123], a[232:235], a[144:147]\n"
  "v_and_b32 v25, 12, v1\n"
  "v_lshl_add_u32 v25, v25, 2, v24\n"
  "v_mfma_f32_16x16x32_bf16 a[148:151], v[124:127], a[232:235], a[148:151]\n"
  "ds_read_b128 a[252:255], v11 offset:53248\n"
  "v_mfma_f32_16x16x32_bf16 a[152:155], v[120:123], a[236:239], a[152:155]\n"
  "v_and_b32 v26, 3, v1\n"
  "v_lshl_add_u32 v25, v25, 2, v26\n"
  "v_mfma_f32_16x16x32_bf16 a[156:159], v[124:127], a[236:239], a[156:159]\n"
  "ds_read_b128 a[192:195], v11 offset:55296\n"
  "s_waitcnt lgkmcnt(5)\n"
  "v_mfma_f32_16x16x32_bf16 v[144:147], a[180:183], v[128:131], v[144:147]\n"
  "buffer_load_dwordx4 v7, s[44:47], s31 offen offset:2048 lds\n"
  "v_mfma_f32_16x16x32_bf16 v[152:155], v[128:131], v[128:131], v[152:155]\n"
  "ds_read_b128 v[136:139], v10 offset:4096\n"
  "v_mfma_f32_16x16x32_bf16 v[148:151], a[180:183], v[132:135], v[148:151]\n"
  "v_lshl_add_u32 v165, v25, 2, s28\n"
  "s_lshl_b32 s28, s24, 9\n"
  "v_mfma_f32_16x16x32_bf16 v[156:159], v[132:135], v[132:135], v[156:159]\n"
  "ds_read_b128 v[140:143], v10 offset:6144\n"
  "s_waitcnt lgkmcnt(4)\n"
  "v_mfma_f32_16x16x32_bf16 v[160:163], a[180:183], a[240:243], v[160:163]\n"
  "s_add_u32 s28, s28, 145408\n"
  "v_mfma_f32_16x16x32_bf16 a[128:131], v[128:131], a[244:247], a[128:131]\n"
  "ds_read_b128 a[196:199], v170 offset:49152\n"
  "v_lshl_add_u32 v22, v24, 2, s28\n"
  "v_mfma_f32_16x16x32_bf16 a[132:135], v[132:135], a[244:247], a[132:135]\n"
  "s_mov_b32 exec_hi, 0\n"
  "buffer_load_dwordx4 v8, s[44:47], s31 offen offset:3072 lds\n"
  "s_mov_b32 exec_hi, -1\n"
  "v_mfma_f32_16x16x32_bf16 a[136:139], v[128:131], a[248:251], a[136:139]\n"
  "ds_read_b128 a[200:203], v12 offset:49152\n"
  "v_and_b32 v25, 48, v1\n"
  "v_mfma_f32_16x16x32_bf16 a[140:143], v[132:135], a[248:251], a[140:143]\n"
  "ds_read_b128 a[204:207], v12 offset:51200\n"
  "s_waitcnt lgkmcnt(5)\n"
  "v_mfma_f32_16x16x32_bf16 a[144:147], v[128:131], a[252:255], a[144:147]\n"
  "v_add_u32 v167, s28, v25\n"
  "v_lshlrev_b32 v24, 2, v24\n"
  "v_mfma_f32_16x16x32_bf16 a[148:151], v[132:135], a[252:255], a[148:151]\n"
  "ds_read_b128 a[208:211], v12 offset:53248\n"
  "v_mfma_f32_16x16x32_bf16 a[152:155], v[128:131], a[192:195], a[152:155]\n"
  "v_add_u32 v168, 145408, v24\n"
  "v_accvgpr_write_b32 a160, v167\n"
  "v_mfma_f32_16x16x32_bf16 a[156:159], v[132:135], a[192:195], a[156:159]\n"
  "ds_read_b128 a[212:215], v12 offset:55296\n"
  "s_waitcnt lgkmcnt(5)\n"
  "v_mfma_f32_16x16x32_bf16 v[144:147], a[180:183], v[136:139], v[144:147]\n"
  "v_accvgpr_write_b32 a161, v168\n"
  "v_mfma_f32_16x16x32_bf16 v[152:155], v[136:139], v[136:139], v[152:155]\n"
  "v_mfma_f32_16x16x32_bf16 v[148:151], a[180:183], v[140:143], v[148:151]\n"
  "v_mfma_f32_16x16x32_bf16 v[156:159], v[140:143], v[140:143], v[156:159]\n"
  "s_waitcnt lgkmcnt(2)\n"
  "v_mfma_f32_16x16x32_bf16 v[160:163], a[180:183], a[196:199], v[160:163]\n"
  "v_mfma_f32_16x16x32_bf16 a[128:131], v[136:139], a[200:203], a[128:131]\n"
  "v_mfma_f32_16x16x32_bf16 a[132:135], v[140:143], a[200:203], a[132:135]\n"
  "v_mul_f32 v24, 0xbb124925, v144\n"
  "v_mul_f32 v25, 0xbb124925, v145\n"
  "v_mul_f32 v26, 0xbb124925, v146\n"
  "v_mfma_f32_16x16x32_bf16 a[136:139], v[136:139], a[204:207], a[136:139]\n"
  "v_mul_f32 v27, 0xbb124925, v147\n"
  "v_mul_f32 v28, 0xbb124925, v148\n"
  "v_mul_f32 v29, 0xbb124925, v149\n"
  "v_mul_f32 v30, 0xbb124925, v150\n"
  "v_mfma_f32_16x16x32_bf16 a[140:143], v[140:143], a[204:207], a[140:143]\n"
  "s_waitcnt vmcnt(8)\n"
  "s_barrier\n"
  "s_add_u32 m0, s35, 16384\n"
  "s_mov_b32 s31, 344064\n"
  "v_mul_f32 v31, 0xbb124925, v151\n"
  "s_waitcnt lgkmcnt(0)\n"
  "v_mfma_f32_16x16x32_bf16 a[144:147], v[136:139], a[208:211], a[144:147]\n"
  "buffer_load_dwordx4 v5, s[44:47], s31 offen offset:0 lds\n"
  "ds_write_b128 v164, v[152:155] offset:0\n"
  "v_mfma_f32_16x16x32_bf16 a[148:151], v[140:143], a[208:211], a[148:151]\n"
  "ds_read_b64_tr_b16 a[216:217], v15 offset:0\n"
  "ds_read_b64_tr_b16 a[218:219], v15 offset:1792\n"
  "ds_write_b128 v164, v[156:159] offset:1024\n"
  "v_mfma_f32_16x16x32_bf16 a[152:155], v[136:139], a[212:215], a[152:155]\n"
  "ds_read_b64_tr_b16 a[220:221], v16 offset:32\n"
  "ds_read_b32 v11, v165 offset:0\n"
  "ds_read_b32 v12, v165 offset:1024\n"
  "ds_write_b32 v22, v160 offset:256\n"
  "v_mfma_f32_16x16x32_bf16 a[156:159], v[140:143], a[212:215], a[156:159]\n"
  "buffer_load_dwordx4 v6, s[44:47], s31 offen offset:1024 lds\n"
  "v_mfma_f32_16x16x32_bf16 v[224:227], a[186:189], v[32:35], v[24:27]\n"
  "ds_read_b64_tr_b16 a[222:223], v16 offset:1824\n"
  "ds_read_b64_tr_b16 a[224:225], v15 offset:64\n"
  "v_mfma_f32_16x16x32_bf16 v[228:231], a[184:187], v[32:35], v[24:27]\n"
  "ds_read_b64_tr_b16 a[226:227], v15 offset:1856\n"
  "v_mfma_f32_16x16x32_bf16 v[232:235], a[186:189], v[36:39], v[28:31]\n"
  "buffer_load_dwordx4 v7, s[44:47], s31 offen offset:2048 lds\n"
  "v_mfma_f32_16x16x32_bf16 v[236:239], a[184:187], v[36:39], v[28:31]\n"
  "ds_read_b64_tr_b16 a[228:229], v16 offset:96\n"
  "ds_read_b64_tr_b16 a[230:231], v16 offset:1888\n"
  "v_mfma_f32_16x16x32_bf16 v[240:243], a[186:189], v[40:43], v[24:27]\n"
  "ds_read_b64_tr_b16 a[232:233], v15 offset:128\n"
  "v_cvt_pk_bf16_f32 v32, v224, v225\n"
  "v_cvt_pk_bf16_f32 v33, v226, v227\n"
  "v_mfma_f32_16x16x32_bf16 v[244:247], a[184:187], v[40:43], v[24:27]\n"
  "ds_read_b64_tr_b16 a[234:235], v15 offset:1920\n"
  "v_cvt_pk_bf16_f32 v34, v228, v229\n"
  "v_cvt_pk_bf16_f32 v35, v230, v231\n"
  "v_mfma_f32_16x16x32_bf16 v[248:251], a[186:189], v[44:47], v[28:31]\n"
  "s_mov_b32 exec_hi, 0\n"
  "buffer_load_dwordx4 v8, s[44:47], s31 offen offset:3072 lds\n"
  "s_mov_b32 exec_hi, -1\n"
  "v_cvt_pk_bf16_f32 v36, v232, v233\n"
  "v_cvt_pk_bf16_f32 v37, v234, v235\n"
  "v_mfma_f32_16x16x32_bf16 v[252:255], a[184:187], v[44:47], v[28:31]\n"
  "ds_read_b64_tr_b16 a[236:237], v16 offset:160\n"
  "ds_read_b64_tr_b16 a[238:239], v16 offset:1952\n"
  "v_cvt_pk_bf16_f32 v38, v236, v237\n"
  "v_cvt_pk_bf16_f32 v39, v238, v239\n"
  "s_waitcnt lgkmcnt(9)\n"
  "v_mfma_f32_16x16x32_bf16 v[144:147], a[216:219], v[32:35], 0\n"
  "ds_read_b64_tr_b16 a[240:241], v15 offset:192\n"
  "v_cvt_pk_bf16_f32 v40, v240, v241\n"
  "v_cvt_pk_bf16_f32 v41, v242, v243\n"
  "s_add_u32 m0, s35, 32768\n"
  "s_mov_b32 s31, 458752\n"
  "v_mfma_f32_16x16x32_bf16 v[148:151], a[216:219], v[36:39], 0\n"
  "buffer_load_dwordx4 v5, s[44:47], s31 offen offset:0 lds\n"
  "v_cvt_pk_bf16_f32 v42, v244, v245\n"
  "v_cvt_pk_bf16_f32 v43, v246, v247\n"
  "s_waitcnt lgkmcnt(7)\n"
  "v_mfma_f32_16x16x32_bf16 v[152:155], a[220:223], v[32:35], 0\n"
  "ds_read_b64_tr_b16 a[242:243], v15 offset:1984\n"
  "ds_read_b64_tr_b16 a[244:245], v16 offset:224\n"
  "v_cvt_pk_bf16_f32 v44, v248, v249\n"
  "v_cvt_pk_bf16_f32 v45, v250, v251\n"
  "v_mfma_f32_16x16x32_bf16 v[156:159], a[220:223], v[36:39], 0\n"
  "ds_read_b64_tr_b16 a[246:247], v16 offset:2016\n"
  "v_cvt_pk_bf16_f32 v46, v252, v253\n"
  "v_cvt_pk_bf16_f32 v47, v254, v255\n"
  "v_mfma_f32_16x16x32_bf16 v[160:163], a[224:227], v[32:35], 0\n"
  "buffer_load_dwordx4 v6, s[44:47], s31 offen offset:1024 lds\n"
  "v_mfma_f32_16x16x32_bf16 v[164:167], a[224:227], v[36:39], 0\n"
  "ds_read_b64_tr_b16 a[248:249], v15 offset:256\n"
  "ds_read_b64_tr_b16 a[250:251], v15 offset:2048\n"
  "s_waitcnt lgkmcnt(8)\n"
  "v_mfma_f32_16x16x32_bf16 v[168:171], a[228:231], v[32:35], 0\n"
  "ds_read_b64_tr_b16 a[252:253], v16 offset:288\n"
  "v_mul_f32 v2, v24, v24\n"
  "v_mfma_f32_16x16x32_bf16 v[172:175], a[228:231], v[36:39], 0\n"
  "ds_read_b64_tr_b16 a[254:255], v16 offset:2080\n"
  "v_mul_f32 v4, v28, v28\n"
  "v_mfma_f32_16x16x32_bf16 v[176:179], a[232:235], v[32:35], 0\n"
  "buffer_load_dwordx4 v7, s[44:47], s31 offen offset:2048 lds\n"
  "v_mfma_f32_16x16x32_bf16 v[180:183], a[232:235], v[36:39], 0\n"
  "ds_read_b64_tr_b16 a[192:193], v15 offset:320\n"
  "ds_read_b64_tr_b16 a[194:195], v15 offset:2112\n"
  "s_waitcnt lgkmcnt(9)\n"
  "v_mfma_f32_16x16x32_bf16 v[184:187], a[236:239], v[32:35], 0\n"
  "ds_read_b64_tr_b16 a[196:197], v16 offset:352\n"
  "v_mul_f32 v11, 0x3b124925, v11\n"
  "v_mfma_f32_16x16x32_bf16 v[188:191], a[236:239], v[36:39], 0\n"
  "s_mov_b32 exec_hi, 0\n"
  "buffer_load_dwordx4 v8, s[44:47], s31 offen offset:3072 lds\n"
  "s_mov_b32 exec_hi, -1\n"
  "s_waitcnt lgkmcnt(7)\n"
  "v_mfma_f32_16x16x32_bf16 v[192:195], a[240:243], v[32:35], 0\n"
  "ds_read_b64_tr_b16 a[198:199], v16 offset:2144\n"
  "ds_read_b64_tr_b16 a[200:201], v15 offset:384\n"
  "v_mfma_f32_16x16x32_bf16 v[196:199], a[240:243], v[36:39], 0\n"
  "ds_read_b64_tr_b16 a[202:203], v15 offset:2176\n"
  "v_mul_f32 v12, 0x3b124925, v12\n"
  "v_mfma_f32_16x16x32_bf16 v[200:203], a[244:247], v[32:35], 0\n"
  "ds_read_b64_tr_b16 a[204:205], v16 offset:416\n"
  "v_sub_f32 v2, v11, v2\n"
  "v_mfma_f32_16x16x32_bf16 v[204:207], a[244:247], v[36:39], 0\n"
  "s_waitcnt vmcnt(8)\n"
  "s_barrier\n"
  "ds_read_b64_tr_b16 a[206:207], v16 offset:2208\n"
  "s_add_u32 m0, s34, 8192\n"
  "s_mov_b32 s30, 0\n"
  "s_waitcnt lgkmcnt(8)\n"
  "v_mfma_f32_16x16x32_bf16 v[208:211], a[248:251], v[32:35], 0\n"
  "ds_read_b64_tr_b16 a[208:209], v15 offset:16384\n"
  "buffer_load_dwordx4 v3, s[52:55], s30 offen offset:0 nt lds\n"
  "v_mfma_f32_16x16x32_bf16 v[212:215], a[248:251], v[36:39], 0\n"
  "ds_read_b64_tr_b16 a[210:211], v15 offset:18176\n"
  "v_mfma_f32_16x16x32_bf16 v[216:219], a[252:255], v[32:35], 0\n"
  "ds_read_b64_tr_b16 a[212:213], v16 offset:16416\n"
  "s_mov_b32 s30, 64512\n"
  "v_sub_f32 v4, v12, v4\n"
  "v_mfma_f32_16x16x32_bf16 v[220:223], a[252:255], v[36:39], 0\n"
  "buffer_load_dwordx4 v3, s[52:55], s30 offen offset:1024 nt lds\n"
  "s_waitcnt lgkmcnt(8)\n"
  "v_mfma_f32_16x16x32_bf16 v[224:227], a[192:195], v[32:35], 0\n"
  "ds_read_b64_tr_b16 a[214:215], v16 offset:18208\n"
  "ds_read_b64_tr_b16 a[216:217], v15 offset:16448\n"
  "v_mfma_f32_16x16x32_bf16 v[228:231], a[192:195], v[36:39], 0\n"
  "ds_read_b64_tr_b16 a[218:219], v15 offset:18240\n"
  "v_max_f32 v2, 0, v2\n"
  "s_waitcnt lgkmcnt(8)\n"
  "v_mfma_f32_16x16x32_bf16 v[232:235], a[196:199], v[32:35], 0\n"
  "s_mov_b32 s30, 14336\n"
  "v_max_f32 v4, 0, v4\n"
  "v_add_f32 v2, 0x3727c5ac, v2\n"
  "v_mfma_f32_16x16x32_bf16 v[236:239], a[196:199], v[36:39], 0\n"
  "buffer_load_dwordx4 v3, s[52:55], s30 offen offset:2048 nt lds\n"
  "v_mfma_f32_16x16x32_bf16 v[240:243], a[200:203], v[32:35], 0\n"
  "ds_read_b64_tr_b16 a[220:221], v16 offset:16480\n"
  "v_mfma_f32_16x16x32_bf16 v[244:247], a[200:203], v[36:39], 0\n"
  "ds_read_b64_tr_b16 a[222:223], v16 offset:18272\n"
  "v_add_f32 v4, 0x3727c5ac, v4\n"
  "s_waitcnt lgkmcnt(6)\n"
  "v_mfma_f32_16x16x32_bf16 v[248:251], a[204:207], v[32:35], 0\n"
  "ds_read_b64_tr_b16 a[224:225], v15 offset:16512\n"
  "s_mov_b32 s30, 78848\n"
  "v_rsq_f32 v11, v2\n"
  "v_mfma_f32_16x16x32_bf16 v[252:255], a[204:207], v[36:39], 0\n"
  "buffer_load_dwordx4 v3, s[52:55], s30 offen offset:3072 nt lds\n"
  "v_mfma_f32_16x16x32_bf16 v[144:147], a[208:211], v[40:43], v[144:147]\n"
  "ds_read_b64_tr_b16 a[226:227], v15 offset:18304\n"
  "ds_read_b64_tr_b16 a[228:229], v16 offset:16544\n"
  "v_mfma_f32_16x16x32_bf16 v[148:151], a[208:211], v[44:47], v[148:151]\n"
  "ds_read_b64_tr_b16 a[230:231], v16 offset:18336\n"
  "v_rsq_f32 v12, v4\n"
  "s_waitcnt lgkmcnt(6)\n"
  "v_mfma_f32_16x16x32_bf16 v[152:155], a[212:215], v[40:43], v[152:155]\n"
  "ds_read_b64_tr_b16 a[232:233], v15 offset:16576\n"
  "s_add_u32 m0, s34, 12288\n"
  "s_mov_b32 s30, 128\n"
  "ds_write_b32 v22, v24 offset:128\n"
  "v_mfma_f32_16x16x32_bf16 v[156:159], a[212:215], v[44:47], v[156:159]\n"
  "ds_read_b64_tr_b16 a[234:235], v15 offset:18368\n"
  "buffer_load_dwordx4 v3, s[52:55], s30 offen offset:0 nt lds\n"
  "v_mfma_f32_16x16x32_bf16 v[160:163], a[216:219], v[40:43], v[160:163]\n"
  "ds_read_b64_tr_b16 a[236:237], v16 offset:16608\n"
  "ds_write_b32 v22, v28 offset:192\n"
  "v_mfma_f32_16x16x32_bf16 v[164:167], a[216:219], v[44:47], v[164:167]\n"
  "ds_read_b64_tr_b16 a[238:239], v16 offset:18400\n"
  "v_mfma_f32_16x16x32_bf16 v[32:35], a[186:189], v[48:51], v[24:27]\n"
  "ds_read_b64_tr_b16 a[240:241], v15 offset:16640\n"
  "s_mov_b32 s30, 64640\n"
  "v_xor_b32 v2, s70, v11\n"
  "v_mfma_f32_16x16x32_bf16 v[36:39], a[184:187], v[48:51], v[24:27]\n"
  "buffer_load_dwordx4 v3, s[52:55], s30 offen offset:1024 nt lds\n"
  "s_waitcnt lgkmcnt(10)\n"
  "v_mfma_f32_16x16x32_bf16 v[168:171], a[220:223], v[40:43], v[168:171]\n"
  "ds_read_b64_tr_b16 a[242:243], v15 offset:18432\n"
  "v_xor_b32 v4, s70, v12\n"
  "v_mfma_f32_16x16x32_bf16 v[172:175], a[220:223], v[44:47], v[172:175]\n"
  "ds_write_b32 v22, v2 offset:0\n"
  "ds_write_b32 v22, v4 offset:64\n"
  "s_waitcnt lgkmcnt(10)\n"
  "v_mfma_f32_16x16x32_bf16 v[176:179], a[224:227], v[40:43], v[176:179]\n"
  "ds_read_b64_tr_b16 a[244:245], v16 offset:16672\n"
  "v_cvt_pk_bf16_f32 v48, v32, v33\n"
  "s_mov_b32 s30, 14464\n"
  "v_mfma_f32_16x16x32_bf16 v[180:183], a[224:227], v[44:47], v[180:183]\n"
  "buffer_load_dwordx4 v3, s[52:55], s30 offen offset:2048 nt lds\n"
  "v_cvt_pk_bf16_f32 v49, v34, v35\n"
  "v_mfma_f32_16x16x32_bf16 v[184:187], a[228:231], v[40:43], v[184:187]\n"
  "ds_read_b64_tr_b16 a[246:247], v16 offset:18464\n"
  "ds_read_b64_tr_b16 a[248:249], v15 offset:16704\n"
  "v_cvt_pk_bf16_f32 v50, v36, v37\n"
  "v_mfma_f32_16x16x32_bf16 v[188:191], a[228:231], v[44:47], v[188:191]\n"
  "ds_read_b64_tr_b16 a[250:251], v15 offset:18496\n"
  "v_cvt_pk_bf16_f32 v51, v38, v39\n"
  "s_waitcnt lgkmcnt(8)\n"
  "v_mfma_f32_16x16x32_bf16 v[192:195], a[232:235], v[40:43], v[192:195]\n"
  "ds_read_b64_tr_b16 a[252:253], v16 offset:16736\n"
  "s_mov_b32 s30, 78976\n"
  "v_mfma_f32_16x16x32_bf16 v[196:199], a[232:235], v[44:47], v[196:199]\n"
  "buffer_load_dwordx4 v3, s[52:55], s30 offen offset:3072 nt lds\n"
  "v_mfma_f32_16x16x32_bf16 v[200:203], a[236:239], v[40:43], v[200:203]\n"
  "ds_read_b64_tr_b16 a[254:255], v16 offset:18528\n"
  "ds_read_b64_tr_b16 a[192:193], v15 offset:16768\n"
  "v_mfma_f32_16x16x32_bf16 v[204:207], a[236:239], v[44:47], v[204:207]\n"
  "ds_read_b64_tr_b16 a[194:195], v15 offset:18560\n"
  "s_waitcnt lgkmcnt(8)\n"
  "v_mfma_f32_16x16x32_bf16 v[208:211], a[240:243], v[40:43], v[208:211]\n"
  "ds_read_b64_tr_b16 a[196:197], v16 offset:16800\n"
  "v_mfma_f32_16x16x32_bf16 v[212:215], a[240:243], v[44:47], v[212:215]\n"
  "s_barrier\n"
  "ds_read_b64_tr_b16 a[198:199], v16 offset:18592\n"
  "v_mfma_f32_16x16x32_bf16 v[32:35], a[186:189], v[52:55], v[28:31]\n"
  "ds_read_b64_tr_b16 a[200:201], v13 offset:0\n"
  "v_mfma_f32_16x16x32_bf16 v[36:39], a[184:187], v[52:55], v[28:31]\n"
  "ds_read_b64_tr_b16 a[202:203], v13 offset:1792\n"
  "s_waitcnt lgkmcnt(8)\n"
  "v_mfma_f32_16x16x32_bf16 v[216:219], a[244:247], v[40:43], v[216:219]\n"
  "ds_read_b64_tr_b16 a[204:205], v14 offset:32\n"
  "v_mfma_f32_16x16x32_bf16 v[220:223], a[244:247], v[44:47], v[220:223]\n"
  "ds_read_b64_tr_b16 a[206:207], v14 offset:1824\n"
  "v_mfma_f32_16x16x32_bf16 v[224:227], a[248:251], v[40:43], v[224:227]\n"
  "ds_read_b64_tr_b16 a[208:209], v13 offset:64\n"
  "v_cvt_pk_bf16_f32 v52, v32, v33\n"
  "v_mfma_f32_16x16x32_bf16 v[228:231], a[248:251], v[44:47], v[228:231]\n"
  "ds_read_b64_tr_b16 a[210:211], v13 offset:1856\n"
  "v_cvt_pk_bf16_f32 v53, v34, v35\n"
  "s_waitcnt lgkmcnt(8)\n"
  "v_mfma_f32_16x16x32_bf16 v[232:235], a[252:255], v[40:43], v[232:235]\n"
  "v_cvt_pk_bf16_f32 v54, v36, v37\n"
  "v_mfma_f32_16x16x32_bf16 v[236:239], a[252:255], v[44:47], v[236:239]\n"
  "v_cvt_pk_bf16_f32 v55, v38, v39\n"
  "v_mfma_f32_16x16x32_bf16 v[240:243], a[192:195], v[40:43], v[240:243]\n"
  "ds_read_b64_tr_b16 a[212:213], v14 offset:96\n"
  "v_mfma_f32_16x16x32_bf16 v[244:247], a[192:195], v[44:47], v[244:247]\n"
  "ds_read_b64_tr_b16 a[214:215], v14 offset:1888\n"
  "s_waitcnt lgkmcnt(6)\n"
  "v_mfma_f32_16x16x32_bf16 v[248:251], a[196:199], v[40:43], v[248:251]\n"
  "ds_read_b64_tr_b16 a[216:217], v13 offset:128\n"
  "v_mfma_f32_16x16x32_bf16 v[252:255], a[196:199], v[44:47], v[252:255]\n"
  "ds_read_b64_tr_b16 a[218:219], v13 offset:1920\n"
  "v_mfma_f32_16x16x32_bf16 v[144:147], a[200:203], v[48:51], v[144:147]\n"
  "ds_read_b64_tr_b16 a[220:221], v14 offset:160\n"
  "v_mfma_f32_16x16x32_bf16 v[148:151], a[200:203], v[52:55], v[148:151]\n"
  "ds_read_b64_tr_b16 a[222:223], v14 offset:1952\n"
  "s_waitcnt lgkmcnt(6)\n"
  "v_mfma_f32_16x16x32_bf16 v[152:155], a[204:207], v[48:51], v[152:155]\n"
  "ds_read_b64_tr_b16 a[224:225], v13 offset:192\n"
  "v_mfma_f32_16x16x32_bf16 v[156:159], a[204:207], v[52:55], v[156:159]\n"
  "ds_read_b64_tr_b16 a[226:227], v13 offset:1984\n"
  "v_mfma_f32_16x16x32_bf16 v[160:163], a[208:211], v[48:51], v[160:163]\n"
  "ds_read_b64_tr_b16 a[228:229], v14 offset:224\n"
  "v_mfma_f32_16x16x32_bf16 v[164:167], a[208:211], v[52:55], v[164:167]\n"
  "ds_read_b64_tr_b16 a[230:231], v14 offset:2016\n"
  "v_mfma_f32_16x16x32_bf16 v[32:35], a[186:189], v[56:59], v[24:27]\n"
  "ds_read_b64_tr_b16 a[232:233], v13 offset:256\n"
  "v_mfma_f32_16x16x32_bf16 v[36:39], a[184:187], v[56:59], v[24:27]\n"
  "ds_read_b64_tr_b16 a[234:235], v13 offset:2048\n"
  "s_waitcnt lgkmcnt(8)\n"
  "v_mfma_f32_16x16x32_bf16 v[168:171], a[212:215], v[48:51], v[168:171]\n"
  "v_mfma_f32_16x16x32_bf16 v[172:175], a[212:215], v[52:55], v[172:175]\n"
  "v_mfma_f32_16x16x32_bf16 v[176:179], a[216:219], v[48:51], v[176:179]\n"
  "ds_read_b64_tr_b16 a[236:237], v14 offset:288\n"
  "v_cvt_pk_bf16_f32 v56, v32, v33\n"
  "v_mfma_f32_16x16x32_bf16 v[180:183], a[216:219], v[52:55], v[180:183]\n"
  "ds_read_b64_tr_b16 a[238:239], v14 offset:2080\n"
  "v_cvt_pk_bf16_f32 v57, v34, v35\n"
  "s_waitcnt lgkmcnt(6)\n"
  "v_mfma_f32_16x16x32_bf16 v[184:187], a[220:223], v[48:51], v[184:187]\n"
  "ds_read_b64_tr_b16 a[240:241], v13 offset:320\n"
  "v_cvt_pk_bf16_f32 v58, v36, v37\n"
  "v_mfma_f32_16x16x32_bf16 v[188:191], a[220:223], v[52:55], v[188:191]\n"
  "ds_read_b64_tr_b16 a[242:243], v13 offset:2112\n"
  "v_cvt_pk_bf16_f32 v59, v38, v39\n"
  "v_mfma_f32_16x16x32_bf16 v[192:195], a[224:227], v[48:51], v[192:195]\n"
  "ds_read_b64_tr_b16 a[244:245], v14 offset:352\n"
  "v_mfma_f32_16x16x32_bf16 v[196:199], a[224:227], v[52:55], v[196:199]\n"
  "ds_read_b64_tr_b16 a[246:247], v14 offset:2144\n"
  "s_waitcnt lgkmcnt(6)\n"
  "v_mfma_f32_16x16x32_bf16 v[200:203], a[228:231], v[48:51], v[200:203]\n"
  "ds_read_b64_tr_b16 a[248:249], v13 offset:384\n"
  "v_mfma_f32_16x16x32_bf16 v[204:207], a[228:231], v[52:55], v[204:207]\n"
  "ds_read_b64_tr_b16 a[250:251], v13 offset:2176\n"
  "v_mfma_f32_16x16x32_bf16 v[208:211], a[232:235], v[48:51], v[208:211]\n"
  "ds_read_b64_tr_b16 a[252:253], v14 offset:416\n"
  "v_mfma_f32_16x16x32_bf16 v[212:215], a[232:235], v[52:55], v[212:215]\n"
  "s_waitcnt vmcnt(12)\n"
  "s_barrier\n"
  "ds_read_b64_tr_b16 a[254:255], v14 offset:2208\n"
  "v_mfma_f32_16x16x32_bf16 v[32:35], a[186:189], v[60:63], v[28:31]\n"
  "ds_read_b64_tr_b16 a[192:193], v13 offset:16384\n"
  "v_mfma_f32_16x16x32_bf16 v[36:39], a[184:187], v[60:63], v[28:31]\n"
  "ds_read_b64_tr_b16 a[194:195], v13 offset:18176\n"
  "s_waitcnt lgkmcnt(8)\n"
  "v_mfma_f32_16x16x32_bf16 v[216:219], a[236:239], v[48:51], v[216:219]\n"
  "ds_read_b64_tr_b16 a[196:197], v14 offset:16416\n"
  "v_mfma_f32_16x16x32_bf16 v[220:223], a[236:239], v[52:55], v[220:223]\n"
  "ds_read_b64_tr_b16 a[198:199], v14 offset:18208\n"
  "v_mfma_f32_16x16x32_bf16 v[224:227], a[240:243], v[48:51], v[224:227]\n"
  "ds_read_b64_tr_b16 a[200:201], v13 offset:16448\n"
  "v_cvt_pk_bf16_f32 v60, v32, v33\n"
  "v_mfma_f32_16x16x32_bf16 v[228:231], a[240:243], v[52:55], v[228:231]\n"
  "ds_read_b64_tr_b16 a[202:203], v13 offset:18240\n"
  "v_cvt_pk_bf16_f32 v61, v34, v35\n"
  "s_waitcnt lgkmcnt(8)\n"
  "v_mfma_f32_16x16x32_bf16 v[232:235], a[244:247], v[48:51], v[232:235]\n"
  "v_cvt_pk_bf16_f32 v62, v36, v37\n"
  "v_mfma_f32_16x16x32_bf16 v[236:239], a[244:247], v[52:55], v[236:239]\n"
  "v_cvt_pk_bf16_f32 v63, v38, v39\n"
  "v_mfma_f32_16x16x32_bf16 v[240:243], a[248:251], v[48:51], v[240:243]\n"
  "ds_read_b64_tr_b16 a[204:205], v14 offset:16480\n"
  "v_mfma_f32_16x16x32_bf16 v[244:247], a[248:251], v[52:55], v[244:247]\n"
  "ds_read_b64_tr_b16 a[206:207], v14 offset:18272\n"
  "s_waitcnt lgkmcnt(6)\n"
  "v_mfma_f32_16x16x32_bf16 v[248:251], a[252:255], v[48:51], v[248:251]\n"
  "ds_read_b64_tr_b16 a[208:209], v13 offset:16512\n"
  "v_mfma_f32_16x16x32_bf16 v[252:255], a[252:255], v[52:55], v[252:255]\n"
  "ds_read_b64_tr_b16 a[210:211], v13 offset:18304\n"
  "s_add_u32 m0, s35, 49152\n"
  "s_mov_b32 s31, 573440\n"
  "v_mfma_f32_16x16x32_bf16 v[144:147], a[192:195], v[56:59], v[144:147]\n"
  "buffer_load_dwordx4 v5, s[44:47], s31 offen offset:0 lds\n"
  "v_mfma_f32_16x16x32_bf16 v[148:151], a[192:195], v[60:63], v[148:151]\n"
  "ds_read_b64_tr_b16 a[212:213], v14 offset:16544\n"
  "ds_read_b64_tr_b16 a[214:215], v14 offset:18336\n"
  "s_waitcnt lgkmcnt(6)\n"
  "v_mfma_f32_16x16x32_bf16 v[152:155], a[196:199], v[56:59], v[152:155]\n"
  "ds_read_b64_tr_b16 a[216:217], v13 offset:16576\n"
  "v_accvgpr_read_b32 v22, a160\n"
  "v_mfma_f32_16x16x32_bf16 v[156:159], a[196:199], v[60:63], v[156:159]\n"
  "ds_read_b64_tr_b16 a[218:219], v13 offset:18368\n"
  "ds_read_b128 v[48:51], v22 offset:128\n"
  "v_mfma_f32_16x16x32_bf16 v[160:163], a[200:203], v[56:59], v[160:163]\n"
  "buffer_load_dwordx4 v6, s[44:47], s31 offen offset:1024 lds\n"
  "v_mfma_f32_16x16x32_bf16 v[164:167], a[200:203], v[60:63], v[164:167]\n"
  "ds_read_b64_tr_b16 a[220:221], v14 offset:16608\n"
  "ds_read_b64_tr_b16 a[222:223], v14 offset:18400\n"
  "v_mfma_f32_16x16x32_bf16 v[32:35], a[186:189], v[64:67], v[24:27]\n"
  "ds_read_b64_tr_b16 a[224:225], v13 offset:16640\n"
  "ds_read_b128 v[52:55], v22 offset:192\n"
  "v_mfma_f32_16x16x32_bf16 v[36:39], a[184:187], v[64:67], v[24:27]\n"
  "ds_read_b64_tr_b16 a[226:227], v13 offset:18432\n"
  "ds_read_b128 v[40:43], v22 offset:0\n"
  "s_waitcnt lgkmcnt(11)\n"
  "v_mfma_f32_16x16x32_bf16 v[168:171], a[204:207], v[56:59], v[168:171]\n"
  "ds_read_b128 v[44:47], v22 offset:64\n"
  "v_accvgpr_read_b32 v22, a161\n"
  "v_mfma_f32_16x16x32_bf16 v[172:175], a[204:207], v[60:63], v[172:175]\n"
  "buffer_load_dwordx4 v7, s[44:47], s31 offen offset:2048 lds\n"
  "v_mfma_f32_16x16x32_bf16 v[176:179], a[208:211], v[56:59], v[176:179]\n"
  "ds_read_b64_tr_b16 a[228:229], v14 offset:16672\n"
  "v_cvt_pk_bf16_f32 v64, v32, v33\n"
  "v_mfma_f32_16x16x32_bf16 v[180:183], a[208:211], v[60:63], v[180:183]\n"
  "ds_read_b64_tr_b16 a[230:231], v14 offset:18464\n"
  "v_cvt_pk_bf16_f32 v65, v34, v35\n"
  "s_waitcnt lgkmcnt(9)\n"
  "v_mfma_f32_16x16x32_bf16 v[184:187], a[212:215], v[56:59], v[184:187]\n"
  "ds_read_b64_tr_b16 a[232:233], v13 offset:16704\n"
  "v_cvt_pk_bf16_f32 v66, v36, v37\n"
  "v_mfma_f32_16x16x32_bf16 v[188:191], a[212:215], v[60:63], v[188:191]\n"
  "ds_read_b64_tr_b16 a[234:235], v13 offset:18496\n"
  "v_cvt_pk_bf16_f32 v67, v38, v39\n"
  "v_mfma_f32_16x16x32_bf16 v[192:195], a[216:219], v[56:59], v[192:195]\n"
  "s_mov_b32 exec_hi, 0\n"
  "buffer_load_dwordx4 v8, s[44:47], s31 offen offset:3072 lds\n"
  "s_mov_b32 exec_hi, -1\n"
  "v_mfma_f32_16x16x32_bf16 v[196:199], a[216:219], v[60:63], v[196:199]\n"
  "ds_read_b64_tr_b16 a[236:237], v14 offset:16736\n"
  "ds_read_b64_tr_b16 a[238:239], v14 offset:18528\n"
  "s_waitcnt lgkmcnt(7)\n"
  "v_mfma_f32_16x16x32_bf16 v[200:203], a[220:223], v[56:59], v[200:203]\n"
  "ds_read_b64_tr_b16 a[240:241], v13 offset:16768\n"
  "ds_read_b32 v2, v22 offset:256\n"
  "v_mfma_f32_16x16x32_bf16 v[204:207], a[220:223], v[60:63], v[204:207]\n"
  "ds_read_b64_tr_b16 a[242:243], v13 offset:18560\n"
  "ds_read_b32 v4, v22 offset:768\n"
  "v_mfma_f32_16x16x32_bf16 v[208:211], a[224:227], v[56:59], v[208:211]\n"
  "ds_read_b64_tr_b16 a[244:245], v14 offset:16800\n"
  "v_mfma_f32_16x16x32_bf16 v[212:215], a[224:227], v[60:63], v[212:215]\n"
  "s_waitcnt vmcnt(12)\n"
  "s_barrier\n"
  "ds_read_b64_tr_b16 a[246:247], v14 offset:18592\n"
  "s_add_u32 m0, s35, 65536\n"
  "s_mov_b32 s31, 688128\n"
  "v_mfma_f32_16x16x32_bf16 v[32:35], a[186:189], v[68:71], v[28:31]\n"
  "buffer_load_dwordx4 v5, s[44:47], s31 offen offset:0 lds\n"
  "v_mfma_f32_16x16x32_bf16 v[36:39], a[184:187], v[68:71], v[28:31]\n"
  "ds_read_b64_tr_b16 a[248:249], v13 offset:32768\n"
  "ds_read_b64_tr_b16 a[250:251], v13 offset:34560\n"
  "s_waitcnt lgkmcnt(10)\n"
  "v_mfma_f32_16x16x32_bf16 v[216:219], a[228:231], v[56:59], v[216:219]\n"
  "ds_read_b64_tr_b16 a[252:253], v14 offset:32800\n"
  "v_mfma_f32_16x16x32_bf16 v[220:223], a[228:231], v[60:63], v[220:223]\n"
  "Lto1%=:\n"
  "s_cbranch_execz Ltor0%=\n"
  "buffer_load_dwordx4 v6, s[44:47], s31 offen offset:1024 lds\n"
  "v_mfma_f32_16x16x32_bf16 v[224:227], a[232:235], v[56:59], v[224:227]\n"
  "ds_read_b64_tr_b16 a[254:255], v14 offset:34592\n"
  "ds_read_b64_tr_b16 a[192:193], v13 offset:32832\n"
  "v_cvt_pk_bf16_f32 v68, v32, v33\n"
  "v_mfma_f32_16x16x32_bf16 v[228:231], a[232:235], v[60:63], v[228:231]\n"
  "ds_read_b64_tr_b16 a[194:195], v13 offset:34624\n"
  "v_cvt_pk_bf16_f32 v69, v34, v35\n"
  "s_waitcnt lgkmcnt(8)\n"
  "v_mfma_f32_16x16x32_bf16 v[232:235], a[236:239], v[56:59], v[232:235]\n"
  "v_cvt_pk_bf16_f32 v70, v36, v37\n"
  "v_mfma_f32_16x16x32_bf16 v[236:239], a[236:239], v[60:63], v[236:239]\n"
  "buffer_load_dwordx4 v7, s[44:47], s31 offen offset:2048 lds\n"
  "v_cvt_pk_bf16_f32 v71, v38, v39\n"
  "v_mfma_f32_16x16x32_bf16 v[240:243], a[240:243], v[56:59], v[240:243]\n"
  "ds_read_b64_tr_b16 a[196:197], v14 offset:32864\n"
  "v_mfma_f32_16x16x32_bf16 v[244:247], a[240:243], v[60:63], v[244:247]\n"
  "ds_read_b64_tr_b16 a[198:199], v14 offset:34656\n"
  "s_waitcnt lgkmcnt(6)\n"
  "v_mfma_f32_16x16x32_bf16 v[248:251], a[244:247], v[56:59], v[248:251]\n"
  "ds_read_b64_tr_b16 a[200:201], v13 offset:32896\n"
  "v_mfma_f32_16x16x32_bf16 v[252:255], a[244:247], v[60:63], v[252:255]\n"
  "s_mov_b32 exec_hi, 0\n"
  "buffer_load_dwordx4 v8, s[44:47], s31 offen offset:3072 lds\n"
  "s_mov_b32 exec_hi, -1\n"
  "v_mfma_f32_16x16x32_bf16 v[144:147], a[248:251], v[64:67], v[144:147]\n"
  "ds_read_b64_tr_b16 a[202:203], v13 offset:34688\n"
  "ds_read_b64_tr_b16 a[204:205], v14 offset:32928\n"
  "v_mfma_f32_16x16x32_bf16 v[148:151], a[248:251], v[68:71], v[148:151]\n"
  "ds_read_b64_tr_b16 a[206:207], v14 offset:34720\n"
  "s_waitcnt lgkmcnt(6)\n"
  "v_mfma_f32_16x16x32_bf16 v[152:155], a[252:255], v[64:67], v[152:155]\n"
  "ds_read_b64_tr_b16 a[208:209], v13 offset:32960\n"
  "s_add_u32 m0, s35, 81920\n"
  "s_mov_b32 s31, 802816\n"
  "v_mfma_f32_16x16x32_bf16 v[156:159], a[252:255], v[68:71], v[156:159]\n"
  "buffer_load_dwordx4 v5, s[44:47], s31 offen offset:0 lds\n"
  "v_mfma_f32_16x16x32_bf16 v[160:163], a[192:195], v[64:67], v[160:163]\n"
  "ds_read_b64_tr_b16 a[210:211], v13 offset:34752\n"
  "ds_read_b64_tr_b16 a[212:213], v14 offset:32992\n"
  "v_mfma_f32_16x16x32_bf16 v[164:167], a[192:195], v[68:71], v[164:167]\n"
  "ds_read_b64_tr_b16 a[214:215], v14 offset:34784\n"
  "v_mfma_f32_16x16x32_bf16 v[32:35], a[186:189], v[72:75], v[24:27]\n"
  "ds_read_b64_tr_b16 a[216:217], v13 offset:33024\n"
  "v_mfma_f32_16x16x32_bf16 v[36:39], a[184:187], v[72:75], v[24:27]\n"
  "buffer_load_dwordx4 v6, s[44:47], s31 offen offset:1024 lds\n"
  "s_waitcnt lgkmcnt(8)\n"
  "v_mfma_f32_16x16x32_bf16 v[168:171], a[196:199], v[64:67], v[168:171]\n"
  "ds_read_b64_tr_b16 a[218:219], v13 offset:34816\n"
  "v_mfma_f32_16x16x32_bf16 v[172:175], a[196:199], v[68:71], v[172:175]\n"
  "s_waitcnt lgkmcnt(6)\n"
  "v_mfma_f32_16x16x32_bf16 v[176:179], a[200:203], v[64:67], v[176:179]\n"
  "ds_read_b64_tr_b16 a[220:221], v14 offset:33056\n"
  "v_cvt_pk_bf16_f32 v72, v32, v33\n"
  "v_mfma_f32_16x16x32_bf16 v[180:183], a[200:203], v[68:71], v[180:183]\n"
  "buffer_load_dwordx4 v7, s[44:47], s31 offen offset:2048 lds\n"
  "v_cvt_pk_bf16_f32 v73, v34, v35\n"
  "v_mfma_f32_16x16x32_bf16 v[184:187], a[204:207], v[64:67], v[184:187]\n"
  "ds_read_b64_tr_b16 a[222:223], v14 offset:34848\n"
  "ds_read_b64_tr_b16 a[224:225], v13 offset:33088\n"
  "v_cvt_pk_bf16_f32 v74, v36, v37\n"
  "v_mfma_f32_16x16x32_bf16 v[188:191], a[204:207], v[68:71], v[188:191]\n"
  "ds_read_b64_tr_b16 a[226:227], v13 offset:34880\n"
  "v_cvt_pk_bf16_f32 v75, v38, v39\n"
  "s_waitcnt lgkmcnt(6)\n"
  "v_mfma_f32_16x16x32_bf16 v[192:195], a[208:211], v[64:67], v[192:195]\n"
  "ds_read_b64_tr_b16 a[228:229], v14 offset:33120\n"
  "v_mfma_f32_16x16x32_bf16 v[196:199], a[208:211], v[68:71], v[196:199]\n"
  "s_mov_b32 exec_hi, 0\n"
  "buffer_load_dwordx4 v8, s[44:47], s31 offen offset:3072 lds\n"
  "s_mov_b32 exec_hi, -1\n"
  "v_mfma_f32_16x16x32_bf16 v[200:203], a[212:215], v[64:67], v[200:203]\n"
  "ds_read_b64_tr_b16 a[230:231], v14 offset:34912\n"
  "ds_read_b64_tr_b16 a[232:233], v13 offset:33152\n"
  "v_mfma_f32_16x16x32_bf16 v[204:207], a[212:215], v[68:71], v[204:207]\n"
  "ds_read_b64_tr_b16 a[234:235], v13 offset:34944\n"
  "s_waitcnt lgkmcnt(8)\n"
  "v_mfma_f32_16x16x32_bf16 v[208:211], a[216:219], v[64:67], v[208:211]\n"
  "ds_read_b64_tr_b16 a[236:237], v14 offset:33184\n"
  "v_mfma_f32_16x16x32_bf16 v[212:215], a[216:219], v[68:71], v[212:215]\n"
  "s_waitcnt vmcnt(8)\n"
  "s_barrier\n"
  "ds_read_b64_tr_b16 a[238:239], v14 offset:34976\n"
  "s_add_u32 m0, s35, 0\n"
  "s_mov_b32 s31, 917504\n"
  "v_mfma_f32_16x16x32_bf16 v[32:35], a[186:189], v[76:79], v[28:31]\n"
  "buffer_load_dwordx4 v5, s[44:47], s31 offen offset:0 lds\n"
  "v_mfma_f32_16x16x32_bf16 v[36:39], a[184:187], v[76:79], v[28:31]\n"
  "ds_read_b64_tr_b16 a[240:241], v13 offset:49152\n"
  "ds_read_b64_tr_b16 a[242:243], v13 offset:50944\n"
  "ds_read_b128 a[0:3], v9 offset:8192\n"
  "s_waitcnt lgkmcnt(9)\n"
  "v_mfma_f32_16x16x32_bf16 v[216:219], a[220:223], v[64:67], v[216:219]\n"
  "ds_read_b64_tr_b16 a[244:245], v14 offset:49184\n"
  "v_mfma_f32_16x16x32_bf16 v[220:223], a[220:223], v[68:71], v[220:223]\n"
  "buffer_load_dwordx4 v6, s[44:47], s31 offen offset:1024 lds\n"
  "v_mfma_f32_16x16x32_bf16 v[224:227], a[224:227], v[64:67], v[224:227]\n"
  "ds_read_b64_tr_b16 a[246:247], v14 offset:50976\n"
  "ds_read_b128 a[4:7], v9 offset:10240\n"
  "ds_read_b64_tr_b16 a[248:249], v13 offset:49216\n"
  "v_cvt_pk_bf16_f32 v76, v32, v33\n"
  "v_mfma_f32_16x16x32_bf16 v[228:231], a[224:227], v[68:71], v[228:231]\n"
  "ds_read_b64_tr_b16 a[250:251], v13 offset:51008\n"
  "ds_read_b128 a[8:11], v10 offset:8192\n"
  "v_cvt_pk_bf16_f32 v77, v34, v35\n"
  "s_waitcnt lgkmcnt(11)\n"
  "v_mfma_f32_16x16x32_bf16 v[232:235], a[228:231], v[64:67], v[232:235]\n"
  "v_cvt_pk_bf16_f32 v78, v36, v37\n"
  "v_mfma_f32_16x16x32_bf16 v[236:239], a[228:231], v[68:71], v[236:239]\n"
  "buffer_load_dwordx4 v7, s[44:47], s31 offen offset:2048 lds\n"
  "v_cvt_pk_bf16_f32 v79, v38, v39\n"
  "v_mfma_f32_16x16x32_bf16 v[240:243], a[232:235], v[64:67], v[240:243]\n"
  "ds_read_b128 a[12:15], v10 offset:10240\n"
  "ds_read_b64_tr_b16 a[252:253], v14 offset:49248\n"
  "v_mfma_f32_16x16x32_bf16 v[244:247], a[232:235], v[68:71], v[244:247]\n"
  "ds_read_b64_tr_b16 a[254:255], v14 offset:51040\n"
  "ds_read_b128 a[16:19], v9 offset:12288\n"
  "s_waitcnt lgkmcnt(10)\n"
  "v_mfma_f32_16x16x32_bf16 v[248:251], a[236:239], v[64:67], v[248:251]\n"
  "ds_read_b64_tr_b16 a[192:193], v13 offset:49280\n"
  "v_mfma_f32_16x16x32_bf16 v[252:255], a[236:239], v[68:71], v[252:255]\n"
  "s_mov_b32 exec_hi, 0\n"
  "buffer_load_dwordx4 v8, s[44:47], s31 offen offset:3072 lds\n"
  "s_mov_b32 exec_hi, -1\n"
  "v_mfma_f32_16x16x32_bf16 v[144:147], a[240:243], v[72:75], v[144:147]\n"
  "ds_read_b64_tr_b16 a[194:195], v13 offset:51072\n"
  "ds_read_b128 a[20:23], v9 offset:14336\n"
  "ds_read_b64_tr_b16 a[196:197], v14 offset:49312\n"
  "v_mfma_f32_16x16x32_bf16 v[148:151], a[240:243], v[76:79], v[148:151]\n"
  "ds_read_b64_tr_b16 a[198:199], v14 offset:51104\n"
  "ds_read_b128 a[24:27], v10 offset:12288\n"
  "s_waitcnt lgkmcnt(10)\n"
  "v_mfma_f32_16x16x32_bf16 v[152:155], a[244:247], v[72:75], v[152:155]\n"
  "ds_read_b64_tr_b16 a[200:201], v13 offset:49344\n"
  "s_add_u32 m0, s35, 16384\n"
  "s_mov_b32 s31, 1032192\n"
  "v_mfma_f32_16x16x32_bf16 v[156:159], a[244:247], v[76:79], v[156:159]\n"
  "buffer_load_dwordx4 v5, s[44:47], s31 offen offset:0 lds\n"
  "v_mfma_f32_16x16x32_bf16 v[160:163], a[248:251], v[72:75], v[160:163]\n"
  "ds_read_b64_tr_b16 a[202:203], v13 offset:51136\n"
  "ds_read_b128 a[28:31], v10 offset:14336\n"
  "ds_read_b64_tr_b16 a[204:205], v14 offset:49376\n"
  "v_mfma_f32_16x16x32_bf16 v[164:167], a[248:251], v[76:79], v[164:167]\n"
  "ds_read_b64_tr_b16 a[206:207], v14 offset:51168\n"
  "v_mfma_f32_16x16x32_bf16 v[32:35], a[186:189], v[80:83], v[24:27]\n"
  "ds_read_b64_tr_b16 a[208:209], v13 offset:49408\n"
  "v_mfma_f32_16x16x32_bf16 v[36:39], a[184:187], v[80:83], v[24:27]\n"
  "buffer_load_dwordx4 v6, s[44:47], s31 offen offset:1024 lds\n"
  "s_waitcnt lgkmcnt(11)\n"
  "v_mfma_f32_16x16x32_bf16 v[168:171], a[252:255], v[72:75], v[168:171]\n"
  "ds_read_b64_tr_b16 a[210:211], v13 offset:51200\n"
  "v_accvgpr_read_b32 v60, a132\n"
  "v_accvgpr_read_b32 v61, a133\n"
  "v_accvgpr_read_b32 v62, a134\n"
  "v_mfma_f32_16x16x32_bf16 v[172:175], a[252:255], v[76:79], v[172:175]\n"
  "v_accvgpr_read_b32 v63, a135\n"
  "v_accvgpr_read_b32 v56, a128\n"
  "s_waitcnt lgkmcnt(7)\n"
  "v_mfma_f32_16x16x32_bf16 v[176:179], a[192:195], v[72:75], v[176:179]\n"
  "ds_read_b64_tr_b16 a[212:213], v14 offset:49440\n"
  "v_cvt_pk_bf16_f32 v80, v32, v33\n"
  "v_accvgpr_read_b32 v57, a129\n"
  "v_accvgpr_read_b32 v58, a130\n"
  "v_mfma_f32_16x16x32_bf16 v[180:183], a[192:195], v[76:79], v[180:183]\n"
  "buffer_load_dwordx4 v7, s[44:47], s31 offen offset:2048 lds\n"
  "v_cvt_pk_bf16_f32 v81, v34, v35\n"
  "v_mfma_f32_16x16x32_bf16 v[184:187], a[196:199], v[72:75], v[184:187]\n"
  "ds_read_b64_tr_b16 a[214:215], v14 offset:51232\n"
  "ds_read_b64_tr_b16 a[216:217], v13 offset:49472\n"
  "v_cvt_pk_bf16_f32 v82, v36, v37\n"
  "v_accvgpr_read_b32 v59, a131\n"
  "v_mfma_f32_16x16x32_bf16 v[188:191], a[196:199], v[76:79], v[188:191]\n"
  "ds_read_b64_tr_b16 a[218:219], v13 offset:51264\n"
  "v_cvt_pk_bf16_f32 v83, v38, v39\n"
  "s_waitcnt lgkmcnt(6)\n"
  "v_mfma_f32_16x16x32_bf16 v[192:195], a[200:203], v[72:75], v[192:195]\n"
  "ds_read_b64_tr_b16 a[220:221], v14 offset:49504\n"
  "v_fma_f32 v60, v52, v2, v60\n"
  "v_fma_f32 v61, v53, v2, v61\n"
  "v_fma_f32 v62, v54, v2, v62\n"
  "v_mfma_f32_16x16x32_bf16 v[196:199], a[200:203], v[76:79], v[196:199]\n"
  "s_mov_b32 exec_hi, 0\n"
  "buffer_load_dwordx4 v8, s[44:47], s31 offen offset:3072 lds\n"
  "s_mov_b32 exec_hi, -1\n"
  "v_mfma_f32_16x16x32_bf16 v[200:203], a[204:207], v[72:75], v[200:203]\n"
  "ds_read_b64_tr_b16 a[222:223], v14 offset:51296\n"
  "ds_read_b64_tr_b16 a[224:225], v13 offset:49536\n"
  "v_fma_f32 v63, v55, v2, v63\n"
  "v_mfma_f32_16x16x32_bf16 v[204:207], a[204:207], v[76:79], v[204:207]\n"
  "ds_read_b64_tr_b16 a[226:227], v13 offset:51328\n"
  "v_fma_f32 v56, v48, v2, v56\n"
  "v_fma_f32 v57, v49, v2, v57\n"
  "s_waitcnt lgkmcnt(8)\n"
  "v_mfma_f32_16x16x32_bf16 v[208:211], a[208:211], v[72:75], v[208:211]\n"
  "ds_read_b64_tr_b16 a[228:229], v14 offset:49568\n"
  "v_fma_f32 v58, v50, v2, v58\n"
  "v_fma_f32 v59, v51, v2, v59\n"
  "v_mfma_f32_16x16x32_bf16 v[212:215], a[208:211], v[76:79], v[212:215]\n"
  "s_waitcnt vmcnt(12)\n"
  "s_barrier\n"
  "ds_read_b64_tr_b16 a[230:231], v14 offset:51360\n"
  "s_add_u32 m0, s35, 32768\n"
  "s_mov_b32 s31, 1146880\n"
  "ds_read_b32 v2, v22 offset:1280\n"
  "v_mfma_f32_16x16x32_bf16 v[32:35], a[186:189], v[84:87], v[28:31]\n"
  "buffer_load_dwordx4 v5, s[44:47], s31 offen offset:0 lds\n"
  "v_mul_f32 v60, v60, v44\n"
  "v_mfma_f32_16x16x32_bf16 v[36:39], a[184:187], v[84:87], v[28:31]\n"
  "ds_read_b64_tr_b16 a[232:233], v15 offset:0\n"
  "ds_read_b64_tr_b16 a[234:235], v15 offset:1792\n"
  "v_mul_f32 v61, v61, v45\n"
  "s_waitcnt lgkmcnt(9)\n"
  "v_mfma_f32_16x16x32_bf16 v[216:219], a[212:215], v[72:75], v[216:219]\n"
  "ds_read_b64_tr_b16 a[236:237], v16 offset:32\n"
  "v_mul_f32 v62, v62, v46\n"
  "v_mul_f32 v63, v63, v47\n"
  "v_mfma_f32_16x16x32_bf16 v[220:223], a[212:215], v[76:79], v[220:223]\n"
  "buffer_load_dwordx4 v6, s[44:47], s31 offen offset:1024 lds\n"
  "v_mfma_f32_16x16x32_bf16 v[224:227], a[216:219], v[72:75], v[224:227]\n"
  "ds_read_b64_tr_b16 a[238:239], v16 offset:1824\n"
  "ds_read_b64_tr_b16 a[240:241], v15 offset:64\n"
  "v_cvt_pk_bf16_f32 v84, v32, v33\n"
  "v_mul_f32 v56, v56, v40\n"
  "v_mfma_f32_16x16x32_bf16 v[228:231], a[216:219], v[76:79], v[228:231]\n"
  "ds_read_b64_tr_b16 a[242:243], v15 offset:1856\n"
  "v_cvt_pk_bf16_f32 v85, v34, v35\n"
  "s_waitcnt lgkmcnt(9)\n"
  "v_mfma_f32_16x16x32_bf16 v[232:235], a[220:223], v[72:75], v[232:235]\n"
  "buffer_load_dwordx4 v7, s[44:47], s31 offen offset:2048 lds\n"
  "v_cvt_pk_bf16_f32 v86, v36, v37\n"
  "v_mfma_f32_16x16x32_bf16 v[236:239], a[220:223], v[76:79], v[236:239]\n"
  "v_cvt_pk_bf16_f32 v87, v38, v39\n"
  "v_mul_f32 v57, v57, v41\n"
  "v_mul_f32 v58, v58, v42\n"
  "v_mfma_f32_16x16x32_bf16 v[240:243], a[224:227], v[72:75], v[240:243]\n"
  "ds_read_b64_tr_b16 a[244:245], v16 offset:96\n"
  "v_mul_f32 v59, v59, v43\n"
  "v_cvt_pk_bf16_f32 v56, v56, v57\n"
  "v_mfma_f32_16x16x32_bf16 v[244:247], a[224:227], v[76:79], v[244:247]\n"
  "s_mov_b32 exec_hi, 0\n"
  "buffer_load_dwordx4 v8, s[44:47], s31 offen offset:3072 lds\n"
  "s_mov_b32 exec_hi, -1\n"
  "s_waitcnt lgkmcnt(5)\n"
  "v_mfma_f32_16x16x32_bf16 v[248:251], a[228:231], v[72:75], v[248:251]\n"
  "ds_read_b64_tr_b16 a[246:247], v16 offset:1888\n"
  "ds_read_b64_tr_b16 a[248:249], v15 offset:128\n"
  "v_cvt_pk_bf16_f32 v57, v58, v59\n"
  "v_cvt_pk_bf16_f32 v58, v60, v61\n"
  "v_mfma_f32_16x16x32_bf16 v[252:255], a[228:231], v[76:79], v[252:255]\n"
  "ds_read_b64_tr_b16 a[250:251], v15 offset:1920\n"
  "s_add_u32 m0, s34, 0\n"
  "s_mov_b32 s30, 0\n"
  "v_cvt_pk_bf16_f32 v59, v62, v63\n"
  "v_mfma_f32_16x16x32_bf16 v[144:147], a[232:235], v[80:83], v[144:147]\n"
  "ds_read_b64_tr_b16 a[252:253], v16 offset:160\n"
  "v_xor_b32 v3, 0x100, v3\n"
  "buffer_load_dwordx4 v3, s[52:55], s30 offen offset:0 nt lds\n"
  "v_mfma_f32_16x16x32_bf16 v[148:151], a[232:235], v[84:87], v[148:151]\n"
  "ds_read_b64_tr_b16 a[254:255], v16 offset:1952\n"
  "s_add_u32 s29, s71, 0\n"
  "buffer_store_dwordx4 v[56:59], v17, s[60:63], s29 offen sc1\n"
  "s_waitcnt lgkmcnt(6)\n"
  "v_mfma_f32_16x16x32_bf16 v[152:155], a[236:239], v[80:83], v[152:155]\n"
  "ds_read_b64_tr_b16 a[192:193], v15 offset:192\n"
  "s_mov_b32 s30, 64512\n"
  "v_accvgpr_read_b32 v60, a140\n"
  "v_accvgpr_read_b32 v61, a141\n"
  "v_mfma_f32_16x16x32_bf16 v[156:159], a[236:239], v[84:87], v[156:159]\n"
  "buffer_load_dwordx4 v3, s[52:55], s30 offen offset:1024 nt lds\n"
  "v_mfma_f32_16x16x32_bf16 v[160:163], a[240:243], v[80:83], v[160:163]\n"
  "ds_read_b64_tr_b16 a[194:195], v15 offset:1984\n"
  "ds_read_b64_tr_b16 a[196:197], v16 offset:224\n"
  "v_accvgpr_read_b32 v62, a142\n"
  "v_mfma_f32_16x16x32_bf16 v[164:167], a[240:243], v[84:87], v[164:167]\n"
  "ds_read_b64_tr_b16 a[198:199], v16 offset:2016\n"
  "s_mov_b32 s30, 14336\n"
  "v_accvgpr_read_b32 v63, a143\n"
  "v_accvgpr_read_b32 v56, a136\n"
  "v_mfma_f32_16x16x32_bf16 v[32:35], a[186:189], v[88:91], v[24:27]\n"
  "buffer_load_dwordx4 v3, s[52:55], s30 offen offset:2048 nt lds\n"
  "v_mfma_f32_16x16x32_bf16 v[36:39], a[184:187], v[88:91], v[24:27]\n"
  "ds_read_b64_tr_b16 a[200:201], v15 offset:256\n"
  "ds_read_b64_tr_b16 a[202:203], v15 offset:2048\n"
  "v_accvgpr_read_b32 v57, a137\n"
  "s_waitcnt lgkmcnt(8)\n"
  "v_mfma_f32_16x16x32_bf16 v[168:171], a[244:247], v[80:83], v[168:171]\n"
  "s_mov_b32 s30, 78848\n"
  "v_accvgpr_read_b32 v58, a138\n"
  "v_accvgpr_read_b32 v59, a139\n"
  "v_fma_f32 v60, v52, v4, v60\n"
  "v_mfma_f32_16x16x32_bf16 v[172:175], a[244:247], v[84:87], v[172:175]\n"
  "buffer_load_dwordx4 v3, s[52:55], s30 offen offset:3072 nt lds\n"
  "v_mfma_f32_16x16x32_bf16 v[176:179], a[248:251], v[80:83], v[176:179]\n"
  "ds_read_b64_tr_b16 a[204:205], v16 offset:288\n"
  "v_cvt_pk_bf16_f32 v88, v32, v33\n"
  "v_fma_f32 v61, v53, v4, v61\n"
  "v_mfma_f32_16x16x32_bf16 v[180:183], a[248:251], v[84:87], v[180:183]\n"
  "ds_read_b64_tr_b16 a[206:207], v16 offset:2080\n"
  "v_cvt_pk_bf16_f32 v89, v34, v35\n"
  "v_fma_f32 v62, v54, v4, v62\n"
  "s_waitcnt lgkmcnt(7)\n"
  "v_mfma_f32_16x16x32_bf16 v[184:187], a[252:255], v[80:83], v[184:187]\n"
  "ds_read_b64_tr_b16 a[208:209], v15 offset:320\n"
  "s_bfm_b64 exec, 28, 0\n"
  "buffer_load_dword a163, v23, s[48:51], 0 offen\n"
  "s_mov_b64 exec, -1\n"
  "v_cvt_pk_bf16_f32 v90, v36, v37\n"
  "v_mfma_f32_16x16x32_bf16 v[188:191], a[252:255], v[84:87], v[188:191]\n"
  "ds_read_b64_tr_b16 a[210:211], v15 offset:2112\n"
  "v_cvt_pk_bf16_f32 v91, v38, v39\n"
  "v_fma_f32 v63, v55, v4, v63\n"
  "s_waitcnt lgkmcnt(6)\n"
  "v_mfma_f32_16x16x32_bf16 v[192:195], a[192:195], v[80:83], v[192:195]\n"
  "ds_read_b64_tr_b16 a[212:213], v16 offset:352\n"
  "v_fma_f32 v56, v48, v4, v56\n"
  "v_fma_f32 v57, v49, v4, v57\n"
  "v_mfma_f32_16x16x32_bf16 v[196:199], a[192:195], v[84:87], v[196:199]\n"
  "ds_read_b64_tr_b16 a[214:215], v16 offset:2144\n"
  "v_fma_f32 v58, v50, v4, v58\n"
  "v_mfma_f32_16x16x32_bf16 v[200:203], a[196:199], v[80:83], v[200:203]\n"
  "ds_read_b64_tr_b16 a[216:217], v15 offset:384\n"
  "v_fma_f32 v59, v51, v4, v59\n"
  "ds_read_b32 v4, v22 offset:1792\n"
  "v_mfma_f32_16x16x32_bf16 v[204:207], a[196:199], v[84:87], v[204:207]\n"
  "ds_read_b64_tr_b16 a[218:219], v15 offset:2176\n"
  "v_mul_f32 v60, v60, v44\n"
  "v_mul_f32 v61, v61, v45\n"
  "s_waitcnt lgkmcnt(9)\n"
  "v_mfma_f32_16x16x32_bf16 v[208:211], a[200:203], v[80:83], v[208:211]\n"
  "ds_read_b64_tr_b16 a[220:221], v16 offset:416\n"
  "v_mul_f32 v62, v62, v46\n"
  "v_mul_f32 v63, v63, v47\n"
  "v_mfma_f32_16x16x32_bf16 v[212:215], a[200:203], v[84:87], v[212:215]\n"
  "s_waitcnt vmcnt(18)\n"
  "s_barrier\n"
  "ds_read_b64_tr_b16 a[222:223], v16 offset:2208\n"
  "s_add_u32 m0, s35, 49152\n"
  "s_mov_b32 s31, 1261568\n"
  "v_mfma_f32_16x16x32_bf16 v[32:35], a[186:189], v[92:95], v[28:31]\n"
  "buffer_load_dwordx4 v5, s[44:47], s31 offen offset:0 lds\n"
  "v_mfma_f32_16x16x32_bf16 v[36:39], a[184:187], v[92:95], v[28:31]\n"
  "ds_read_b64_tr_b16 a[224:225], v15 offset:16384\n"
  "ds_read_b64_tr_b16 a[226:227], v15 offset:18176\n"
  "v_mul_f32 v56, v56, v40\n"
  "s_waitcnt lgkmcnt(9)\n"
  "v_mfma_f32_16x16x32_bf16 v[216:219], a[204:207], v[80:83], v[216:219]\n"
  "ds_read_b64_tr_b16 a[228:229], v16 offset:16416\n"
  "v_mul_f32 v57, v57, v41\n"
  "v_mul_f32 v58, v58, v42\n"
  "v_mfma_f32_16x16x32_bf16 v[220:223], a[204:207], v[84:87], v[220:223]\n"
  "ds_read_b64_tr_b16 a[230:231], v16 offset:18208\n"
  "v_mul_f32 v59, v59, v43\n"
  "v_cvt_pk_bf16_f32 v56, v56, v57\n"
  "v_mfma_f32_16x16x32_bf16 v[224:227], a[208:211], v[80:83], v[224:227]\n"
  "ds_read_b64_tr_b16 a[232:233], v15 offset:16448\n"
  "v_cvt_pk_bf16_f32 v92, v32, v33\n"
  "v_cvt_pk_bf16_f32 v57, v58, v59\n"
  "v_mfma_f32_16x16x32_bf16 v[228:231], a[208:211], v[84:87], v[228:231]\n"
  "ds_read_b64_tr_b16 a[234:235], v15 offset:18240\n"
  "v_cvt_pk_bf16_f32 v93, v34, v35\n"
  "v_cvt_pk_bf16_f32 v58, v60, v61\n"
  "s_waitcnt lgkmcnt(8)\n"
  "v_mfma_f32_16x16x32_bf16 v[232:235], a[212:215], v[80:83], v[232:235]\n"
  "v_cvt_pk_bf16_f32 v94, v36, v37\n"
  "v_cvt_pk_bf16_f32 v59, v62, v63\n"
  "v_mfma_f32_16x16x32_bf16 v[236:239], a[212:215], v[84:87], v[236:239]\n"
  "buffer_load_dwordx4 v6, s[44:47], s31 offen offset:1024 lds\n"
  "v_cvt_pk_bf16_f32 v95, v38, v39\n"
  "s_add_u32 s29, s71, 65536\n"
  "buffer_store_dwordx4 v[56:59], v17, s[60:63], s29 offen sc1\n"
  "v_mfma_f32_16x16x32_bf16 v[240:243], a[216:219], v[80:83], v[240:243]\n"
  "ds_read_b64_tr_b16 a[236:237], v16 offset:16480\n"
  "v_accvgpr_read_b32 v60, a148\n"
  "v_accvgpr_read_b32 v61, a149\n"
  "v_mfma_f32_16x16x32_bf16 v[244:247], a[216:219], v[84:87], v[244:247]\n"
  "ds_read_b64_tr_b16 a[238:239], v16 offset:18272\n"
  "v_accvgpr_read_b32 v62, a150\n"
  "s_waitcnt lgkmcnt(6)\n"
  "v_mfma_f32_16x16x32_bf16 v[248:251], a[220:223], v[80:83], v[248:251]\n"
  "ds_read_b64_tr_b16 a[240:241], v15 offset:16512\n"
  "v_accvgpr_read_b32 v63, a151\n"
  "v_accvgpr_read_b32 v56, a144\n"
  "v_mfma_f32_16x16x32_bf16 v[252:255], a[220:223], v[84:87], v[252:255]\n"
  "ds_read_b64_tr_b16 a[242:243], v15 offset:18304\n"
  "v_accvgpr_read_b32 v57, a145\n"
  "v_mfma_f32_16x16x32_bf16 v[144:147], a[224:227], v[88:91], v[144:147]\n"
  "ds_read_b64_tr_b16 a[244:245], v16 offset:16544\n"
  "v_accvgpr_read_b32 v58, a146\n"
  "v_accvgpr_read_b32 v59, a147\n"
  "v_mfma_f32_16x16x32_bf16 v[148:151], a[224:227], v[92:95], v[148:151]\n"
  "ds_read_b64_tr_b16 a[246:247], v16 offset:18336\n"
  "v_fma_f32 v60, v52, v2, v60\n"
  "s_waitcnt lgkmcnt(6)\n"
  "v_mfma_f32_16x16x32_bf16 v[152:155], a[228:231], v[88:91], v[152:155]\n"
  "ds_read_b64_tr_b16 a[248:249], v15 offset:16576\n"
  "v_fma_f32 v61, v53, v2, v61\n"
  "v_mfma_f32_16x16x32_bf16 v[156:159], a[228:231], v[92:95], v[156:159]\n"
  "buffer_load_dwordx4 v7, s[44:47], s31 offen offset:2048 lds\n"
  "v_mfma_f32_16x16x32_bf16 v[160:163], a[232:235], v[88:91], v[160:163]\n"
  "ds_read_b64_tr_b16 a[250:251], v15 offset:18368\n"
  "ds_read_b64_tr_b16 a[252:253], v16 offset:16608\n"
  "v_fma_f32 v62, v54, v2, v62\n"
  "v_mfma_f32_16x16x32_bf16 v[164:167], a[232:235], v[92:95], v[164:167]\n"
  "ds_read_b64_tr_b16 a[254:255], v16 offset:18400\n"
  "v_fma_f32 v63, v55, v2, v63\n"
  "v_fma_f32 v56, v48, v2, v56\n"
  "v_mfma_f32_16x16x32_bf16 v[32:35], a[186:189], v[96:99], v[24:27]\n"
  "ds_read_b64_tr_b16 a[192:193], v15 offset:16640\n"
  "v_fma_f32 v57, v49, v2, v57\n"
  "v_fma_f32 v58, v50, v2, v58\n"
  "v_mfma_f32_16x16x32_bf16 v[36:39], a[184:187], v[96:99], v[24:27]\n"
  "ds_read_b64_tr_b16 a[194:195], v15 offset:18432\n"
  "v_fma_f32 v59, v51, v2, v59\n"
  "s_waitcnt lgkmcnt(8)\n"
  "v_mfma_f32_16x16x32_bf16 v[168:171], a[236:239], v[88:91], v[168:171]\n"
  "v_mul_f32 v60, v60, v44\n"
  "v_mul_f32 v61, v61, v45\n"
  "v_mul_f32 v62, v62, v46\n"
  "v_mfma_f32_16x16x32_bf16 v[172:175], a[236:239], v[92:95], v[172:175]\n"
  "v_mul_f32 v63, v63, v47\n"
  "v_mfma_f32_16x16x32_bf16 v[176:179], a[240:243], v[88:91], v[176:179]\n"
  "ds_read_b64_tr_b16 a[196:197], v16 offset:16672\n"
  "v_cvt_pk_bf16_f32 v96, v32, v33\n"
  "v_mul_f32 v56, v56, v40\n"
  "v_mfma_f32_16x16x32_bf16 v[180:183], a[240:243], v[92:95], v[180:183]\n"
  "s_mov_b32 exec_hi, 0\n"
  "buffer_load_dwordx4 v8, s[44:47], s31 offen offset:3072 lds\n"
  "s_mov_b32 exec_hi, -1\n"
  "v_cvt_pk_bf16_f32 v97, v34, v35\n"
  "s_waitcnt lgkmcnt(6)\n"
  "v_mfma_f32_16x16x32_bf16 v[184:187], a[244:247], v[88:91], v[184:187]\n"
  "ds_read_b64_tr_b16 a[198:199], v16 offset:18464\n"
  "ds_read_b64_tr_b16 a[200:201], v15 offset:16704\n"
  "v_cvt_pk_bf16_f32 v98, v36, v37\n"
  "v_mfma_f32_16x16x32_bf16 v[188:191], a[244:247], v[92:95], v[188:191]\n"
  "ds_read_b64_tr_b16 a[202:203], v15 offset:18496\n"
  "v_cvt_pk_bf16_f32 v99, v38, v39\n"
  "s_waitcnt lgkmcnt(6)\n"
  "v_mfma_f32_16x16x32_bf16 v[192:195], a[248:251], v[88:91], v[192:195]\n"
  "ds_read_b64_tr_b16 a[204:205], v16 offset:16736\n"
  "v_mul_f32 v57, v57, v41\n"
  "v_mul_f32 v58, v58, v42\n"
  "v_mfma_f32_16x16x32_bf16 v[196:199], a[248:251], v[92:95], v[196:199]\n"
  "ds_read_b64_tr_b16 a[206:207], v16 offset:18528\n"
  "v_mul_f32 v59, v59, v43\n"
  "v_mfma_f32_16x16x32_bf16 v[200:203], a[252:255], v[88:91], v[200:203]\n"
  "ds_read_b64_tr_b16 a[208:209], v15 offset:16768\n"
  "v_cvt_pk_bf16_f32 v56, v56, v57\n"
  "v_cvt_pk_bf16_f32 v57, v58, v59\n"
  "v_mfma_f32_16x16x32_bf16 v[204:207], a[252:255], v[92:95], v[204:207]\n"
  "ds_read_b64_tr_b16 a[210:211], v15 offset:18560\n"
  "v_cvt_pk_bf16_f32 v58, v60, v61\n"
  "s_waitcnt lgkmcnt(8)\n"
  "v_mfma_f32_16x16x32_bf16 v[208:211], a[192:195], v[88:91], v[208:211]\n"
  "ds_read_b64_tr_b16 a[212:213], v16 offset:16800\n"
  "v_cvt_pk_bf16_f32 v59, v62, v63\n"
  "v_mfma_f32_16x16x32_bf16 v[212:215], a[192:195], v[92:95], v[212:215]\n"
  "s_waitcnt vmcnt(19)\n"
  "s_barrier\n"
  "ds_read_b64_tr_b16 a[214:215], v16 offset:18592\n"
  "s_add_u32 m0, s34, 4096\n"
  "s_mov_b32 s30, 128\n"
  "v_mfma_f32_16x16x32_bf16 v[32:35], a[186:189], v[100:103], v[28:31]\n"
  "ds_read_b64_tr_b16 a[216:217], v13 offset:0\n"
  "buffer_load_dwordx4 v3, s[52:55], s30 offen offset:0 nt lds\n"
  "v_mfma_f32_16x16x32_bf16 v[36:39], a[184:187], v[100:103], v[28:31]\n"
  "ds_read_b64_tr_b16 a[218:219], v13 offset:1792\n"
  "s_add_u32 s29, s71, 131072\n"
  "buffer_store_dwordx4 v[56:59], v17, s[60:63], s29 offen sc1\n"
  "s_waitcnt lgkmcnt(8)\n"
  "v_mfma_f32_16x16x32_bf16 v[216:219], a[196:199], v[88:91], v[216:219]\n"
  "ds_read_b64_tr_b16 a[220:221], v14 offset:32\n"
  "v_accvgpr_read_b32 v60, a156\n"
  "v_accvgpr_read_b32 v61, a157\n"
  "v_mfma_f32_16x16x32_bf16 v[220:223], a[196:199], v[92:95], v[220:223]\n"
  "ds_read_b64_tr_b16 a[222:223], v14 offset:1824\n"
  "v_accvgpr_read_b32 v62, a158\n"
  "v_mfma_f32_16x16x32_bf16 v[224:227], a[200:203], v[88:91], v[224:227]\n"
  "ds_read_b64_tr_b16 a[224:225], v13 offset:64\n"
  "v_cvt_pk_bf16_f32 v100, v32, v33\n"
  "v_accvgpr_read_b32 v63, a159\n"
  "v_mfma_f32_16x16x32_bf16 v[228:231], a[200:203], v[92:95], v[228:231]\n"
  "ds_read_b64_tr_b16 a[226:227], v13 offset:1856\n"
  "v_cvt_pk_bf16_f32 v101, v34, v35\n"
  "s_waitcnt lgkmcnt(8)\n"
  "v_mfma_f32_16x16x32_bf16 v[232:235], a[204:207], v[88:91], v[232:235]\n"
  "v_cvt_pk_bf16_f32 v102, v36, v37\n"
  "s_mov_b32 s30, 64640\n"
  "v_accvgpr_read_b32 v56, a152\n"
  "v_accvgpr_read_b32 v57, a153\n"
  "v_mfma_f32_16x16x32_bf16 v[236:239], a[204:207], v[92:95], v[236:239]\n"
  "buffer_load_dwordx4 v3, s[52:55], s30 offen offset:1024 nt lds\n"
  "v_cvt_pk_bf16_f32 v103, v38, v39\n"
  "v_mfma_f32_16x16x32_bf16 v[240:243], a[208:211], v[88:91], v[240:243]\n"
  "ds_read_b64_tr_b16 a[228:229], v14 offset:96\n"
  "v_accvgpr_read_b32 v58, a154\n"
  "v_accvgpr_read_b32 v59, a155\n"
  "v_mfma_f32_16x16x32_bf16 v[244:247], a[208:211], v[92:95], v[244:247]\n"
  "ds_read_b64_tr_b16 a[230:231], v14 offset:1888\n"
  "v_fma_f32 v60, v52, v4, v60\n"
  "s_waitcnt lgkmcnt(6)\n"
  "v_mfma_f32_16x16x32_bf16 v[248:251], a[212:215], v[88:91], v[248:251]\n"
  "ds_read_b64_tr_b16 a[232:233], v13 offset:128\n"
  "v_fma_f32 v61, v53, v4, v61\n"
  "v_mfma_f32_16x16x32_bf16 v[252:255], a[212:215], v[92:95], v[252:255]\n"
  "ds_read_b64_tr_b16 a[234:235], v13 offset:1920\n"
  "v_fma_f32 v62, v54, v4, v62\n"
  "v_mfma_f32_16x16x32_bf16 v[144:147], a[216:219], v[96:99], v[144:147]\n"
  "ds_read_b64_tr_b16 a[236:237], v14 offset:160\n"
  "v_fma_f32 v63, v55, v4, v63\n"
  "v_fma_f32 v56, v48, v4, v56\n"
  "v_mfma_f32_16x16x32_bf16 v[148:151], a[216:219], v[100:103], v[148:151]\n"
  "ds_read_b64_tr_b16 a[238:239], v14 offset:1952\n"
  "v_fma_f32 v57, v49, v4, v57\n"
  "s_waitcnt lgkmcnt(6)\n"
  "v_mfma_f32_16x16x32_bf16 v[152:155], a[220:223], v[96:99], v[152:155]\n"
  "ds_read_b64_tr_b16 a[240:241], v13 offset:192\n"
  "s_mov_b32 s30, 14464\n"
  "v_fma_f32 v58, v50, v4, v58\n"
  "v_fma_f32 v59, v51, v4, v59\n"
  "v_mfma_f32_16x16x32_bf16 v[156:159], a[220:223], v[100:103], v[156:159]\n"
  "buffer_load_dwordx4 v3, s[52:55], s30 offen offset:2048 nt lds\n"
  "v_mfma_f32_16x16x32_bf16 v[160:163], a[224:227], v[96:99], v[160:163]\n"
  "ds_read_b64_tr_b16 a[242:243], v13 offset:1984\n"
  "ds_read_b64_tr_b16 a[244:245], v14 offset:224\n"
  "v_mul_f32 v60, v60, v44\n"
  "v_mfma_f32_16x16x32_bf16 v[164:167], a[224:227], v[100:103], v[164:167]\n"
  "ds_read_b64_tr_b16 a[246:247], v14 offset:2016\n"
  "v_mul_f32 v61, v61, v45\n"
  "v_mul_f32 v62, v62, v46\n"
  "v_mfma_f32_16x16x32_bf16 v[32:35], a[186:189], v[104:107], v[24:27]\n"
  "ds_read_b64_tr_b16 a[248:249], v13 offset:256\n"
  "v_mul_f32 v63, v63, v47\n"
  "v_mfma_f32_16x16x32_bf16 v[36:39], a[184:187], v[104:107], v[24:27]\n"
  "ds_read_b64_tr_b16 a[250:251], v13 offset:2048\n"
  "v_mul_f32 v56, v56, v40\n"
  "s_waitcnt lgkmcnt(8)\n"
  "v_mfma_f32_16x16x32_bf16 v[168:171], a[228:231], v[96:99], v[168:171]\n"
  "v_mul_f32 v57, v57, v41\n"
  "v_mul_f32 v58, v58, v42\n"
  "v_mul_f32 v59, v59, v43\n"
  "v_mfma_f32_16x16x32_bf16 v[172:175], a[228:231], v[100:103], v[172:175]\n"
  "v_cvt_pk_bf16_f32 v56, v56, v57\n"
  "v_cvt_pk_bf16_f32 v57, v58, v59\n"
  "v_mfma_f32_16x16x32_bf16 v[176:179], a[232:235], v[96:99], v[176:179]\n"
  "ds_read_b64_tr_b16 a[252:253], v14 offset:288\n"
  "v_cvt_pk_bf16_f32 v104, v32, v33\n"
  "s_mov_b32 s30, 78976\n"
  "v_cvt_pk_bf16_f32 v58, v60, v61\n"
  "v_mfma_f32_16x16x32_bf16 v[180:183], a[232:235], v[100:103], v[180:183]\n"
  "buffer_load_dwordx4 v3, s[52:55], s30 offen offset:3072 nt lds\n"
  "v_cvt_pk_bf16_f32 v105, v34, v35\n"
  "s_waitcnt lgkmcnt(6)\n"
  "v_mfma_f32_16x16x32_bf16 v[184:187], a[236:239], v[96:99], v[184:187]\n"
  "ds_read_b64_tr_b16 a[254:255], v14 offset:2080\n"
  "ds_read_b64_tr_b16 a[192:193], v13 offset:320\n"
  "v_cvt_pk_bf16_f32 v106, v36, v37\n"
  "v_mfma_f32_16x16x32_bf16 v[188:191], a[236:239], v[100:103], v[188:191]\n"
  "ds_read_b64_tr_b16 a[194:195], v13 offset:2112\n"
  "Lto2%=:\n"
  "s_cbranch_execz Ltor0%=\n"
  "v_cvt_pk_bf16_f32 v107, v38, v39\n"
  "s_waitcnt lgkmcnt(6)\n"
  "v_mfma_f32_16x16x32_bf16 v[192:195], a[240:243], v[96:99], v[192:195]\n"
  "ds_read_b64_tr_b16 a[196:197], v14 offset:352\n"
  "v_cvt_pk_bf16_f32 v59, v62, v63\n"
  "v_mfma_f32_16x16x32_bf16 v[196:199], a[240:243], v[100:103], v[196:199]\n"
  "ds_read_b64_tr_b16 a[198:199], v14 offset:2144\n"
  "s_add_u32 s29, s71, 196608\n"
  "buffer_store_dwordx4 v[56:59], v17, s[60:63], s29 offen sc1\n"
  "v_mfma_f32_16x16x32_bf16 v[200:203], a[244:247], v[96:99], v[200:203]\n"
  "ds_read_b64_tr_b16 a[200:201], v13 offset:384\n"
  "v_mfma_f32_16x16x32_bf16 v[204:207], a[244:247], v[100:103], v[204:207]\n"
  "ds_read_b64_tr_b16 a[202:203], v13 offset:2176\n"
  "s_waitcnt lgkmcnt(8)\n"
  "v_mfma_f32_16x16x32_bf16 v[208:211], a[248:251], v[96:99], v[208:211]\n"
  "ds_read_b64_tr_b16 a[204:205], v14 offset:416\n"
  "v_mfma_f32_16x16x32_bf16 v[212:215], a[248:251], v[100:103], v[212:215]\n"
  "s_waitcnt vmcnt(21)\n"
  "s_barrier\n"
  "ds_read_b64_tr_b16 a[206:207], v14 offset:2208\n"
  "s_add_u32 m0, s35, 65536\n"
  "s_mov_b32 s31, 1376256\n"
  "v_mfma_f32_16x16x32_bf16 v[32:35], a[186:189], v[108:111], v[28:31]\n"
  "buffer_load_dwordx4 v5, s[44:47], s31 offen offset:0 lds\n"
  "v_mfma_f32_16x16x32_bf16 v[36:39], a[184:187], v[108:111], v[28:31]\n"
  "ds_read_b64_tr_b16 a[208:209], v13 offset:16384\n"
  "ds_read_b64_tr_b16 a[210:211], v13 offset:18176\n"
  "s_waitcnt vmcnt(13)\n"
  "ds_read_b128 a[32:35], v9 offset:0\n"
  "s_waitcnt lgkmcnt(9)\n"
  "v_mfma_f32_16x16x32_bf16 v[216:219], a[252:255], v[96:99], v[216:219]\n"
  "ds_read_b64_tr_b16 a[212:213], v14 offset:16416\n"
  "v_mfma_f32_16x16x32_bf16 v[220:223], a[252:255], v[100:103], v[220:223]\n"
  "buffer_load_dwordx4 v6, s[44:47], s31 offen offset:1024 lds\n"
  "v_mfma_f32_16x16x32_bf16 v[224:227], a[192:195], v[96:99], v[224:227]\n"
  "ds_read_b64_tr_b16 a[214:215], v14 offset:18208\n"
  "ds_read_b128 a[36:39], v9 offset:2048\n"
  "ds_read_b64_tr_b16 a[216:217], v13 offset:16448\n"
  "v_cvt_pk_bf16_f32 v108, v32, v33\n"
  "v_mfma_f32_16x16x32_bf16 v[228:231], a[192:195], v[100:103], v[228:231]\n"
  "ds_read_b64_tr_b16 a[218:219], v13 offset:18240\n"
  "ds_read_b128 a[40:43], v10 offset:0\n"
  "v_cvt_pk_bf16_f32 v109, v34, v35\n"
  "s_waitcnt lgkmcnt(11)\n"
  "v_mfma_f32_16x16x32_bf16 v[232:235], a[196:199], v[96:99], v[232:235]\n"
  "v_cvt_pk_bf16_f32 v110, v36, v37\n"
  "v_mfma_f32_16x16x32_bf16 v[236:239], a[196:199], v[100:103], v[236:239]\n"
  "buffer_load_dwordx4 v7, s[44:47], s31 offen offset:2048 lds\n"
  "v_cvt_pk_bf16_f32 v111, v38, v39\n"
  "v_mfma_f32_16x16x32_bf16 v[240:243], a[200:203], v[96:99], v[240:243]\n"
  "ds_read_b128 a[44:47], v10 offset:2048\n"
  "ds_read_b64_tr_b16 a[220:221], v14 offset:16480\n"
  "v_mfma_f32_16x16x32_bf16 v[244:247], a[200:203], v[100:103], v[244:247]\n"
  "ds_read_b64_tr_b16 a[222:223], v14 offset:18272\n"
  "s_waitcnt lgkmcnt(9)\n"
  "v_mfma_f32_16x16x32_bf16 v[248:251], a[204:207], v[96:99], v[248:251]\n"
  "ds_read_b64_tr_b16 a[224:225], v13 offset:16512\n"
  "v_mfma_f32_16x16x32_bf16 v[252:255], a[204:207], v[100:103], v[252:255]\n"
  "s_mov_b32 exec_hi, 0\n"
  "buffer_load_dwordx4 v8, s[44:47], s31 offen offset:3072 lds\n"
  "s_mov_b32 exec_hi, -1\n"
  "v_mfma_f32_16x16x32_bf16 v[144:147], a[208:211], v[104:107], v[144:147]\n"
  "ds_read_b64_tr_b16 a[226:227], v13 offset:18304\n"
  "ds_read_b64_tr_b16 a[228:229], v14 offset:16544\n"
  "v_mfma_f32_16x16x32_bf16 v[148:151], a[208:211], v[108:111], v[148:151]\n"
  "ds_read_b64_tr_b16 a[230:231], v14 offset:18336\n"
  "s_waitcnt lgkmcnt(7)\n"
  "v_mfma_f32_16x16x32_bf16 v[152:155], a[212:215], v[104:107], v[152:155]\n"
  "ds_read_b64_tr_b16 a[232:233], v13 offset:16576\n"
  "s_add_u32 m0, s35, 81920\n"
  "s_mov_b32 s31, 1490944\n"
  "v_mfma_f32_16x16x32_bf16 v[156:159], a[212:215], v[108:111], v[156:159]\n"
  "buffer_load_dwordx4 v5, s[44:47], s31 offen offset:0 lds\n"
  "v_mfma_f32_16x16x32_bf16 v[160:163], a[216:219], v[104:107], v[160:163]\n"
  "ds_read_b64_tr_b16 a[234:235], v13 offset:18368\n"
  "ds_read_b64_tr_b16 a[236:237], v14 offset:16608\n"
  "v_mfma_f32_16x16x32_bf16 v[164:167], a[216:219], v[108:111], v[164:167]\n"
  "ds_read_b64_tr_b16 a[238:239], v14 offset:18400\n"
  "v_mfma_f32_16x16x32_bf16 v[32:35], a[186:189], v[112:115], v[24:27]\n"
  "ds_read_b64_tr_b16 a[240:241], v13 offset:16640\n"
  "v_mfma_f32_16x16x32_bf16 v[36:39], a[184:187], v[112:115], v[24:27]\n"
  "buffer_load_dwordx4 v6, s[44:47], s31 offen offset:1024 lds\n"
  "s_waitcnt lgkmcnt(8)\n"
  "v_mfma_f32_16x16x32_bf16 v[168:171], a[220:223], v[104:107], v[168:171]\n"
  "ds_read_b64_tr_b16 a[242:243], v13 offset:18432\n"
  "v_mfma_f32_16x16x32_bf16 v[172:175], a[220:223], v[108:111], v[172:175]\n"
  "s_waitcnt lgkmcnt(6)\n"
  "v_mfma_f32_16x16x32_bf16 v[176:179], a[224:227], v[104:107], v[176:179]\n"
  "ds_read_b64_tr_b16 a[244:245], v14 offset:16672\n"
  "v_cvt_pk_bf16_f32 v112, v32, v33\n"
  "v_mfma_f32_16x16x32_bf16 v[180:183], a[224:227], v[108:111], v[180:183]\n"
  "buffer_load_dwordx4 v7, s[44:47], s31 offen offset:2048 lds\n"
  "v_cvt_pk_bf16_f32 v113, v34, v35\n"
  "v_mfma_f32_16x16x32_bf16 v[184:187], a[228:231], v[104:107], v[184:187]\n"
  "ds_read_b64_tr_b16 a[246:247], v14 offset:18464\n"
  "ds_read_b64_tr_b16 a[248:249], v13 offset:16704\n"
  "v_cvt_pk_bf16_f32 v114, v36, v37\n"
  "v_mfma_f32_16x16x32_bf16 v[188:191], a[228:231], v[108:111], v[188:191]\n"
  "ds_read_b64_tr_b16 a[250:251], v13 offset:18496\n"
  "v_cvt_pk_bf16_f32 v115, v38, v39\n"
  "s_waitcnt lgkmcnt(6)\n"
  "v_mfma_f32_16x16x32_bf16 v[192:195], a[232:235], v[104:107], v[192:195]\n"
  "ds_read_b64_tr_b16 a[252:253], v14 offset:16736\n"
  "v_mfma_f32_16x16x32_bf16 v[196:199], a[232:235], v[108:111], v[196:199]\n"
  "s_mov_b32 exec_hi, 0\n"
  "buffer_load_dwordx4 v8, s[44:47], s31 offen offset:3072 lds\n"
  "s_mov_b32 exec_hi, -1\n"
  "v_mfma_f32_16x16x32_bf16 v[200:203], a[236:239], v[104:107], v[200:203]\n"
  "ds_read_b64_tr_b16 a[254:255], v14 offset:18528\n"
  "ds_read_b64_tr_b16 a[192:193], v13 offset:16768\n"
  "v_mfma_f32_16x16x32_bf16 v[204:207], a[236:239], v[108:111], v[204:207]\n"
  "ds_read_b64_tr_b16 a[194:195], v13 offset:18560\n"
  "s_waitcnt lgkmcnt(8)\n"
  "v_mfma_f32_16x16x32_bf16 v[208:211], a[240:243], v[104:107], v[208:211]\n"
  "ds_read_b64_tr_b16 a[196:197], v14 offset:16800\n"
  "v_mfma_f32_16x16x32_bf16 v[212:215], a[240:243], v[108:111], v[212:215]\n"
  "s_barrier\n"
  "ds_read_b64_tr_b16 a[198:199], v14 offset:18592\n"
  "s_add_u32 m0, s35, 0\n"
  "s_mov_b32 s31, 0\n"
  "v_mfma_f32_16x16x32_bf16 v[32:35], a[186:189], v[116:119], v[28:31]\n"
  "v_accvgpr_read_b32 v5, a176\n"
  "v_accvgpr_read_b32 v6, a177\n"
  "v_accvgpr_read_b32 v7, a178\n"
  "v_accvgpr_read_b32 v8, a179\n"
  "buffer_load_dwordx4 v5, s[48:51], s31 offen offset:0 lds\n"
  "v_mfma_f32_16x16x32_bf16 v[36:39], a[184:187], v[116:119], v[28:31]\n"
  "ds_read_b64_tr_b16 a[200:201], v13 offset:32768\n"
  "ds_read_b64_tr_b16 a[202:203], v13 offset:34560\n"
  "s_waitcnt lgkmcnt(8)\n"
  "v_mfma_f32_16x16x32_bf16 v[216:219], a[244:247], v[104:107], v[216:219]\n"
  "ds_read_b64_tr_b16 a[204:205], v14 offset:32800\n"
  "v_mfma_f32_16x16x32_bf16 v[220:223], a[244:247], v[108:111], v[220:223]\n"
  "ds_read_b64_tr_b16 a[206:207], v14 offset:34592\n"
  "v_mfma_f32_16x16x32_bf16 v[224:227], a[248:251], v[104:107], v[224:227]\n"
  "buffer_load_dwordx4 v6, s[48:51], s31 offen offset:1024 lds\n"
  "v_cvt_pk_bf16_f32 v116, v32, v33\n"
  "v_mfma_f32_16x16x32_bf16 v[228:231], a[248:251], v[108:111], v[228:231]\n"
  "ds_read_b64_tr_b16 a[208:209], v13 offset:32832\n"
  "ds_read_b64_tr_b16 a[210:211], v13 offset:34624\n"
  "v_cvt_pk_bf16_f32 v117, v34, v35\n"
  "s_waitcnt lgkmcnt(8)\n"
  "v_mfma_f32_16x16x32_bf16 v[232:235], a[252:255], v[104:107], v[232:235]\n"
  "v_cvt_pk_bf16_f32 v118, v36, v37\n"
  "v_mfma_f32_16x16x32_bf16 v[236:239], a[252:255], v[108:111], v[236:239]\n"
  "v_cvt_pk_bf16_f32 v119, v38, v39\n"
  "v_mfma_f32_16x16x32_bf16 v[240:243], a[192:195], v[104:107], v[240:243]\n"
  "ds_read_b64_tr_b16 a[212:213], v14 offset:32864\n"
  "v_mfma_f32_16x16x32_bf16 v[244:247], a[192:195], v[108:111], v[244:247]\n"
  "buffer_load_dwordx4 v7, s[48:51], s31 offen offset:2048 lds\n"
  "s_waitcnt lgkmcnt(5)\n"
  "v_mfma_f32_16x16x32_bf16 v[248:251], a[196:199], v[104:107], v[248:251]\n"
  "ds_read_b64_tr_b16 a[214:215], v14 offset:34656\n"
  "ds_read_b64_tr_b16 a[216:217], v13 offset:32896\n"
  "v_mfma_f32_16x16x32_bf16 v[252:255], a[196:199], v[108:111], v[252:255]\n"
  "ds_read_b64_tr_b16 a[218:219], v13 offset:34688\n"
  "v_mfma_f32_16x16x32_bf16 v[144:147], a[200:203], v[112:115], v[144:147]\n"
  "ds_read_b64_tr_b16 a[220:221], v14 offset:32928\n"
  "s_waitcnt vmcnt(11)\n"
  "s_mov_b32 s29, s72\n"
  "s_mov_b64 exec, 1\n"
  "buffer_store_dword v20, off, s[60:63], s29 sc1\n"
  "s_mov_b64 exec, -1\n"
  "v_mfma_f32_16x16x32_bf16 v[148:151], a[200:203], v[116:119], v[148:151]\n"
  "ds_read_b64_tr_b16 a[222:223], v14 offset:34720\n"
  "s_waitcnt lgkmcnt(6)\n"
  "v_mfma_f32_16x16x32_bf16 v[152:155], a[204:207], v[112:115], v[152:155]\n"
  "s_mov_b32 exec_hi, 0\n"
  "buffer_load_dwordx4 v8, s[48:51], s31 offen offset:3072 lds\n"
  "s_mov_b32 exec_hi, -1\n"
  "v_mfma_f32_16x16x32_bf16 v[156:159], a[204:207], v[116:119], v[156:159]\n"
  "ds_read_b64_tr_b16 a[224:225], v13 offset:32960\n"
  "ds_read_b64_tr_b16 a[226:227], v13 offset:34752\n"
  "v_mfma_f32_16x16x32_bf16 v[160:163], a[208:211], v[112:115], v[160:163]\n"
  "ds_read_b64_tr_b16 a[228:229], v14 offset:32992\n"
  "v_mfma_f32_16x16x32_bf16 v[164:167], a[208:211], v[116:119], v[164:167]\n"
  "ds_read_b64_tr_b16 a[230:231], v14 offset:34784\n"
  "v_mfma_f32_16x16x32_bf16 v[32:35], a[186:189], v[120:123], v[24:27]\n"
  "ds_read_b64_tr_b16 a[232:233], v13 offset:33024\n"
  "s_add_u32 m0, s34, 8192\n"
  "s_mov_b32 s30, 0\n"
  "v_mfma_f32_16x16x32_bf16 v[36:39], a[184:187], v[120:123], v[24:27]\n"
  "ds_read_b64_tr_b16 a[234:235], v13 offset:34816\n"
  "v_xor_b32 v3, 0x300, v3\n"
  "buffer_load_dwordx4 v3, s[52:55], s30 offen offset:0 nt lds\n"
  "s_waitcnt lgkmcnt(8)\n"
  "v_mfma_f32_16x16x32_bf16 v[168:171], a[212:215], v[112:115], v[168:171]\n"
  "v_mfma_f32_16x16x32_bf16 v[172:175], a[212:215], v[116:119], v[172:175]\n"
  "s_mov_b32 s30, 64512\n"
  "v_mfma_f32_16x16x32_bf16 v[176:179], a[216:219], v[112:115], v[176:179]\n"
  "buffer_load_dwordx4 v3, s[52:55], s30 offen offset:1024 nt lds\n"
  "v_cvt_pk_bf16_f32 v120, v32, v33\n"
  "v_mfma_f32_16x16x32_bf16 v[180:183], a[216:219], v[116:119], v[180:183]\n"
  "ds_read_b64_tr_b16 a[236:237], v14 offset:33056\n"
  "ds_read_b64_tr_b16 a[238:239], v14 offset:34848\n"
  "v_cvt_pk_bf16_f32 v121, v34, v35\n"
  "s_waitcnt lgkmcnt(6)\n"
  "v_mfma_f32_16x16x32_bf16 v[184:187], a[220:223], v[112:115], v[184:187]\n"
  "ds_read_b64_tr_b16 a[240:241], v13 offset:33088\n"
  "v_cvt_pk_bf16_f32 v122, v36, v37\n"
  "s_mov_b32 s30, 14336\n"
  "v_mfma_f32_16x16x32_bf16 v[188:191], a[220:223], v[116:119], v[188:191]\n"
  "buffer_load_dwordx4 v3, s[52:55], s30 offen offset:2048 nt lds\n"
  "v_cvt_pk_bf16_f32 v123, v38, v39\n"
  "v_mfma_f32_16x16x32_bf16 v[192:195], a[224:227], v[112:115], v[192:195]\n"
  "ds_read_b64_tr_b16 a[242:243], v13 offset:34880\n"
  "ds_read_b64_tr_b16 a[244:245], v14 offset:33120\n"
  "v_mfma_f32_16x16x32_bf16 v[196:199], a[224:227], v[116:119], v[196:199]\n"
  "ds_read_b64_tr_b16 a[246:247], v14 offset:34912\n"
  "s_mov_b32 s30, 78848\n"
  "s_waitcnt lgkmcnt(6)\n"
  "v_mfma_f32_16x16x32_bf16 v[200:203], a[228:231], v[112:115], v[200:203]\n"
  "buffer_load_dwordx4 v3, s[52:55], s30 offen offset:3072 nt lds\n"
  "v_mfma_f32_16x16x32_bf16 v[204:207], a[228:231], v[116:119], v[204:207]\n"
  "ds_read_b64_tr_b16 a[248:249], v13 offset:33152\n"
  "ds_read_b64_tr_b16 a[250:251], v13 offset:34944\n"
  "v_mfma_f32_16x16x32_bf16 v[208:211], a[232:235], v[112:115], v[208:211]\n"
  "ds_read_b64_tr_b16 a[252:253], v14 offset:33184\n"
  "v_mfma_f32_16x16x32_bf16 v[212:215], a[232:235], v[116:119], v[212:215]\n"
  "s_barrier\n"
  "ds_read_b64_tr_b16 a[254:255], v14 offset:34976\n"
  "s_add_u32 m0, s34, 12288\n"
  "s_mov_b32 s30, 128\n"
  "v_mfma_f32_16x16x32_bf16 v[32:35], a[186:189], v[124:127], v[28:31]\n"
  "ds_read_b64_tr_b16 a[192:193], v13 offset:49152\n"
  "buffer_load_dwordx4 v3, s[52:55], s30 offen offset:0 nt lds\n"
  "v_mfma_f32_16x16x32_bf16 v[36:39], a[184:187], v[124:127], v[28:31]\n"
  "ds_read_b64_tr_b16 a[194:195], v13 offset:50944\n"
  "ds_read_b128 a[48:51], v9 offset:4096\n"
  "s_waitcnt lgkmcnt(10)\n"
  "v_mfma_f32_16x16x32_bf16 v[216:219], a[236:239], v[112:115], v[216:219]\n"
  "ds_read_b64_tr_b16 a[196:197], v14 offset:49184\n"
  "v_mfma_f32_16x16x32_bf16 v[220:223], a[236:239], v[116:119], v[220:223]\n"
  "ds_read_b64_tr_b16 a[198:199], v14 offset:50976\n"
  "ds_read_b128 a[52:55], v9 offset:6144\n"
  "s_waitcnt lgkmcnt(10)\n"
  "v_mfma_f32_16x16x32_bf16 v[224:227], a[240:243], v[112:115], v[224:227]\n"
  "ds_read_b64_tr_b16 a[200:201], v13 offset:49216\n"
  "v_cvt_pk_bf16_f32 v124, v32, v33\n"
  "v_mfma_f32_16x16x32_bf16 v[228:231], a[240:243], v[116:119], v[228:231]\n"
  "ds_read_b64_tr_b16 a[202:203], v13 offset:51008\n"
  "ds_read_b128 a[56:59], v10 offset:4096\n"
  "v_cvt_pk_bf16_f32 v125, v34, v35\n"
  "v_mfma_f32_16x16x32_bf16 v[232:235], a[244:247], v[112:115], v[232:235]\n"
  "v_cvt_pk_bf16_f32 v126, v36, v37\n"
  "s_mov_b32 s30, 64640\n"
  "v_mfma_f32_16x16x32_bf16 v[236:239], a[244:247], v[116:119], v[236:239]\n"
  "buffer_load_dwordx4 v3, s[52:55], s30 offen offset:1024 nt lds\n"
  "v_cvt_pk_bf16_f32 v127, v38, v39\n"
  "s_waitcnt lgkmcnt(9)\n"
  "v_mfma_f32_16x16x32_bf16 v[240:243], a[248:251], v[112:115], v[240:243]\n"
  "ds_read_b128 a[60:63], v10 offset:6144\n"
  "ds_read_b64_tr_b16 a[204:205], v14 offset:49248\n"
  "v_mfma_f32_16x16x32_bf16 v[244:247], a[248:251], v[116:119], v[244:247]\n"
  "ds_read_b64_tr_b16 a[206:207], v14 offset:51040\n"
  "v_mfma_f32_16x16x32_bf16 v[248:251], a[252:255], v[112:115], v[248:251]\n"
  "ds_read_b64_tr_b16 a[208:209], v13 offset:49280\n"
  "v_mfma_f32_16x16x32_bf16 v[252:255], a[252:255], v[116:119], v[252:255]\n"
  "ds_read_b64_tr_b16 a[210:211], v13 offset:51072\n"
  "s_waitcnt lgkmcnt(8)\n"
  "v_mfma_f32_16x16x32_bf16 v[144:147], a[192:195], v[120:123], v[144:147]\n"
  "ds_read_b64_tr_b16 a[212:213], v14 offset:49312\n"
  "v_mfma_f32_16x16x32_bf16 v[148:151], a[192:195], v[124:127], v[148:151]\n"
  "ds_read_b64_tr_b16 a[214:215], v14 offset:51104\n"
  "v_mfma_f32_16x16x32_bf16 v[152:155], a[196:199], v[120:123], v[152:155]\n"
  "ds_read_b64_tr_b16 a[216:217], v13 offset:49344\n"
  "s_mov_b32 s30, 14464\n"
  "v_mfma_f32_16x16x32_bf16 v[156:159], a[196:199], v[124:127], v[156:159]\n"
  "buffer_load_dwordx4 v3, s[52:55], s30 offen offset:2048 nt lds\n"
  "s_waitcnt lgkmcnt(8)\n"
  "v_mfma_f32_16x16x32_bf16 v[160:163], a[200:203], v[120:123], v[160:163]\n"
  "ds_read_b64_tr_b16 a[218:219], v13 offset:51136\n"
  "ds_read_b64_tr_b16 a[220:221], v14 offset:49376\n"
  "v_mfma_f32_16x16x32_bf16 v[164:167], a[200:203], v[124:127], v[164:167]\n"
  "ds_read_b64_tr_b16 a[222:223], v14 offset:51168\n"
  "v_mfma_f32_16x16x32_bf16 v[32:35], a[186:189], v[128:131], v[24:27]\n"
  "ds_read_b64_tr_b16 a[224:225], v13 offset:49408\n"
  "v_mfma_f32_16x16x32_bf16 v[36:39], a[184:187], v[128:131], v[24:27]\n"
  "ds_read_b64_tr_b16 a[226:227], v13 offset:51200\n"
  "s_waitcnt lgkmcnt(8)\n"
  "v_mfma_f32_16x16x32_bf16 v[168:171], a[204:207], v[120:123], v[168:171]\n"
  "v_mfma_f32_16x16x32_bf16 v[172:175], a[204:207], v[124:127], v[172:175]\n"
  "v_mfma_f32_16x16x32_bf16 v[176:179], a[208:211], v[120:123], v[176:179]\n"
  "ds_read_b64_tr_b16 a[228:229], v14 offset:49440\n"
  "v_cvt_pk_bf16_f32 v128, v32, v33\n"
  "s_mov_b32 s30, 78976\n"
  "v_mfma_f32_16x16x32_bf16 v[180:183], a[208:211], v[124:127], v[180:183]\n"
  "buffer_load_dwordx4 v3, s[52:55], s30 offen offset:3072 nt lds\n"
  "v_cvt_pk_bf16_f32 v129, v34, v35\n"
  "s_waitcnt lgkmcnt(6)\n"
  "v_mfma_f32_16x16x32_bf16 v[184:187], a[212:215], v[120:123], v[184:187]\n"
  "ds_read_b64_tr_b16 a[230:231], v14 offset:51232\n"
  "ds_read_b64_tr_b16 a[232:233], v13 offset:49472\n"
  "v_cvt_pk_bf16_f32 v130, v36, v37\n"
  "v_mfma_f32_16x16x32_bf16 v[188:191], a[212:215], v[124:127], v[188:191]\n"
  "ds_read_b64_tr_b16 a[234:235], v13 offset:51264\n"
  "v_cvt_pk_bf16_f32 v131, v38, v39\n"
  "s_waitcnt lgkmcnt(6)\n"
  "v_mfma_f32_16x16x32_bf16 v[192:195], a[216:219], v[120:123], v[192:195]\n"
  "ds_read_b64_tr_b16 a[236:237], v14 offset:49504\n"
  "v_mfma_f32_16x16x32_bf16 v[196:199], a[216:219], v[124:127], v[196:199]\n"
  "ds_read_b64_tr_b16 a[238:239], v14 offset:51296\n"
  "v_mfma_f32_16x16x32_bf16 v[200:203], a[220:223], v[120:123], v[200:203]\n"
  "ds_read_b64_tr_b16 a[240:241], v13 offset:49536\n"
  "v_mfma_f32_16x16x32_bf16 v[204:207], a[220:223], v[124:127], v[204:207]\n"
  "ds_read_b64_tr_b16 a[242:243], v13 offset:51328\n"
  "s_waitcnt lgkmcnt(8)\n"
  "v_mfma_f32_16x16x32_bf16 v[208:211], a[224:227], v[120:123], v[208:211]\n"
  "ds_read_b64_tr_b16 a[244:245], v14 offset:49568\n"
  "v_mfma_f32_16x16x32_bf16 v[212:215], a[224:227], v[124:127], v[212:215]\n"
  "s_waitcnt vmcnt(17)\n"
  "s_barrier\n"
  "ds_read_b64_tr_b16 a[246:247], v14 offset:51360\n"
  "v_mfma_f32_16x16x32_bf16 v[32:35], a[186:189], v[132:135], v[28:31]\n"
  "ds_read_b64_tr_b16 a[248:249], v15 offset:0\n"
  "v_mfma_f32_16x16x32_bf16 v[36:39], a[184:187], v[132:135], v[28:31]\n"
  "ds_read_b64_tr_b16 a[250:251], v15 offset:1792\n"
  "s_waitcnt lgkmcnt(8)\n"
  "v_mfma_f32_16x16x32_bf16 v[216:219], a[228:231], v[120:123], v[216:219]\n"
  "ds_read_b64_tr_b16 a[252:253], v16 offset:32\n"
  "v_mfma_f32_16x16x32_bf16 v[220:223], a[228:231], v[124:127], v[220:223]\n"
  "ds_read_b64_tr_b16 a[254:255], v16 offset:1824\n"
  "v_mfma_f32_16x16x32_bf16 v[224:227], a[232:235], v[120:123], v[224:227]\n"
  "ds_read_b64_tr_b16 a[192:193], v15 offset:64\n"
  "v_cvt_pk_bf16_f32 v132, v32, v33\n"
  "v_mfma_f32_16x16x32_bf16 v[228:231], a[232:235], v[124:127], v[228:231]\n"
  "ds_read_b64_tr_b16 a[194:195], v15 offset:1856\n"
  "v_cvt_pk_bf16_f32 v133, v34, v35\n"
  "s_waitcnt lgkmcnt(8)\n"
  "v_mfma_f32_16x16x32_bf16 v[232:235], a[236:239], v[120:123], v[232:235]\n"
  "v_cvt_pk_bf16_f32 v134, v36, v37\n"
  "v_mfma_f32_16x16x32_bf16 v[236:239], a[236:239], v[124:127], v[236:239]\n"
  "v_cvt_pk_bf16_f32 v135, v38, v39\n"
  "v_mfma_f32_16x16x32_bf16 v[240:243], a[240:243], v[120:123], v[240:243]\n"
  "ds_read_b64_tr_b16 a[196:197], v16 offset:96\n"
  "v_mfma_f32_16x16x32_bf16 v[244:247], a[240:243], v[124:127], v[244:247]\n"
  "ds_read_b64_tr_b16 a[198:199], v16 offset:1888\n"
  "s_waitcnt lgkmcnt(6)\n"
  "v_mfma_f32_16x16x32_bf16 v[248:251], a[244:247], v[120:123], v[248:251]\n"
  "ds_read_b64_tr_b16 a[200:201], v15 offset:128\n"
  "v_mfma_f32_16x16x32_bf16 v[252:255], a[244:247], v[124:127], v[252:255]\n"
  "ds_read_b64_tr_b16 a[202:203], v15 offset:1920\n"
  "v_mfma_f32_16x16x32_bf16 v[144:147], a[248:251], v[128:131], v[144:147]\n"
  "ds_read_b64_tr_b16 a[204:205], v16 offset:160\n"
  "v_mfma_f32_16x16x32_bf16 v[148:151], a[248:251], v[132:135], v[148:151]\n"
  "ds_read_b64_tr_b16 a[206:207], v16 offset:1952\n"
  "s_waitcnt lgkmcnt(6)\n"
  "v_mfma_f32_16x16x32_bf16 v[152:155], a[252:255], v[128:131], v[152:155]\n"
  "ds_read_b64_tr_b16 a[208:209], v15 offset:192\n"
  "v_mfma_f32_16x16x32_bf16 v[156:159], a[252:255], v[132:135], v[156:159]\n"
  "ds_read_b64_tr_b16 a[210:211], v15 offset:1984\n"
  "v_mfma_f32_16x16x32_bf16 v[160:163], a[192:195], v[128:131], v[160:163]\n"
  "ds_read_b64_tr_b16 a[212:213], v16 offset:224\n"
  "v_mfma_f32_16x16x32_bf16 v[164:167], a[192:195], v[132:135], v[164:167]\n"
  "ds_read_b64_tr_b16 a[214:215], v16 offset:2016\n"
  "v_mfma_f32_16x16x32_bf16 v[32:35], a[186:189], v[136:139], v[24:27]\n"
  "ds_read_b64_tr_b16 a[216:217], v15 offset:256\n"
  "v_mfma_f32_16x16x32_bf16 v[36:39], a[184:187], v[136:139], v[24:27]\n"
  "ds_read_b64_tr_b16 a[218:219], v15 offset:2048\n"
  "s_waitcnt lgkmcnt(8)\n"
  "v_mfma_f32_16x16x32_bf16 v[168:171], a[196:199], v[128:131], v[168:171]\n"
  "v_mfma_f32_16x16x32_bf16 v[172:175], a[196:199], v[132:135], v[172:175]\n"
  "v_mfma_f32_16x16x32_bf16 v[176:179], a[200:203], v[128:131], v[176:179]\n"
  "ds_read_b64_tr_b16 a[220:221], v16 offset:288\n"
  "v_cvt_pk_bf16_f32 v136, v32, v33\n"
  "v_mfma_f32_16x16x32_bf16 v[180:183], a[200:203], v[132:135], v[180:183]\n"
  "ds_read_b64_tr_b16 a[222:223], v16 offset:2080\n"
  "v_cvt_pk_bf16_f32 v137, v34, v35\n"
  "s_waitcnt lgkmcnt(6)\n"
  "v_mfma_f32_16x16x32_bf16 v[184:187], a[204:207], v[128:131], v[184:187]\n"
  "ds_read_b64_tr_b16 a[224:225], v15 offset:320\n"
  "v_cvt_pk_bf16_f32 v138, v36, v37\n"
  "v_mfma_f32_16x16x32_bf16 v[188:191], a[204:207], v[132:135], v[188:191]\n"
  "ds_read_b64_tr_b16 a[226:227], v15 offset:2112\n"
  "v_cvt_pk_bf16_f32 v139, v38, v39\n"
  "v_mfma_f32_16x16x32_bf16 v[192:195], a[208:211], v[128:131], v[192:195]\n"
  "ds_read_b64_tr_b16 a[228:229], v16 offset:352\n"
  "v_mfma_f32_16x16x32_bf16 v[196:199], a[208:211], v[132:135], v[196:199]\n"
  "ds_read_b64_tr_b16 a[230:231], v16 offset:2144\n"
  "s_waitcnt lgkmcnt(6)\n"
  "v_mfma_f32_16x16x32_bf16 v[200:203], a[212:215], v[128:131], v[200:203]\n"
  "ds_read_b64_tr_b16 a[232:233], v15 offset:384\n"
  "v_mfma_f32_16x16x32_bf16 v[204:207], a[212:215], v[132:135], v[204:207]\n"
  "ds_read_b64_tr_b16 a[234:235], v15 offset:2176\n"
  "v_mfma_f32_16x16x32_bf16 v[208:211], a[216:219], v[128:131], v[208:211]\n"
  "ds_read_b64_tr_b16 a[236:237], v16 offset:416\n"
  "v_mfma_f32_16x16x32_bf16 v[212:215], a[216:219], v[132:135], v[212:215]\n"
  "s_waitcnt vmcnt(13)\n"
  "s_barrier\n"
  "ds_read_b64_tr_b16 a[238:239], v16 offset:2208\n"
  "v_mfma_f32_16x16x32_bf16 v[32:35], a[186:189], v[140:143], v[28:31]\n"
  "ds_read_b64_tr_b16 a[240:241], v15 offset:16384\n"
  "v_mfma_f32_16x16x32_bf16 v[36:39], a[184:187], v[140:143], v[28:31]\n"
  "ds_read_b64_tr_b16 a[242:243], v15 offset:18176\n"
  "s_waitcnt lgkmcnt(8)\n"
  "v_mfma_f32_16x16x32_bf16 v[216:219], a[220:223], v[128:131], v[216:219]\n"
  "ds_read_b64_tr_b16 a[244:245], v16 offset:16416\n"
  "v_mfma_f32_16x16x32_bf16 v[220:223], a[220:223], v[132:135], v[220:223]\n"
  "ds_read_b64_tr_b16 a[246:247], v16 offset:18208\n"
  "v_mfma_f32_16x16x32_bf16 v[224:227], a[224:227], v[128:131], v[224:227]\n"
  "ds_read_b64_tr_b16 a[248:249], v15 offset:16448\n"
  "v_cvt_pk_bf16_f32 v140, v32, v33\n"
  "v_mfma_f32_16x16x32_bf16 v[228:231], a[224:227], v[132:135], v[228:231]\n"
  "ds_read_b64_tr_b16 a[250:251], v15 offset:18240\n"
  "v_cvt_pk_bf16_f32 v141, v34, v35\n"
  "s_waitcnt lgkmcnt(8)\n"
  "v_mfma_f32_16x16x32_bf16 v[232:235], a[228:231], v[128:131], v[232:235]\n"
  "ds_read_b64_tr_b16 a[252:253], v16 offset:16480\n"
  "v_cvt_pk_bf16_f32 v142, v36, v37\n"
  "v_mfma_f32_16x16x32_bf16 v[236:239], a[228:231], v[132:135], v[236:239]\n"
  "ds_read_b64_tr_b16 a[254:255], v16 offset:18272\n"
  "v_cvt_pk_bf16_f32 v143, v38, v39\n"
  "v_mfma_f32_16x16x32_bf16 v[240:243], a[232:235], v[128:131], v[240:243]\n"
  "ds_read_b64_tr_b16 a[192:193], v15 offset:16512\n"
  "v_mfma_f32_16x16x32_bf16 v[244:247], a[232:235], v[132:135], v[244:247]\n"
  "ds_read_b64_tr_b16 a[194:195], v15 offset:18304\n"
  "s_waitcnt lgkmcnt(8)\n"
  "v_mfma_f32_16x16x32_bf16 v[248:251], a[236:239], v[128:131], v[248:251]\n"
  "ds_read_b64_tr_b16 a[196:197], v16 offset:16544\n"
  "s_add_u32 m0, s35, 16384\n"
  "s_mov_b32 s31, 28672\n"
  "v_mfma_f32_16x16x32_bf16 v[252:255], a[236:239], v[132:135], v[252:255]\n"
  "buffer_load_dwordx4 v5, s[48:51], s31 offen offset:0 lds\n"
  "v_mfma_f32_16x16x32_bf16 v[144:147], a[240:243], v[136:139], v[144:147]\n"
  "ds_read_b64_tr_b16 a[198:199], v16 offset:18336\n"
  "ds_read_b64_tr_b16 a[200:201], v15 offset:16576\n"
  "v_mfma_f32_16x16x32_bf16 v[148:151], a[240:243], v[140:143], v[148:151]\n"
  "ds_read_b64_tr_b16 a[202:203], v15 offset:18368\n"
  "s_waitcnt lgkmcnt(8)\n"
  "v_mfma_f32_16x16x32_bf16 v[152:155], a[244:247], v[136:139], v[152:155]\n"
  "ds_read_b64_tr_b16 a[204:205], v16 offset:16608\n"
  "v_mfma_f32_16x16x32_bf16 v[156:159], a[244:247], v[140:143], v[156:159]\n"
  "buffer_load_dwordx4 v6, s[48:51], s31 offen offset:1024 lds\n"
  "v_mfma_f32_16x16x32_bf16 v[160:163], a[248:251], v[136:139], v[160:163]\n"
  "ds_read_b64_tr_b16 a[206:207], v16 offset:18400\n"
  "ds_read_b64_tr_b16 a[208:209], v15 offset:16640\n"
  "v_mfma_f32_16x16x32_bf16 v[164:167], a[248:251], v[140:143], v[164:167]\n"
  "ds_read_b64_tr_b16 a[210:211], v15 offset:18432\n"
  "s_waitcnt lgkmcnt(8)\n"
  "v_mfma_f32_16x16x32_bf16 v[168:171], a[252:255], v[136:139], v[168:171]\n"
  "ds_read_b64_tr_b16 a[212:213], v16 offset:16672\n"
  "v_mfma_f32_16x16x32_bf16 v[172:175], a[252:255], v[140:143], v[172:175]\n"
  "buffer_load_dwordx4 v7, s[48:51], s31 offen offset:2048 lds\n"
  "v_mfma_f32_16x16x32_bf16 v[176:179], a[192:195], v[136:139], v[176:179]\n"
  "ds_read_b64_tr_b16 a[214:215], v16 offset:18464\n"
  "ds_read_b64_tr_b16 a[216:217], v15 offset:16704\n"
  "v_cvt_pk_bf16_f32 v144, v144, v145\n"
  "v_mfma_f32_16x16x32_bf16 v[180:183], a[192:195], v[140:143], v[180:183]\n"
  "ds_read_b64_tr_b16 a[218:219], v15 offset:18496\n"
  "v_cvt_pk_bf16_f32 v145, v146, v147\n"
  "v_cvt_pk_bf16_f32 v146, v152, v153\n"
  "s_waitcnt lgkmcnt(8)\n"
  "v_mfma_f32_16x16x32_bf16 v[184:187], a[196:199], v[136:139], v[184:187]\n"
  "ds_read_b64_tr_b16 a[220:221], v16 offset:16736\n"
  "v_cvt_pk_bf16_f32 v147, v154, v155\n"
  "v_mfma_f32_16x16x32_bf16 v[188:191], a[196:199], v[140:143], v[188:191]\n"
  "s_mov_b32 exec_hi, 0\n"
  "buffer_load_dwordx4 v8, s[48:51], s31 offen offset:3072 lds\n"
  "s_mov_b32 exec_hi, -1\n"
  "v_mfma_f32_16x16x32_bf16 v[192:195], a[200:203], v[136:139], v[192:195]\n"
  "ds_read_b64_tr_b16 a[222:223], v16 offset:18528\n"
  "ds_read_b64_tr_b16 a[224:225], v15 offset:16768\n"
  "v_pk_max_i16 v144, v144, 0\n"
  "v_mfma_f32_16x16x32_bf16 v[196:199], a[200:203], v[140:143], v[196:199]\n"
  "ds_read_b64_tr_b16 a[226:227], v15 offset:18560\n"
  "v_pk_max_i16 v145, v145, 0\n"
  "v_pk_max_i16 v146, v146, 0\n"
  "s_waitcnt lgkmcnt(8)\n"
  "v_mfma_f32_16x16x32_bf16 v[200:203], a[204:207], v[136:139], v[200:203]\n"
  "ds_read_b64_tr_b16 a[228:229], v16 offset:16800\n"
  "v_pk_max_i16 v147, v147, 0\n"
  "v_mfma_f32_16x16x32_bf16 v[204:207], a[204:207], v[140:143], v[204:207]\n"
  "s_waitcnt vmcnt(12)\n"
  "s_barrier\n"
  "ds_read_b64_tr_b16 a[230:231], v16 offset:18592\n"
  "s_add_u32 m0, s35, 32768\n"
  "s_mov_b32 s31, 57344\n"
  "v_mfma_f32_16x16x32_bf16 v[208:211], a[208:211], v[136:139], v[208:211]\n"
  "buffer_load_dwordx4 v5, s[48:51], s31 offen offset:0 lds\n"
  "v_mfma_f32_16x16x32_bf16 v[212:215], a[208:211], v[140:143], v[212:215]\n"
  "ds_read_b64_tr_b16 a[232:233], v13 offset:0\n"
  "ds_read_b64_tr_b16 a[234:235], v13 offset:1792\n"
  "v_cvt_pk_bf16_f32 v148, v148, v149\n"
  "s_waitcnt lgkmcnt(8)\n"
  "v_mfma_f32_16x16x32_bf16 v[216:219], a[212:215], v[136:139], v[216:219]\n"
  "ds_read_b64_tr_b16 a[236:237], v14 offset:32\n"
  "v_cvt_pk_bf16_f32 v149, v150, v151\n"
  "v_cvt_pk_bf16_f32 v150, v156, v157\n"
  "v_mfma_f32_16x16x32_bf16 v[220:223], a[212:215], v[140:143], v[220:223]\n"
  "buffer_load_dwordx4 v6, s[48:51], s31 offen offset:1024 lds\n"
  "v_mfma_f32_16x16x32_bf16 v[224:227], a[216:219], v[136:139], v[224:227]\n"
  "ds_read_b64_tr_b16 a[238:239], v14 offset:1824\n"
  "ds_read_b64_tr_b16 a[240:241], v13 offset:64\n"
  "v_cvt_pk_bf16_f32 v151, v158, v159\n"
  "v_mfma_f32_16x16x32_bf16 v[228:231], a[216:219], v[140:143], v[228:231]\n"
  "ds_read_b64_tr_b16 a[242:243], v13 offset:1856\n"
  "v_pk_max_i16 v148, v148, 0\n"
  "s_waitcnt lgkmcnt(8)\n"
  "v_mfma_f32_16x16x32_bf16 v[232:235], a[220:223], v[136:139], v[232:235]\n"
  "buffer_load_dwordx4 v7, s[48:51], s31 offen offset:2048 lds\n"
  "v_mfma_f32_16x16x32_bf16 v[236:239], a[220:223], v[140:143], v[236:239]\n"
  "ds_read_b64_tr_b16 a[244:245], v14 offset:96\n"
  "ds_read_b64_tr_b16 a[246:247], v14 offset:1888\n"
  "v_pk_max_i16 v149, v149, 0\n"
  "v_mfma_f32_16x16x32_bf16 v[240:243], a[224:227], v[136:139], v[240:243]\n"
  "ds_read_b64_tr_b16 a[248:249], v13 offset:128\n"
  "v_pk_max_i16 v150, v150, 0\n"
  "v_pk_max_i16 v151, v151, 0\n"
  "v_mfma_f32_16x16x32_bf16 v[244:247], a[224:227], v[140:143], v[244:247]\n"
  "s_mov_b32 exec_hi, 0\n"
  "buffer_load_dwordx4 v8, s[48:51], s31 offen offset:3072 lds\n"
  "s_mov_b32 exec_hi, -1\n"
  "s_waitcnt lgkmcnt(7)\n"
  "v_mfma_f32_16x16x32_bf16 v[248:251], a[228:231], v[136:139], v[248:251]\n"
  "ds_read_b64_tr_b16 a[250:251], v13 offset:1920\n"
  "ds_read_b64_tr_b16 a[252:253], v14 offset:160\n"
  "v_mfma_f32_16x16x32_bf16 v[252:255], a[228:231], v[140:143], v[252:255]\n"
  "ds_read_b64_tr_b16 a[254:255], v14 offset:1952\n"
  "s_add_u32 m0, s35, 49152\n"
  "s_mov_b32 s31, 86016\n"
  "v_mfma_f32_16x16x32_bf16 v[32:35], a[232:235], v[144:147], 0\n"
  "buffer_load_dwordx4 v5, s[48:51], s31 offen offset:0 lds\n"
  "v_mfma_f32_16x16x32_bf16 v[36:39], a[232:235], v[148:151], 0\n"
  "ds_read_b64_tr_b16 a[192:193], v13 offset:192\n"
  "ds_read_b64_tr_b16 a[194:195], v13 offset:1984\n"
  "s_waitcnt lgkmcnt(8)\n"
  "v_mfma_f32_16x16x32_bf16 v[40:43], a[236:239], v[144:147], 0\n"
  "ds_read_b64_tr_b16 a[196:197], v14 offset:224\n"
  "ds_read_b64_tr_b16 v[152:153], v13 offset:384\n"
  "v_cvt_pk_bf16_f32 v160, v160, v161\n"
  "v_cvt_pk_bf16_f32 v161, v162, v163\n"
  "v_mfma_f32_16x16x32_bf16 v[44:47], a[236:239], v[148:151], 0\n"
  "buffer_load_dwordx4 v6, s[48:51], s31 offen offset:1024 lds\n"
  "v_mfma_f32_16x16x32_bf16 v[48:51], a[240:243], v[144:147], 0\n"
  "ds_read_b64_tr_b16 a[198:199], v14 offset:2016\n"
  "ds_read_b64_tr_b16 v[154:155], v13 offset:2176\n"
  "ds_read_b64_tr_b16 a[200:201], v13 offset:256\n"
  "v_cvt_pk_bf16_f32 v162, v168, v169\n"
  "v_mfma_f32_16x16x32_bf16 v[52:55], a[240:243], v[148:151], 0\n"
  "ds_read_b64_tr_b16 a[202:203], v13 offset:2048\n"
  "v_cvt_pk_bf16_f32 v163, v170, v171\n"
  "s_waitcnt lgkmcnt(11)\n"
  "v_mfma_f32_16x16x32_bf16 v[56:59], a[244:247], v[144:147], 0\n"
  "buffer_load_dwordx4 v7, s[48:51], s31 offen offset:2048 lds\n"
  "v_pk_max_i16 v160, v160, 0\n"
  "v_mfma_f32_16x16x32_bf16 v[60:63], a[244:247], v[148:151], 0\n"
  "ds_read_b64_tr_b16 a[204:205], v14 offset:288\n"
  "ds_read_b64_tr_b16 a[206:207], v14 offset:2080\n"
  "Lto3%=:\n"
  "s_cbranch_execz Ltor0%=\n"
  "s_waitcnt lgkmcnt(10)\n"
  "v_mfma_f32_16x16x32_bf16 v[64:67], a[248:251], v[144:147], 0\n"
  "ds_read_b64_tr_b16 a[208:209], v13 offset:320\n"
  "ds_read_b64_tr_b16 v[156:157], v14 offset:416\n"
  "v_pk_max_i16 v161, v161, 0\n"
  "v_pk_max_i16 v162, v162, 0\n"
  "v_mfma_f32_16x16x32_bf16 v[68:71], a[248:251], v[148:151], 0\n"
  "s_mov_b32 exec_hi, 0\n"
  "buffer_load_dwordx4 v8, s[48:51], s31 offen offset:3072 lds\n"
  "s_mov_b32 exec_hi, -1\n"
  "v_mfma_f32_16x16x32_bf16 v[72:75], a[252:255], v[144:147], 0\n"
  "ds_read_b64_tr_b16 a[210:211], v13 offset:2112\n"
  "ds_read_b64_tr_b16 v[158:159], v14 offset:2208\n"
  "ds_read_b64_tr_b16 a[212:213], v14 offset:352\n"
  "v_mfma_f32_16x16x32_bf16 v[76:79], a[252:255], v[148:151], 0\n"
  "s_waitcnt vmcnt(8)\n"
  "s_barrier\n"
  "ds_read_b64_tr_b16 a[214:215], v14 offset:2144\n"
  "s_add_u32 m0, s35, 65536\n"
  "s_mov_b32 s31, 114688\n"
  "s_waitcnt lgkmcnt(12)\n"
  "v_mfma_f32_16x16x32_bf16 v[80:83], a[192:195], v[144:147], 0\n"
  "buffer_load_dwordx4 v5, s[48:51], s31 offen offset:0 lds\n"
  "v_pk_max_i16 v163, v163, 0\n"
  "v_mfma_f32_16x16x32_bf16 v[84:87], a[192:195], v[148:151], 0\n"
  "ds_read_b64_tr_b16 a[216:217], v13 offset:16384\n"
  "ds_read_b64_tr_b16 a[218:219], v13 offset:18176\n"
  "ds_read_b128 a[64:67], v9 offset:8192\n"
  "s_waitcnt lgkmcnt(11)\n"
  "v_mfma_f32_16x16x32_bf16 v[88:91], a[196:199], v[144:147], 0\n"
  "ds_read_b64_tr_b16 a[220:221], v14 offset:16416\n"
  "v_cvt_pk_bf16_f32 v164, v164, v165\n"
  "v_cvt_pk_bf16_f32 v165, v166, v167\n"
  "v_mfma_f32_16x16x32_bf16 v[92:95], a[196:199], v[148:151], 0\n"
  "buffer_load_dwordx4 v6, s[48:51], s31 offen offset:1024 lds\n"
  "v_mfma_f32_16x16x32_bf16 v[96:99], a[200:203], v[144:147], 0\n"
  "ds_read_b64_tr_b16 a[222:223], v14 offset:18208\n"
  "ds_read_b128 a[68:71], v9 offset:10240\n"
  "ds_read_b64_tr_b16 a[224:225], v13 offset:16448\n"
  "v_cvt_pk_bf16_f32 v166, v172, v173\n"
  "v_mfma_f32_16x16x32_bf16 v[100:103], a[200:203], v[148:151], 0\n"
  "ds_read_b64_tr_b16 a[226:227], v13 offset:18240\n"
  "ds_read_b128 a[72:75], v10 offset:8192\n"
  "v_cvt_pk_bf16_f32 v167, v174, v175\n"
  "s_waitcnt lgkmcnt(13)\n"
  "v_mfma_f32_16x16x32_bf16 v[104:107], a[204:207], v[144:147], 0\n"
  "buffer_load_dwordx4 v7, s[48:51], s31 offen offset:2048 lds\n"
  "v_mfma_f32_16x16x32_bf16 v[108:111], a[204:207], v[148:151], 0\n"
  "ds_read_b64_tr_b16 a[228:229], v14 offset:16480\n"
  "ds_read_b64_tr_b16 a[230:231], v14 offset:18272\n"
  "ds_read_b128 a[76:79], v10 offset:10240\n"
  "s_waitcnt lgkmcnt(12)\n"
  "v_mfma_f32_16x16x32_bf16 v[112:115], a[208:211], v[144:147], 0\n"
  "ds_read_b64_tr_b16 a[232:233], v13 offset:16512\n"
  "v_pk_max_i16 v164, v164, 0\n"
  "v_pk_max_i16 v165, v165, 0\n"
  "v_mfma_f32_16x16x32_bf16 v[116:119], a[208:211], v[148:151], 0\n"
  "ds_read_b64_tr_b16 a[234:235], v13 offset:18304\n"
  "ds_read_b128 a[80:83], v9 offset:12288\n"
  "s_mov_b32 exec_hi, 0\n"
  "buffer_load_dwordx4 v8, s[48:51], s31 offen offset:3072 lds\n"
  "s_mov_b32 exec_hi, -1\n"
  "v_mfma_f32_16x16x32_bf16 v[120:123], a[212:215], v[144:147], 0\n"
  "ds_read_b64_tr_b16 a[236:237], v14 offset:16544\n"
  "v_pk_max_i16 v166, v166, 0\n"
  "v_pk_max_i16 v167, v167, 0\n"
  "v_mfma_f32_16x16x32_bf16 v[124:127], a[212:215], v[148:151], 0\n"
  "ds_read_b64_tr_b16 a[238:239], v14 offset:18336\n"
  "ds_read_b128 a[84:87], v9 offset:14336\n"
  "s_add_u32 m0, s35, 81920\n"
  "s_mov_b32 s31, 143360\n"
  "s_waitcnt lgkmcnt(11)\n"
  "v_mfma_f32_16x16x32_bf16 v[32:35], a[216:219], v[160:163], v[32:35]\n"
  "buffer_load_dwordx4 v5, s[48:51], s31 offen offset:0 lds\n"
  "v_mfma_f32_16x16x32_bf16 v[36:39], a[216:219], v[164:167], v[36:39]\n"
  "ds_read_b64_tr_b16 a[240:241], v13 offset:16576\n"
  "ds_read_b64_tr_b16 a[242:243], v13 offset:18368\n"
  "ds_read_b128 a[88:91], v10 offset:12288\n"
  "v_mfma_f32_16x16x32_bf16 v[40:43], a[220:223], v[160:163], v[40:43]\n"
  "ds_read_b64_tr_b16 a[244:245], v14 offset:16608\n"
  "ds_read_b64_tr_b16 v[168:169], v13 offset:16768\n"
  "v_cvt_pk_bf16_f32 v176, v176, v177\n"
  "v_cvt_pk_bf16_f32 v177, v178, v179\n"
  "v_mfma_f32_16x16x32_bf16 v[44:47], a[220:223], v[164:167], v[44:47]\n"
  "buffer_load_dwordx4 v6, s[48:51], s31 offen offset:1024 lds\n"
  "s_waitcnt lgkmcnt(10)\n"
  "v_mfma_f32_16x16x32_bf16 v[48:51], a[224:227], v[160:163], v[48:51]\n"
  "ds_read_b64_tr_b16 a[246:247], v14 offset:18400\n"
  "ds_read_b64_tr_b16 v[170:171], v13 offset:18560\n"
  "ds_read_b128 a[92:95], v10 offset:14336\n"
  "ds_read_b64_tr_b16 a[248:249], v13 offset:16640\n"
  "v_mfma_f32_16x16x32_bf16 v[52:55], a[224:227], v[164:167], v[52:55]\n"
  "ds_read_b64_tr_b16 a[250:251], v13 offset:18432\n"
  "v_cvt_pk_bf16_f32 v178, v184, v185\n"
  "v_mfma_f32_16x16x32_bf16 v[56:59], a[228:231], v[160:163], v[56:59]\n"
  "buffer_load_dwordx4 v7, s[48:51], s31 offen offset:2048 lds\n"
  "v_cvt_pk_bf16_f32 v179, v186, v187\n"
  "v_mfma_f32_16x16x32_bf16 v[60:63], a[228:231], v[164:167], v[60:63]\n"
  "ds_read_b64_tr_b16 a[252:253], v14 offset:16672\n"
  "ds_read_b64_tr_b16 a[254:255], v14 offset:18464\n"
  "s_waitcnt lgkmcnt(12)\n"
  "v_mfma_f32_16x16x32_bf16 v[64:67], a[232:235], v[160:163], v[64:67]\n"
  "ds_read_b64_tr_b16 a[192:193], v13 offset:16704\n"
  "ds_read_b64_tr_b16 v[172:173], v14 offset:16800\n"
  "v_pk_max_i16 v176, v176, 0\n"
  "v_pk_max_i16 v177, v177, 0\n"
  "v_mfma_f32_16x16x32_bf16 v[68:71], a[232:235], v[164:167], v[68:71]\n"
  "s_mov_b32 exec_hi, 0\n"
  "buffer_load_dwordx4 v8, s[48:51], s31 offen offset:3072 lds\n"
  "s_mov_b32 exec_hi, -1\n"
  "v_mfma_f32_16x16x32_bf16 v[72:75], a[236:239], v[160:163], v[72:75]\n"
  "ds_read_b64_tr_b16 a[194:195], v13 offset:18496\n"
  "ds_read_b64_tr_b16 v[174:175], v14 offset:18592\n"
  "ds_read_b64_tr_b16 a[196:197], v14 offset:16736\n"
  "v_mfma_f32_16x16x32_bf16 v[76:79], a[236:239], v[164:167], v[76:79]\n"
  "s_waitcnt vmcnt(12)\n"
  "s_barrier\n"
  "ds_read_b64_tr_b16 a[198:199], v14 offset:18528\n"
  "s_waitcnt lgkmcnt(13)\n"
  "v_mfma_f32_16x16x32_bf16 v[80:83], a[240:243], v[160:163], v[80:83]\n"
  "ds_read_b64_tr_b16 a[200:201], v13 offset:32768\n"
  "v_lshrrev_b32 v22, 4, v1\n"
  "v_and_b32 v23, 15, v1\n"
  "v_lshlrev_b32 v23, 2, v23\n"
  "s_lshl_b32 s87, s21, 9\n"
  "v_lshl_add_u32 v22, v22, 7, v23\n"
  "s_mul_i32 s26, s22, 48\n"
  "s_add_u32 s87, s87, s26\n"
  "s_add_u32 s87, s87, 8192\n"
  "buffer_load_dword v21, v22, s[60:63], s87 offen sc1\n"
  "v_mfma_f32_16x16x32_bf16 v[84:87], a[240:243], v[164:167], v[84:87]\n"
  "ds_read_b64_tr_b16 a[202:203], v13 offset:34560\n"
  "v_pk_max_i16 v178, v178, 0\n"
  "v_pk_max_i16 v179, v179, 0\n"
  "s_waitcnt lgkmcnt(8)\n"
  "v_mfma_f32_16x16x32_bf16 v[88:91], a[244:247], v[160:163], v[88:91]\n"
  "ds_read_b64_tr_b16 a[204:205], v14 offset:32800\n"
  "s_add_u32 m0, s35, 0\n"
  "s_mov_b32 s31, 172032\n"
  "v_cvt_pk_bf16_f32 v180, v180, v181\n"
  "v_mfma_f32_16x16x32_bf16 v[92:95], a[244:247], v[164:167], v[92:95]\n"
  "buffer_load_dwordx4 v5, s[48:51], s31 offen offset:0 lds\n"
  "v_mfma_f32_16x16x32_bf16 v[96:99], a[248:251], v[160:163], v[96:99]\n"
  "ds_read_b64_tr_b16 a[206:207], v14 offset:34592\n"
  "ds_read_b64_tr_b16 a[208:209], v13 offset:32832\n"
  "v_cvt_pk_bf16_f32 v181, v182, v183\n"
  "v_cvt_pk_bf16_f32 v182, v188, v189\n"
  "v_mfma_f32_16x16x32_bf16 v[100:103], a[248:251], v[164:167], v[100:103]\n"
  "ds_read_b64_tr_b16 a[210:211], v13 offset:34624\n"
  "v_cvt_pk_bf16_f32 v183, v190, v191\n"
  "v_mfma_f32_16x16x32_bf16 v[104:107], a[252:255], v[160:163], v[104:107]\n"
  "buffer_load_dwordx4 v6, s[48:51], s31 offen offset:1024 lds\n"
  "v_pk_max_i16 v180, v180, 0\n"
  "v_mfma_f32_16x16x32_bf16 v[108:111], a[252:255], v[164:167], v[108:111]\n"
  "ds_read_b64_tr_b16 a[212:213], v14 offset:32864\n"
  "ds_read_b64_tr_b16 a[214:215], v14 offset:34656\n"
  "s_waitcnt lgkmcnt(7)\n"
  "v_mfma_f32_16x16x32_bf16 v[112:115], a[192:195], v[160:163], v[112:115]\n"
  "ds_read_b64_tr_b16 a[216:217], v13 offset:32896\n"
  "v_pk_max_i16 v181, v181, 0\n"
  "v_pk_max_i16 v182, v182, 0\n"
  "v_mfma_f32_16x16x32_bf16 v[116:119], a[192:195], v[164:167], v[116:119]\n"
  "buffer_load_dwordx4 v7, s[48:51], s31 offen offset:2048 lds\n"
  "v_mfma_f32_16x16x32_bf16 v[120:123], a[196:199], v[160:163], v[120:123]\n"
  "ds_read_b64_tr_b16 a[218:219], v13 offset:34688\n"
  "ds_read_b64_tr_b16 a[220:221], v14 offset:32928\n"
  "v_pk_max_i16 v183, v183, 0\n"
  "v_mfma_f32_16x16x32_bf16 v[124:127], a[196:199], v[164:167], v[124:127]\n"
  "ds_read_b64_tr_b16 a[222:223], v14 offset:34720\n"
  "s_waitcnt lgkmcnt(9)\n"
  "v_mfma_f32_16x16x32_bf16 v[32:35], a[200:203], v[176:179], v[32:35]\n"
  "s_mov_b32 exec_hi, 0\n"
  "buffer_load_dwordx4 v8, s[48:51], s31 offen offset:3072 lds\n"
  "s_mov_b32 exec_hi, -1\n"
  "v_mfma_f32_16x16x32_bf16 v[36:39], a[200:203], v[180:183], v[36:39]\n"
  "ds_read_b64_tr_b16 a[224:225], v13 offset:32960\n"
  "ds_read_b64_tr_b16 a[226:227], v13 offset:34752\n"
  "s_waitcnt lgkmcnt(8)\n"
  "v_mfma_f32_16x16x32_bf16 v[40:43], a[204:207], v[176:179], v[40:43]\n"
  "ds_read_b64_tr_b16 a[228:229], v14 offset:32992\n"
  "ds_read_b64_tr_b16 v[184:185], v13 offset:33152\n"
  "s_add_u32 m0, s34, 0\n"
  "s_mov_b32 s30, 0\n"
  "v_cvt_pk_bf16_f32 v192, v192, v193\n"
  "v_mfma_f32_16x16x32_bf16 v[44:47], a[204:207], v[180:183], v[44:47]\n"
  "ds_read_b64_tr_b16 a[230:231], v14 offset:34784\n"
  "ds_read_b64_tr_b16 v[186:187], v13 offset:34944\n"
  "v_xor_b32 v3, 0x100, v3\n"
  "buffer_load_dwordx4 v3, s[52:55], s30 offen offset:0 nt lds\n"
  "v_mfma_f32_16x16x32_bf16 v[48:51], a[208:211], v[176:179], v[48:51]\n"
  "ds_read_b64_tr_b16 a[232:233], v13 offset:33024\n"
  "v_cvt_pk_bf16_f32 v193, v194, v195\n"
  "v_cvt_pk_bf16_f32 v194, v200, v201\n"
  "v_mfma_f32_16x16x32_bf16 v[52:55], a[208:211], v[180:183], v[52:55]\n"
  "ds_read_b64_tr_b16 a[234:235], v13 offset:34816\n"
  "s_mov_b32 s30, 64512\n"
  "v_cvt_pk_bf16_f32 v195, v202, v203\n"
  "v_pk_max_i16 v192, v192, 0\n"
  "s_waitcnt lgkmcnt(9)\n"
  "v_mfma_f32_16x16x32_bf16 v[56:59], a[212:215], v[176:179], v[56:59]\n"
  "buffer_load_dwordx4 v3, s[52:55], s30 offen offset:1024 nt lds\n"
  "v_mfma_f32_16x16x32_bf16 v[60:63], a[212:215], v[180:183], v[60:63]\n"
  "ds_read_b64_tr_b16 a[236:237], v14 offset:33056\n"
  "ds_read_b64_tr_b16 a[238:239], v14 offset:34848\n"
  "v_pk_max_i16 v193, v193, 0\n"
  "v_mfma_f32_16x16x32_bf16 v[64:67], a[216:219], v[176:179], v[64:67]\n"
  "ds_read_b64_tr_b16 a[240:241], v13 offset:33088\n"
  "ds_read_b64_tr_b16 v[188:189], v14 offset:33184\n"
  "s_mov_b32 s30, 14336\n"
  "v_mfma_f32_16x16x32_bf16 v[68:71], a[216:219], v[180:183], v[68:71]\n"
  "buffer_load_dwordx4 v3, s[52:55], s30 offen offset:2048 nt lds\n"
  "s_waitcnt lgkmcnt(8)\n"
  "v_mfma_f32_16x16x32_bf16 v[72:75], a[220:223], v[176:179], v[72:75]\n"
  "ds_read_b64_tr_b16 a[242:243], v13 offset:34880\n"
  "ds_read_b64_tr_b16 v[190:191], v14 offset:34976\n"
  "ds_read_b64_tr_b16 a[244:245], v14 offset:33120\n"
  "v_mfma_f32_16x16x32_bf16 v[76:79], a[220:223], v[180:183], v[76:79]\n"
  "s_waitcnt vmcnt(16)\n"
  "s_barrier\n"
  "ds_read_b64_tr_b16 a[246:247], v14 offset:34912\n"
  "s_mov_b32 s30, 78848\n"
  "v_mfma_f32_16x16x32_bf16 v[80:83], a[224:227], v[176:179], v[80:83]\n"
  "buffer_load_dwordx4 v3, s[52:55], s30 offen offset:3072 nt lds\n"
  "v_mfma_f32_16x16x32_bf16 v[84:87], a[224:227], v[180:183], v[84:87]\n"
  "ds_read_b64_tr_b16 a[248:249], v13 offset:49152\n"
  "ds_read_b64_tr_b16 a[250:251], v13 offset:50944\n"
  "s_waitcnt lgkmcnt(10)\n"
  "v_mfma_f32_16x16x32_bf16 v[88:91], a[228:231], v[176:179], v[88:91]\n"
  "ds_read_b64_tr_b16 a[252:253], v14 offset:49184\n"
  "s_add_u32 m0, s34, 4096\n"
  "s_mov_b32 s30, 128\n"
  "v_pk_max_i16 v194, v194, 0\n"
  "v_pk_max_i16 v195, v195, 0\n"
  "v_mfma_f32_16x16x32_bf16 v[92:95], a[228:231], v[180:183], v[92:95]\n"
  "ds_read_b64_tr_b16 a[254:255], v14 offset:50976\n"
  "buffer_load_dwordx4 v3, s[52:55], s30 offen offset:0 nt lds\n"
  "v_mfma_f32_16x16x32_bf16 v[96:99], a[232:235], v[176:179], v[96:99]\n"
  "ds_read_b64_tr_b16 a[192:193], v13 offset:49216\n"
  "v_cvt_pk_bf16_f32 v196, v196, v197\n"
  "v_cvt_pk_bf16_f32 v197, v198, v199\n"
  "v_mfma_f32_16x16x32_bf16 v[100:103], a[232:235], v[180:183], v[100:103]\n"
  "ds_read_b64_tr_b16 a[194:195], v13 offset:51008\n"
  "v_cvt_pk_bf16_f32 v198, v204, v205\n"
  "s_waitcnt lgkmcnt(10)\n"
  "v_mfma_f32_16x16x32_bf16 v[104:107], a[236:239], v[176:179], v[104:107]\n"
  "ds_read_b64_tr_b16 a[196:197], v14 offset:49248\n"
  "v_cvt_pk_bf16_f32 v199, v206, v207\n"
  "v_pk_max_i16 v196, v196, 0\n"
  "v_mfma_f32_16x16x32_bf16 v[108:111], a[236:239], v[180:183], v[108:111]\n"
  "ds_read_b64_tr_b16 a[198:199], v14 offset:51040\n"
  "s_mov_b32 s30, 64640\n"
  "v_pk_max_i16 v197, v197, 0\n"
  "s_waitcnt lgkmcnt(8)\n"
  "v_mfma_f32_16x16x32_bf16 v[112:115], a[240:243], v[176:179], v[112:115]\n"
  "buffer_load_dwordx4 v3, s[52:55], s30 offen offset:1024 nt lds\n"
  "v_mfma_f32_16x16x32_bf16 v[116:119], a[240:243], v[180:183], v[116:119]\n"
  "ds_read_b64_tr_b16 a[200:201], v13 offset:49280\n"
  "ds_read_b64_tr_b16 a[202:203], v13 offset:51072\n"
  "v_pk_max_i16 v198, v198, 0\n"
  "v_mfma_f32_16x16x32_bf16 v[120:123], a[244:247], v[176:179], v[120:123]\n"
  "ds_read_b64_tr_b16 a[204:205], v14 offset:49312\n"
  "v_pk_max_i16 v199, v199, 0\n"
  "v_mfma_f32_16x16x32_bf16 v[124:127], a[244:247], v[180:183], v[124:127]\n"
  "ds_read_b64_tr_b16 a[206:207], v14 offset:51104\n"
  "s_waitcnt lgkmcnt(8)\n"
  "v_mfma_f32_16x16x32_bf16 v[32:35], a[248:251], v[192:195], v[32:35]\n"
  "ds_read_b64_tr_b16 a[208:209], v13 offset:49344\n"
  "s_mov_b32 s30, 14464\n"
  "v_mfma_f32_16x16x32_bf16 v[36:39], a[248:251], v[196:199], v[36:39]\n"
  "buffer_load_dwordx4 v3, s[52:55], s30 offen offset:2048 nt lds\n"
  "v_mfma_f32_16x16x32_bf16 v[40:43], a[252:255], v[192:195], v[40:43]\n"
  "ds_read_b64_tr_b16 a[210:211], v13 offset:51136\n"
  "ds_read_b64_tr_b16 a[212:213], v14 offset:49376\n"
  "ds_read_b64_tr_b16 v[200:201], v13 offset:49536\n"
  "v_mfma_f32_16x16x32_bf16 v[44:47], a[252:255], v[196:199], v[44:47]\n"
  "ds_read_b64_tr_b16 a[214:215], v14 offset:51168\n"
  "ds_read_b64_tr_b16 v[202:203], v13 offset:51328\n"
  "v_cvt_pk_bf16_f32 v208, v208, v209\n"
  "s_waitcnt lgkmcnt(10)\n"
  "v_mfma_f32_16x16x32_bf16 v[48:51], a[192:195], v[192:195], v[48:51]\n"
  "ds_read_b64_tr_b16 a[216:217], v13 offset:49408\n"
  "v_cvt_pk_bf16_f32 v209, v210, v211\n"
  "v_cvt_pk_bf16_f32 v210, v216, v217\n"
  "v_mfma_f32_16x16x32_bf16 v[52:55], a[192:195], v[196:199], v[52:55]\n"
  "ds_read_b64_tr_b16 a[218:219], v13 offset:51200\n"
  "s_mov_b32 s30, 78976\n"
  "v_cvt_pk_bf16_f32 v211, v218, v219\n"
  "v_pk_max_i16 v208, v208, 0\n"
  "v_mfma_f32_16x16x32_bf16 v[56:59], a[196:199], v[192:195], v[56:59]\n"
  "buffer_load_dwordx4 v3, s[52:55], s30 offen offset:3072 nt lds\n"
  "v_mfma_f32_16x16x32_bf16 v[60:63], a[196:199], v[196:199], v[60:63]\n"
  "ds_read_b64_tr_b16 a[220:221], v14 offset:49440\n"
  "ds_read_b64_tr_b16 a[222:223], v14 offset:51232\n"
  "v_pk_max_i16 v209, v209, 0\n"
  "s_waitcnt lgkmcnt(10)\n"
  "v_mfma_f32_16x16x32_bf16 v[64:67], a[200:203], v[192:195], v[64:67]\n"
  "ds_read_b64_tr_b16 a[224:225], v13 offset:49472\n"
  "ds_read_b64_tr_b16 v[204:205], v14 offset:49568\n"
  "v_mfma_f32_16x16x32_bf16 v[68:71], a[200:203], v[196:199], v[68:71]\n"
  "ds_read_b64_tr_b16 a[226:227], v13 offset:51264\n"
  "ds_read_b64_tr_b16 v[206:207], v14 offset:51360\n"
  "v_pk_max_i16 v210, v210, 0\n"
  "v_mfma_f32_16x16x32_bf16 v[72:75], a[204:207], v[192:195], v[72:75]\n"
  "ds_read_b64_tr_b16 a[228:229], v14 offset:49504\n"
  "v_pk_max_i16 v211, v211, 0\n"
  "v_cvt_pk_bf16_f32 v212, v212, v213\n"
  "v_mfma_f32_16x16x32_bf16 v[76:79], a[204:207], v[196:199], v[76:79]\n"
  "s_waitcnt vmcnt(17)\n"
  "s_barrier\n"
  "ds_read_b64_tr_b16 a[230:231], v14 offset:51296\n"
  "s_waitcnt lgkmcnt(10)\n"
  "v_mfma_f32_16x16x32_bf16 v[80:83], a[208:211], v[192:195], v[80:83]\n"
  "ds_read_b64_tr_b16 a[232:233], v15 offset:0\n"
  "s_waitcnt vmcnt(12)\n"
  "v_cmp_ne_u32 vcc, v20, v21\n"
  "s_nop 0\n"
  "s_and_b64 s[26:27], vcc, exec\n"
  "s_cbranch_scc1 Lsabo%=\n"
  "Lbabo%=:\n"
  "v_mfma_f32_16x16x32_bf16 v[84:87], a[208:211], v[196:199], v[84:87]\n"
  "ds_read_b64_tr_b16 a[234:235], v15 offset:1792\n"
  "v_cvt_pk_bf16_f32 v213, v214, v215\n"
  "v_mfma_f32_16x16x32_bf16 v[88:91], a[212:215], v[192:195], v[88:91]\n"
  "ds_read_b64_tr_b16 a[236:237], v16 offset:32\n"
  "s_add_u32 m0, s68, 16384\n"
  "s_lshl_b32 s31, s80, 17\n"
  "v_cvt_pk_bf16_f32 v214, v220, v221\n"
  "v_mfma_f32_16x16x32_bf16 v[92:95], a[212:215], v[196:199], v[92:95]\n"
  "buffer_load_dwordx4 v17, s[56:59], s31 offen offset:0 sc1 lds\n"
  "s_waitcnt lgkmcnt(9)\n"
  "v_mfma_f32_16x16x32_bf16 v[96:99], a[216:219], v[192:195], v[96:99]\n"
  "ds_read_b64_tr_b16 a[238:239], v16 offset:1824\n"
  "ds_read_b64_tr_b16 a[240:241], v15 offset:64\n"
  "v_cvt_pk_bf16_f32 v215, v222, v223\n"
  "v_mfma_f32_16x16x32_bf16 v[100:103], a[216:219], v[196:199], v[100:103]\n"
  "ds_read_b64_tr_b16 a[242:243], v15 offset:1856\n"
  "v_pk_max_i16 v212, v212, 0\n"
  "v_pk_max_i16 v213, v213, 0\n"
  "v_mfma_f32_16x16x32_bf16 v[104:107], a[220:223], v[192:195], v[104:107]\n"
  "buffer_load_dwordx4 v17, s[56:59], s31 offen offset:1024 sc1 lds\n"
  "v_mfma_f32_16x16x32_bf16 v[108:111], a[220:223], v[196:199], v[108:111]\n"
  "ds_read_b64_tr_b16 a[244:245], v16 offset:96\n"
  "ds_read_b64_tr_b16 a[246:247], v16 offset:1888\n"
  "s_waitcnt lgkmcnt(8)\n"
  "v_mfma_f32_16x16x32_bf16 v[112:115], a[224:227], v[192:195], v[112:115]\n"
  "ds_read_b64_tr_b16 a[248:249], v15 offset:128\n"
  "v_pk_max_i16 v214, v214, 0\n"
  "v_pk_max_i16 v215, v215, 0\n"
  "v_mfma_f32_16x16x32_bf16 v[116:119], a[224:227], v[196:199], v[116:119]\n"
  "buffer_load_dwordx4 v17, s[56:59], s31 offen offset:2048 sc1 lds\n"
  "v_mfma_f32_16x16x32_bf16 v[120:123], a[228:231], v[192:195], v[120:123]\n"
  "ds_read_b64_tr_b16 a[250:251], v15 offset:1920\n"
  "ds_read_b64_tr_b16 a[252:253], v16 offset:160\n"
  "v_mfma_f32_16x16x32_bf16 v[124:127], a[228:231], v[196:199], v[124:127]\n"
  "ds_read_b64_tr_b16 a[254:255], v16 offset:1952\n"
  "s_waitcnt lgkmcnt(9)\n"
  "v_mfma_f32_16x16x32_bf16 v[32:35], a[232:235], v[208:211], v[32:35]\n"
  "buffer_load_dwordx4 v17, s[56:59], s31 offen offset:3072 sc1 lds\n"
  "v_mfma_f32_16x16x32_bf16 v[36:39], a[232:235], v[212:215], v[36:39]\n"
  "ds_read_b64_tr_b16 a[192:193], v15 offset:192\n"
  "ds_read_b64_tr_b16 a[194:195], v15 offset:1984\n"
  "s_waitcnt lgkmcnt(8)\n"
  "v_mfma_f32_16x16x32_bf16 v[40:43], a[236:239], v[208:211], v[40:43]\n"
  "ds_read_b64_tr_b16 a[196:197], v16 offset:224\n"
  "ds_read_b64_tr_b16 v[216:217], v15 offset:384\n"
  "s_add_u32 m0, s68, 32768\n"
  "s_lshl_b32 s31, s80, 17\n"
  "s_add_u32 s31, s31, 0x10000\n"
  "v_cvt_pk_bf16_f32 v224, v224, v225\n"
  "v_cvt_pk_bf16_f32 v225, v226, v227\n"
  "v_mfma_f32_16x16x32_bf16 v[44:47], a[236:239], v[212:215], v[44:47]\n"
  "buffer_load_dwordx4 v17, s[56:59], s31 offen offset:0 sc1 lds\n"
  "v_mfma_f32_16x16x32_bf16 v[48:51], a[240:243], v[208:211], v[48:51]\n"
  "ds_read_b64_tr_b16 a[198:199], v16 offset:2016\n"
  "ds_read_b64_tr_b16 v[218:219], v15 offset:2176\n"
  "ds_read_b64_tr_b16 a[200:201], v15 offset:256\n"
  "v_cvt_pk_bf16_f32 v226, v232, v233\n"
  "v_mfma_f32_16x16x32_bf16 v[52:55], a[240:243], v[212:215], v[52:55]\n"
  "ds_read_b64_tr_b16 a[202:203], v15 offset:2048\n"
  "v_cvt_pk_bf16_f32 v227, v234, v235\n"
  "s_waitcnt lgkmcnt(9)\n"
  "v_mfma_f32_16x16x32_bf16 v[56:59], a[244:247], v[208:211], v[56:59]\n"
  "buffer_load_dwordx4 v17, s[56:59], s31 offen offset:1024 sc1 lds\n"
  "v_mfma_f32_16x16x32_bf16 v[60:63], a[244:247], v[212:215], v[60:63]\n"
  "ds_read_b64_tr_b16 a[204:205], v16 offset:288\n"
  "ds_read_b64_tr_b16 a[206:207], v16 offset:2080\n"
  "v_pk_max_i16 v224, v224, 0\n"
  "v_mfma_f32_16x16x32_bf16 v[64:67], a[248:251], v[208:211], v[64:67]\n"
  "ds_read_b64_tr_b16 a[208:209], v15 offset:320\n"
  "ds_read_b64_tr_b16 v[220:221], v16 offset:416\n"
  "v_pk_max_i16 v225, v225, 0\n"
  "v_mfma_f32_16x16x32_bf16 v[68:71], a[248:251], v[212:215], v[68:71]\n"
  "buffer_load_dwordx4 v17, s[56:59], s31 offen offset:2048 sc1 lds\n"
  "s_waitcnt lgkmcnt(8)\n"
  "v_mfma_f32_16x16x32_bf16 v[72:75], a[252:255], v[208:211], v[72:75]\n"
  "ds_read_b64_tr_b16 a[210:211], v15 offset:2112\n"
  "ds_read_b64_tr_b16 v[222:223], v16 offset:2208\n"
  "ds_read_b64_tr_b16 a[212:213], v16 offset:352\n"
  "v_pk_max_i16 v226, v226, 0\n"
  "v_mfma_f32_16x16x32_bf16 v[76:79], a[252:255], v[212:215], v[76:79]\n"
  "s_barrier\n"
  "ds_read_b64_tr_b16 a[214:215], v16 offset:2144\n"
  "v_mfma_f32_16x16x32_bf16 v[80:83], a[192:195], v[208:211], v[80:83]\n"
  "buffer_load_dwordx4 v17, s[56:59], s31 offen offset:3072 sc1 lds\n"
  "v_mfma_f32_16x16x32_bf16 v[84:87], a[192:195], v[212:215], v[84:87]\n"
  "ds_read_b64_tr_b16 a[216:217], v15 offset:16384\n"
  "ds_read_b64_tr_b16 a[218:219], v15 offset:18176\n"
  "s_waitcnt vmcnt(12)\n"
  "ds_read_b128 a[96:99], v9 offset:0\n"
  "s_waitcnt lgkmcnt(11)\n"
  "v_mfma_f32_16x16x32_bf16 v[88:91], a[196:199], v[208:211], v[88:91]\n"
  "ds_read_b64_tr_b16 a[220:221], v16 offset:16416\n"
  "s_add_u32 m0, s68, 49152\n"
  "s_lshl_b32 s31, s81, 17\n"
  "v_pk_max_i16 v227, v227, 0\n"
  "v_cvt_pk_bf16_f32 v228, v228, v229\n"
  "v_mfma_f32_16x16x32_bf16 v[92:95], a[196:199], v[212:215], v[92:95]\n"
  "buffer_load_dwordx4 v17, s[56:59], s31 offen offset:0 sc1 lds\n"
  "v_mfma_f32_16x16x32_bf16 v[96:99], a[200:203], v[208:211], v[96:99]\n"
  "ds_read_b64_tr_b16 a[222:223], v16 offset:18208\n"
  "ds_read_b128 a[100:103], v9 offset:2048\n"
  "ds_read_b64_tr_b16 a[224:225], v15 offset:16448\n"
  "v_cvt_pk_bf16_f32 v229, v230, v231\n"
  "v_mfma_f32_16x16x32_bf16 v[100:103], a[200:203], v[212:215], v[100:103]\n"
  "ds_read_b64_tr_b16 a[226:227], v15 offset:18240\n"
  "ds_read_b128 a[104:107], v10 offset:0\n"
  "s_waitcnt lgkmcnt(10)\n"
  "v_mfma_f32_16x16x32_bf16 v[104:107], a[204:207], v[208:211], v[104:107]\n"
  "ds_read_b64_tr_b16 a[228:229], v16 offset:16480\n"
  "v_cvt_pk_bf16_f32 v230, v236, v237\n"
  "v_cvt_pk_bf16_f32 v231, v238, v239\n"
  "v_mfma_f32_16x16x32_bf16 v[108:111], a[204:207], v[212:215], v[108:111]\n"
  "buffer_load_dwordx4 v17, s[56:59], s31 offen offset:1024 sc1 lds\n"
  "v_mfma_f32_16x16x32_bf16 v[112:115], a[208:211], v[208:211], v[112:115]\n"
  "ds_read_b64_tr_b16 a[230:231], v16 offset:18272\n"
  "ds_read_b128 a[108:111], v10 offset:2048\n"
  "ds_read_b64_tr_b16 a[232:233], v15 offset:16512\n"
  "v_pk_max_i16 v228, v228, 0\n"
  "v_mfma_f32_16x16x32_bf16 v[116:119], a[208:211], v[212:215], v[116:119]\n"
  "ds_read_b64_tr_b16 a[234:235], v15 offset:18304\n"
  "v_pk_max_i16 v229, v229, 0\n"
  "s_waitcnt lgkmcnt(10)\n"
  "v_mfma_f32_16x16x32_bf16 v[120:123], a[212:215], v[208:211], v[120:123]\n"
  "ds_read_b64_tr_b16 a[236:237], v16 offset:16544\n"
  "v_pk_max_i16 v230, v230, 0\n"
  "v_pk_max_i16 v231, v231, 0\n"
  "v_mfma_f32_16x16x32_bf16 v[124:127], a[212:215], v[212:215], v[124:127]\n"
  "buffer_load_dwordx4 v17, s[56:59], s31 offen offset:2048 sc1 lds\n"
  "v_mfma_f32_16x16x32_bf16 v[32:35], a[216:219], v[224:227], v[32:35]\n"
  "ds_read_b64_tr_b16 a[238:239], v16 offset:18336\n"
  "ds_read_b64_tr_b16 a[240:241], v15 offset:16576\n"
  "v_mfma_f32_16x16x32_bf16 v[36:39], a[216:219], v[228:231], v[36:39]\n"
  "ds_read_b64_tr_b16 a[242:243], v15 offset:18368\n"
  "s_waitcnt lgkmcnt(9)\n"
  "v_mfma_f32_16x16x32_bf16 v[40:43], a[220:223], v[224:227], v[40:43]\n"
  "ds_read_b64_tr_b16 a[244:245], v16 offset:16608\n"
  "ds_read_b64_tr_b16 v[232:233], v15 offset:16768\n"
  "v_cvt_pk_bf16_f32 v240, v240, v241\n"
  "v_mfma_f32_16x16x32_bf16 v[44:47], a[220:223], v[228:231], v[44:47]\n"
  "buffer_load_dwordx4 v17, s[56:59], s31 offen offset:3072 sc1 lds\n"
  "v_mfma_f32_16x16x32_bf16 v[48:51], a[224:227], v[224:227], v[48:51]\n"
  "ds_read_b64_tr_b16 a[246:247], v16 offset:18400\n"
  "ds_read_b64_tr_b16 v[234:235], v15 offset:18560\n"
  "ds_read_b64_tr_b16 a[248:249], v15 offset:16640\n"
  "v_mfma_f32_16x16x32_bf16 v[52:55], a[224:227], v[228:231], v[52:55]\n"
  "ds_read_b64_tr_b16 a[250:251], v15 offset:18432\n"
  "v_cvt_pk_bf16_f32 v241, v242, v243\n"
  "v_cvt_pk_bf16_f32 v242, v248, v249\n"
  "s_waitcnt lgkmcnt(10)\n"
  "v_mfma_f32_16x16x32_bf16 v[56:59], a[228:231], v[224:227], v[56:59]\n"
  "ds_read_b64_tr_b16 a[252:253], v16 offset:16672\n"
  "v_cvt_pk_bf16_f32 v243, v250, v251\n"
  "v_pk_max_i16 v240, v240, 0\n"
  "v_mfma_f32_16x16x32_bf16 v[60:63], a[228:231], v[228:231], v[60:63]\n"
  "ds_read_b64_tr_b16 a[254:255], v16 offset:18464\n"
  "v_pk_max_i16 v241, v241, 0\n"
  "v_mfma_f32_16x16x32_bf16 v[64:67], a[232:235], v[224:227], v[64:67]\n"
  "ds_read_b64_tr_b16 a[192:193], v15 offset:16704\n"
  "ds_read_b64_tr_b16 v[236:237], v16 offset:16800\n"
  "v_pk_max_i16 v242, v242, 0\n"
  "v_mfma_f32_16x16x32_bf16 v[68:71], a[232:235], v[228:231], v[68:71]\n"
  "ds_read_b64_tr_b16 a[194:195], v15 offset:18496\n"
  "ds_read_b64_tr_b16 v[238:239], v16 offset:18592\n"
  "v_pk_max_i16 v243, v243, 0\n"
  "s_waitcnt lgkmcnt(12)\n"
  "v_mfma_f32_16x16x32_bf16 v[72:75], a[236:239], v[224:227], v[72:75]\n"
  "ds_read_b64_tr_b16 a[196:197], v16 offset:16736\n"
  "v_cvt_pk_bf16_f32 v244, v244, v245\n"
  "v_cvt_pk_bf16_f32 v245, v246, v247\n"
  "v_mfma_f32_16x16x32_bf16 v[76:79], a[236:239], v[228:231], v[76:79]\n"
  "s_barrier\n"
  "ds_read_b64_tr_b16 a[198:199], v16 offset:18528\n"
  "s_add_u32 m0, s68, 65536\n"
  "s_lshl_b32 s31, s81, 17\n"
  "s_add_u32 s31, s31, 0x10000\n"
  "v_mfma_f32_16x16x32_bf16 v[80:83], a[240:243], v[224:227], v[80:83]\n"
  "buffer_load_dwordx4 v17, s[56:59], s31 offen offset:0 sc1 lds\n"
  "v_mfma_f32_16x16x32_bf16 v[84:87], a[240:243], v[228:231], v[84:87]\n"
  "ds_read_b64_tr_b16 a[200:201], v13 offset:0\n"
  "ds_read_b64_tr_b16 a[202:203], v13 offset:1792\n"
  "s_waitcnt vmcnt(13)\n"
  "ds_read_b128 a[112:115], v9 offset:4096\n"
  "s_waitcnt lgkmcnt(11)\n"
  "v_mfma_f32_16x16x32_bf16 v[88:91], a[244:247], v[224:227], v[88:91]\n"
  "ds_read_b64_tr_b16 a[204:205], v14 offset:32\n"
  "v_cvt_pk_bf16_f32 v246, v252, v253\n"
  "v_mfma_f32_16x16x32_bf16 v[92:95], a[244:247], v[228:231], v[92:95]\n"
  "ds_read_b64_tr_b16 a[206:207], v14 offset:1824\n"
  "ds_read_b128 a[116:119], v9 offset:6144\n"
  "v_cvt_pk_bf16_f32 v247, v254, v255\n"
  "v_mfma_f32_16x16x32_bf16 v[96:99], a[248:251], v[224:227], v[96:99]\n"
  "buffer_load_dwordx4 v17, s[56:59], s31 offen offset:1024 sc1 lds\n"
  "v_mfma_f32_16x16x32_bf16 v[100:103], a[248:251], v[228:231], v[100:103]\n"
  "ds_read_b64_tr_b16 a[208:209], v13 offset:64\n"
  "ds_read_b64_tr_b16 a[210:211], v13 offset:1856\n"
  "ds_read_b128 a[120:123], v10 offset:4096\n"
  "s_waitcnt lgkmcnt(11)\n"
  "v_mfma_f32_16x16x32_bf16 v[104:107], a[252:255], v[224:227], v[104:107]\n"
  "ds_read_b64_tr_b16 a[212:213], v14 offset:96\n"
  "v_pk_max_i16 v244, v244, 0\n"
  "v_pk_max_i16 v245, v245, 0\n"
  "v_mfma_f32_16x16x32_bf16 v[108:111], a[252:255], v[228:231], v[108:111]\n"
  "ds_read_b64_tr_b16 a[214:215], v14 offset:1888\n"
  "ds_read_b128 a[124:127], v10 offset:6144\n"
  "v_mfma_f32_16x16x32_bf16 v[112:115], a[192:195], v[224:227], v[112:115]\n"
  "buffer_load_dwordx4 v17, s[56:59], s31 offen offset:2048 sc1 lds\n"
  "v_mfma_f32_16x16x32_bf16 v[116:119], a[192:195], v[228:231], v[116:119]\n"
  "ds_read_b64_tr_b16 a[216:217], v13 offset:128\n"
  "ds_read_b64_tr_b16 a[218:219], v13 offset:1920\n"
  "v_pk_max_i16 v246, v246, 0\n"
  "s_waitcnt lgkmcnt(11)\n"
  "v_mfma_f32_16x16x32_bf16 v[120:123], a[196:199], v[224:227], v[120:123]\n"
  "ds_read_b64_tr_b16 a[220:221], v14 offset:160\n"
  "v_pk_max_i16 v247, v247, 0\n"
  "v_mfma_f32_16x16x32_bf16 v[124:127], a[196:199], v[228:231], v[124:127]\n"
  "ds_read_b64_tr_b16 a[222:223], v14 offset:1952\n"
  "v_mfma_f32_16x16x32_bf16 v[32:35], a[200:203], v[240:243], v[32:35]\n"
  "buffer_load_dwordx4 v17, s[56:59], s31 offen offset:3072 sc1 lds\n"
  "v_mfma_f32_16x16x32_bf16 v[36:39], a[200:203], v[244:247], v[36:39]\n"
  "ds_read_b64_tr_b16 a[224:225], v13 offset:192\n"
  "ds_read_b64_tr_b16 a[226:227], v13 offset:1984\n"
  "v_and_b32 v22, 12, v1\n"
  "s_waitcnt lgkmcnt(9)\n"
  "v_mfma_f32_16x16x32_bf16 v[40:43], a[204:207], v[240:243], v[40:43]\n"
  "ds_read_b64_tr_b16 a[228:229], v14 offset:224\n"
  "ds_read_b64_tr_b16 v[248:249], v13 offset:384\n"
  "v_and_b32 v23, 15, v1\n"
  "v_mfma_f32_16x16x32_bf16 v[44:47], a[204:207], v[244:247], v[44:47]\n"
  "ds_read_b64_tr_b16 a[230:231], v14 offset:2016\n"
  "ds_read_b64_tr_b16 v[250:251], v13 offset:2176\n"
  "v_add_u32 v22, v22, v23\n"
  "v_mfma_f32_16x16x32_bf16 v[48:51], a[208:211], v[240:243], v[48:51]\n"
  "ds_read_b64_tr_b16 a[232:233], v13 offset:256\n"
  "v_mul_u32_u24 v22, 896, v22\n"
  "v_mfma_f32_16x16x32_bf16 v[52:55], a[208:211], v[244:247], v[52:55]\n"
  "ds_read_b64_tr_b16 a[234:235], v13 offset:2048\n"
  "v_lshrrev_b32 v23, 4, v1\n"
  "s_mul_i32 s87, s22, 448\n"
  "s_waitcnt lgkmcnt(10)\n"
  "v_mfma_f32_16x16x32_bf16 v[56:59], a[212:215], v[240:243], v[56:59]\n"
  "ds_read_b64_tr_b16 a[236:237], v14 offset:288\n"
  "v_lshl_add_u32 v24, v23, 3, v22\n"
  "v_mfma_f32_16x16x32_bf16 v[60:63], a[212:215], v[244:247], v[60:63]\n"
  "ds_read_b64_tr_b16 a[238:239], v14 offset:2080\n"
  "s_lshl_b32 s26, s79, 6\n"
  "s_add_u32 s87, s87, s26\n"
  "v_mfma_f32_16x16x32_bf16 v[64:67], a[216:219], v[240:243], v[64:67]\n"
  "ds_read_b64_tr_b16 a[240:241], v13 offset:320\n"
  "ds_read_b64_tr_b16 v[252:253], v14 offset:416\n"
  "v_mfma_f32_16x16x32_bf16 v[68:71], a[216:219], v[244:247], v[68:71]\n"
  "ds_read_b64_tr_b16 a[242:243], v13 offset:2112\n"
  "ds_read_b64_tr_b16 v[254:255], v14 offset:2208\n"
  "v_and_b32 v25, 1, v23\n"
  "s_waitcnt lgkmcnt(12)\n"
  "v_mfma_f32_16x16x32_bf16 v[72:75], a[220:223], v[240:243], v[72:75]\n"
  "ds_read_b64_tr_b16 a[244:245], v14 offset:352\n"
  "v_lshrrev_b32 v23, 1, v23\n"
  "v_mfma_f32_16x16x32_bf16 v[76:79], a[220:223], v[244:247], v[76:79]\n"
  "s_waitcnt vmcnt(12)\n"
  "s_barrier\n"
  "ds_read_b64_tr_b16 a[246:247], v14 offset:2144\n"
  "s_add_u32 m0, s68, 81920\n"
  "s_lshl_b32 s31, s82, 17\n"
  "v_mfma_f32_16x16x32_bf16 v[80:83], a[224:227], v[240:243], v[80:83]\n"
  "buffer_load_dwordx4 v17, s[56:59], s31 offen offset:0 sc1 lds\n"
  "v_mfma_f32_16x16x32_bf16 v[84:87], a[224:227], v[244:247], v[84:87]\n"
  "ds_read_b128 a[248:251], v18 offset:16384\n"
  "v_mul_f32 v32, v32, v11\n"
  "s_waitcnt lgkmcnt(9)\n"
  "v_mfma_f32_16x16x32_bf16 v[88:91], a[228:231], v[240:243], v[88:91]\n"
  "ds_read_b128 a[252:255], v18 offset:17408\n"
  "v_mul_f32 v33, v33, v11\n"
  "v_mul_f32 v34, v34, v11\n"
  "v_mfma_f32_16x16x32_bf16 v[92:95], a[228:231], v[244:247], v[92:95]\n"
  "v_mul_f32 v35, v35, v11\n"
  "v_lshl_add_u32 v23, v25, 1, v23\n"
  "v_lshl_add_u32 v25, v23, 4, v22\n"
  "v_mfma_f32_16x16x32_bf16 v[96:99], a[232:235], v[240:243], v[96:99]\n"
  "buffer_load_dwordx4 v17, s[56:59], s31 offen offset:1024 sc1 lds\n"
  "v_mfma_f32_16x16x32_bf16 v[100:103], a[232:235], v[244:247], v[100:103]\n"
  "ds_read_b128 a[192:195], v18 offset:18432\n"
  "v_mul_f32 v36, v36, v12\n"
  "s_mul_i32 s74, s79, 1835008\n"
  "s_waitcnt lgkmcnt(5)\n"
  "v_mfma_f32_16x16x32_bf16 v[104:107], a[236:239], v[240:243], v[104:107]\n"
  "ds_read_b128 a[196:199], v18 offset:19456\n"
  "v_mul_f32 v37, v37, v12\n"
  "v_mul_f32 v38, v38, v12\n"
  "v_mfma_f32_16x16x32_bf16 v[108:111], a[236:239], v[244:247], v[108:111]\n"
  "v_mul_f32 v39, v39, v12\n"
  "v_mfma_f32_16x16x32_bf16 v[112:115], a[240:243], v[240:243], v[112:115]\n"
  "buffer_load_dwordx4 v17, s[56:59], s31 offen offset:2048 sc1 lds\n"
  "v_mfma_f32_16x16x32_bf16 v[116:119], a[240:243], v[244:247], v[116:119]\n"
  "ds_read_b128 a[200:203], v18 offset:20480\n"
  "s_add_u32 s74, s74, s73\n"
  "v_and_b32 v22, 7, v1\n"
  "s_waitcnt lgkmcnt(4)\n"
  "v_mfma_f32_16x16x32_bf16 v[120:123], a[244:247], v[240:243], v[120:123]\n"
  "ds_read_b128 a[204:207], v18 offset:21504\n"
  "v_lshlrev_b32 v22, 2, v22\n"
  "v_mfma_f32_16x16x32_bf16 v[124:127], a[244:247], v[244:247], v[124:127]\n"
  "s_lshl_b32 s79, s79, 14\n"
  "s_add_u32 s79, s79, s33\n"
  "v_mfma_f32_16x16x32_bf16 v[32:35], a[248:251], a[0:3], v[32:35]\n"
  "buffer_load_dwordx4 v17, s[56:59], s31 offen offset:3072 sc1 lds\n"
  "v_mfma_f32_16x16x32_bf16 v[36:39], a[248:251], a[4:7], v[36:39]\n"
  "ds_read_b128 a[208:211], v18 offset:22528\n"
  "s_waitcnt lgkmcnt(4)\n"
  "v_mfma_f32_16x16x32_bf16 v[32:35], a[252:255], a[8:11], v[32:35]\n"
  "ds_read_b128 a[212:215], v18 offset:23552\n"
  "v_mfma_f32_16x16x32_bf16 v[36:39], a[252:255], a[12:15], v[36:39]\n"
  "v_mfma_f32_16x16x32_bf16 v[32:35], a[192:195], a[16:19], v[32:35]\n"
  "ds_read_b128 a[216:219], v18 offset:24576\n"
  "v_mfma_f32_16x16x32_bf16 v[36:39], a[192:195], a[20:23], v[36:39]\n"
  "s_waitcnt lgkmcnt(4)\n"
  "v_mfma_f32_16x16x32_bf16 v[32:35], a[196:199], a[24:27], v[32:35]\n"
  "ds_read_b128 a[220:223], v18 offset:25600\n"
  "v_mfma_f32_16x16x32_bf16 v[36:39], a[196:199], a[28:31], v[36:39]\n"
  "v_mfma_f32_16x16x32_bf16 v[32:35], a[200:203], a[32:35], v[32:35]\n"
  "ds_read_b128 a[224:227], v18 offset:26624\n"
  "v_mfma_f32_16x16x32_bf16 v[36:39], a[200:203], a[36:39], v[36:39]\n"
  "s_waitcnt lgkmcnt(4)\n"
  "v_mfma_f32_16x16x32_bf16 v[32:35], a[204:207], a[40:43], v[32:35]\n"
  "ds_read_b128 a[228:231], v18 offset:27648\n"
  "v_mfma_f32_16x16x32_bf16 v[36:39], a[204:207], a[44:47], v[36:39]\n"
  "v_mfma_f32_16x16x32_bf16 v[32:35], a[208:211], a[48:51], v[32:35]\n"
  "ds_read_b128 a[232:235], v18 offset:28672\n"
  "v_mul_f32 v40, v40, v11\n"
  "v_mfma_f32_16x16x32_bf16 v[36:39], a[208:211], a[52:55], v[36:39]\n"
  "v_mul_f32 v41, v41, v11\n"
  "s_waitcnt lgkmcnt(4)\n"
  "v_mfma_f32_16x16x32_bf16 v[32:35], a[212:215], a[56:59], v[32:35]\n"
  "ds_read_b128 a[236:239], v18 offset:29696\n"
  "v_mul_f32 v42, v42, v11\n"
  "v_mfma_f32_16x16x32_bf16 v[36:39], a[212:215], a[60:63], v[36:39]\n"
  "v_mul_f32 v43, v43, v11\n"
  "v_mfma_f32_16x16x32_bf16 v[32:35], a[216:219], a[64:67], v[32:35]\n"
  "ds_read_b128 a[240:243], v18 offset:30720\n"
  "v_mfma_f32_16x16x32_bf16 v[36:39], a[216:219], a[68:71], v[36:39]\n"
  "v_mul_f32 v44, v44, v12\n"
  "s_waitcnt lgkmcnt(4)\n"
  "v_mfma_f32_16x16x32_bf16 v[32:35], a[220:223], a[72:75], v[32:35]\n"
  "ds_read_b128 a[244:247], v18 offset:31744\n"
  "v_mul_f32 v45, v45, v12\n"
  "v_mfma_f32_16x16x32_bf16 v[36:39], a[220:223], a[76:79], v[36:39]\n"
  "s_waitcnt vmcnt(12)\n"
  "s_barrier\n"
  "s_add_u32 m0, s68, 0\n"
  "s_lshl_b32 s31, s82, 17\n"
  "s_add_u32 s31, s31, 0x10000\n"
  "v_mfma_f32_16x16x32_bf16 v[32:35], a[224:227], a[80:83], v[32:35]\n"
  "buffer_load_dwordx4 v17, s[56:59], s31 offen offset:0 sc1 lds\n"
  "v_mfma_f32_16x16x32_bf16 v[36:39], a[224:227], a[84:87], v[36:39]\n"
  "ds_read_b128 a[248:251], v18 offset:32768\n"
  "s_waitcnt lgkmcnt(4)\n"
  "v_mfma_f32_16x16x32_bf16 v[32:35], a[228:231], a[88:91], v[32:35]\n"
  "ds_read_b128 a[252:255], v18 offset:33792\n"
  "v_mul_f32 v46, v46, v12\n"
  "v_mfma_f32_16x16x32_bf16 v[36:39], a[228:231], a[92:95], v[36:39]\n"
  "v_mul_f32 v47, v47, v12\n"
  "v_mfma_f32_16x16x32_bf16 v[32:35], a[232:235], a[96:99], v[32:35]\n"
  "buffer_load_dwordx4 v17, s[56:59], s31 offen offset:1024 sc1 lds\n"
  "v_mfma_f32_16x16x32_bf16 v[36:39], a[232:235], a[100:103], v[36:39]\n"
  "ds_read_b128 a[192:195], v18 offset:34816\n"
  "s_waitcnt lgkmcnt(4)\n"
  "v_mfma_f32_16x16x32_bf16 v[32:35], a[236:239], a[104:107], v[32:35]\n"
  "ds_read_b128 a[196:199], v18 offset:35840\n"
  "v_mfma_f32_16x16x32_bf16 v[36:39], a[236:239], a[108:111], v[36:39]\n"
  "s_mul_i32 s29, s80, 1835008\n"
  "v_mfma_f32_16x16x32_bf16 v[32:35], a[240:243], a[112:115], v[32:35]\n"
  "buffer_load_dwordx4 v17, s[56:59], s31 offen offset:2048 sc1 lds\n"
  "v_mfma_f32_16x16x32_bf16 v[36:39], a[240:243], a[116:119], v[36:39]\n"
  "ds_read_b128 a[200:203], v18 offset:36864\n"
  "s_add_u32 s29, s29, s73\n"
  "s_waitcnt lgkmcnt(4)\n"
  "v_mfma_f32_16x16x32_bf16 v[32:35], a[244:247], a[120:123], v[32:35]\n"
  "ds_read_b128 a[204:207], v18 offset:37888\n"
  "v_mfma_f32_16x16x32_bf16 v[36:39], a[244:247], a[124:127], v[36:39]\n"
  "s_add_u32 s29, s29, 0\n"
  "v_mfma_f32_16x16x32_bf16 v[40:43], a[248:251], a[0:3], v[40:43]\n"
  "buffer_load_dwordx4 v17, s[56:59], s31 offen offset:3072 sc1 lds\n"
  "v_mfma_f32_16x16x32_bf16 v[44:47], a[248:251], a[4:7], v[44:47]\n"
  "ds_read_b128 a[208:211], v18 offset:38912\n"
  "s_waitcnt lgkmcnt(4)\n"
  "v_mfma_f32_16x16x32_bf16 v[40:43], a[252:255], a[8:11], v[40:43]\n"
  "ds_read_b128 a[212:215], v18 offset:39936\n"
  "v_cvt_pk_bf16_f32 v32, v32, v33\n"
  "v_cvt_pk_bf16_f32 v33, v34, v35\n"
  "v_mfma_f32_16x16x32_bf16 v[44:47], a[252:255], a[12:15], v[44:47]\n"
  "v_cvt_pk_bf16_f32 v34, v36, v37\n"
  "v_cvt_pk_bf16_f32 v35, v38, v39\n"
  "v_mfma_f32_16x16x32_bf16 v[40:43], a[192:195], a[16:19], v[40:43]\n"
  "ds_read_b128 a[216:219], v18 offset:40960\n"
  "v_mfma_f32_16x16x32_bf16 v[44:47], a[192:195], a[20:23], v[44:47]\n"
  "buffer_store_dwordx4 v[32:35], v17, s[60:63], s29 offen offset:0 sc1\n"
  "s_waitcnt lgkmcnt(4)\n"
  "v_mfma_f32_16x16x32_bf16 v[40:43], a[196:199], a[24:27], v[40:43]\n"
  "ds_read_b128 a[220:223], v18 offset:41984\n"
  "v_mfma_f32_16x16x32_bf16 v[44:47], a[196:199], a[28:31], v[44:47]\n"
  "v_mfma_f32_16x16x32_bf16 v[40:43], a[200:203], a[32:35], v[40:43]\n"
  "ds_read_b128 a[224:227], v18 offset:43008\n"
  "v_mfma_f32_16x16x32_bf16 v[44:47], a[200:203], a[36:39], v[44:47]\n"
  "s_waitcnt lgkmcnt(4)\n"
  "v_mfma_f32_16x16x32_bf16 v[40:43], a[204:207], a[40:43], v[40:43]\n"
  "ds_read_b128 a[228:231], v18 offset:44032\n"
  "v_mfma_f32_16x16x32_bf16 v[44:47], a[204:207], a[44:47], v[44:47]\n"
  "v_mfma_f32_16x16x32_bf16 v[40:43], a[208:211], a[48:51], v[40:43]\n"
  "ds_read_b128 a[232:235], v18 offset:45056\n"
  "v_mul_f32 v48, v48, v11\n"
  "v_mfma_f32_16x16x32_bf16 v[44:47], a[208:211], a[52:55], v[44:47]\n"
  "v_mul_f32 v49, v49, v11\n"
  "s_waitcnt lgkmcnt(4)\n"
  "v_mfma_f32_16x16x32_bf16 v[40:43], a[212:215], a[56:59], v[40:43]\n"
  "ds_read_b128 a[236:239], v18 offset:46080\n"
  "v_mul_f32 v50, v50, v11\n"
  "v_mfma_f32_16x16x32_bf16 v[44:47], a[212:215], a[60:63], v[44:47]\n"
  "v_mfma_f32_16x16x32_bf16 v[40:43], a[216:219], a[64:67], v[40:43]\n"
  "ds_read_b128 a[240:243], v18 offset:47104\n"
  "v_mul_f32 v51, v51, v11\n"
  "v_mfma_f32_16x16x32_bf16 v[44:47], a[216:219], a[68:71], v[44:47]\n"
  "v_mul_f32 v52, v52, v12\n"
  "s_waitcnt lgkmcnt(4)\n"
  "v_mfma_f32_16x16x32_bf16 v[40:43], a[220:223], a[72:75], v[40:43]\n"
  "ds_read_b128 a[244:247], v18 offset:48128\n"
  "v_mul_f32 v53, v53, v12\n"
  "v_mfma_f32_16x16x32_bf16 v[44:47], a[220:223], a[76:79], v[44:47]\n"
  "s_waitcnt vmcnt(13)\n"
  "s_barrier\n"
  "s_add_u32 m0, s68, 16384\n"
  "s_lshl_b32 s31, s83, 17\n"
  "v_mfma_f32_16x16x32_bf16 v[40:43], a[224:227], a[80:83], v[40:43]\n"
  "buffer_load_dwordx4 v17, s[56:59], s31 offen offset:0 sc1 lds\n"
  "v_mfma_f32_16x16x32_bf16 v[44:47], a[224:227], a[84:87], v[44:47]\n"
  "ds_read_b128 a[248:251], v18 offset:49152\n"
  "s_waitcnt lgkmcnt(4)\n"
  "v_mfma_f32_16x16x32_bf16 v[40:43], a[228:231], a[88:91], v[40:43]\n"
  "ds_read_b128 a[252:255], v18 offset:50176\n"
  "v_mfma_f32_16x16x32_bf16 v[44:47], a[228:231], a[92:95], v[44:47]\n"
  "v_mul_f32 v54, v54, v12\n"
  "v_mfma_f32_16x16x32_bf16 v[40:43], a[232:235], a[96:99], v[40:43]\n"
  "buffer_load_dwordx4 v17, s[56:59], s31 offen offset:1024 sc1 lds\n"
  "v_mfma_f32_16x16x32_bf16 v[44:47], a[232:235], a[100:103], v[44:47]\n"
  "ds_read_b128 a[192:195], v18 offset:51200\n"
  "s_waitcnt lgkmcnt(4)\n"
  "v_mfma_f32_16x16x32_bf16 v[40:43], a[236:239], a[104:107], v[40:43]\n"
  "ds_read_b128 a[196:199], v18 offset:52224\n"
  "v_mul_f32 v55, v55, v12\n"
  "v_mfma_f32_16x16x32_bf16 v[44:47], a[236:239], a[108:111], v[44:47]\n"
  "v_mfma_f32_16x16x32_bf16 v[40:43], a[240:243], a[112:115], v[40:43]\n"
  "buffer_load_dwordx4 v17, s[56:59], s31 offen offset:2048 sc1 lds\n"
  "v_mfma_f32_16x16x32_bf16 v[44:47], a[240:243], a[116:119], v[44:47]\n"
  "ds_read_b128 a[200:203], v18 offset:53248\n"
  "s_waitcnt lgkmcnt(4)\n"
  "v_mfma_f32_16x16x32_bf16 v[40:43], a[244:247], a[120:123], v[40:43]\n"
  "ds_read_b128 a[204:207], v18 offset:54272\n"
  "v_mfma_f32_16x16x32_bf16 v[44:47], a[244:247], a[124:127], v[44:47]\n"
  "v_mfma_f32_16x16x32_bf16 v[48:51], a[248:251], a[0:3], v[48:51]\n"
  "buffer_load_dwordx4 v17, s[56:59], s31 offen offset:3072 sc1 lds\n"
  "v_mfma_f32_16x16x32_bf16 v[52:55], a[248:251], a[4:7], v[52:55]\n"
  "ds_read_b128 a[208:211], v18 offset:55296\n"
  "s_waitcnt lgkmcnt(4)\n"
  "v_mfma_f32_16x16x32_bf16 v[48:51], a[252:255], a[8:11], v[48:51]\n"
  "ds_read_b128 a[212:215], v18 offset:56320\n"
  "v_cvt_pk_bf16_f32 v40, v40, v41\n"
  "v_cvt_pk_bf16_f32 v41, v42, v43\n"
  "v_mfma_f32_16x16x32_bf16 v[52:55], a[252:255], a[12:15], v[52:55]\n"
  "v_cvt_pk_bf16_f32 v42, v44, v45\n"
  "v_cvt_pk_bf16_f32 v43, v46, v47\n"
  "v_mfma_f32_16x16x32_bf16 v[48:51], a[192:195], a[16:19], v[48:51]\n"
  "ds_read_b128 a[216:219], v18 offset:57344\n"
  "v_mfma_f32_16x16x32_bf16 v[52:55], a[192:195], a[20:23], v[52:55]\n"
  "buffer_store_dwordx4 v[40:43], v17, s[60:63], s29 offen offset:1024 sc1\n"
  "s_waitcnt lgkmcnt(4)\n"
  "v_mfma_f32_16x16x32_bf16 v[48:51], a[196:199], a[24:27], v[48:51]\n"
  "ds_read_b128 a[220:223], v18 offset:58368\n"
  "v_mfma_f32_16x16x32_bf16 v[52:55], a[196:199], a[28:31], v[52:55]\n"
  "v_mfma_f32_16x16x32_bf16 v[48:51], a[200:203], a[32:35], v[48:51]\n"
  "ds_read_b128 a[224:227], v18 offset:59392\n"
  "v_mfma_f32_16x16x32_bf16 v[52:55], a[200:203], a[36:39], v[52:55]\n"
  "s_waitcnt lgkmcnt(4)\n"
  "v_mfma_f32_16x16x32_bf16 v[48:51], a[204:207], a[40:43], v[48:51]\n"
  "ds_read_b128 a[228:231], v18 offset:60416\n"
  "v_mfma_f32_16x16x32_bf16 v[52:55], a[204:207], a[44:47], v[52:55]\n"
  "v_mfma_f32_16x16x32_bf16 v[48:51], a[208:211], a[48:51], v[48:51]\n"
  "ds_read_b128 a[232:235], v18 offset:61440\n"
  "v_mul_f32 v56, v56, v11\n"
  "v_mfma_f32_16x16x32_bf16 v[52:55], a[208:211], a[52:55], v[52:55]\n"
  "v_mul_f32 v57, v57, v11\n"
  "s_waitcnt lgkmcnt(4)\n"
  "v_mfma_f32_16x16x32_bf16 v[48:51], a[212:215], a[56:59], v[48:51]\n"
  "ds_read_b128 a[236:239], v18 offset:62464\n"
  "v_mul_f32 v58, v58, v11\n"
  "v_mfma_f32_16x16x32_bf16 v[52:55], a[212:215], a[60:63], v[52:55]\n"
  "v_mul_f32 v59, v59, v11\n"
  "v_mfma_f32_16x16x32_bf16 v[48:51], a[216:219], a[64:67], v[48:51]\n"
  "ds_read_b128 a[240:243], v18 offset:63488\n"
  "v_mfma_f32_16x16x32_bf16 v[52:55], a[216:219], a[68:71], v[52:55]\n"
  "v_mul_f32 v60, v60, v12\n"
  "s_waitcnt lgkmcnt(4)\n"
  "v_mfma_f32_16x16x32_bf16 v[48:51], a[220:223], a[72:75], v[48:51]\n"
  "ds_read_b128 a[244:247], v18 offset:64512\n"
  "v_mul_f32 v61, v61, v12\n"
  "v_mfma_f32_16x16x32_bf16 v[52:55], a[220:223], a[76:79], v[52:55]\n"
  "s_waitcnt vmcnt(14)\n"
  "s_barrier\n"
  "s_add_u32 m0, s68, 32768\n"
  "s_lshl_b32 s31, s83, 17\n"
  "s_add_u32 s31, s31, 0x10000\n"
  "v_mfma_f32_16x16x32_bf16 v[48:51], a[224:227], a[80:83], v[48:51]\n"
  "buffer_load_dwordx4 v17, s[56:59], s31 offen offset:0 sc1 lds\n"
  "v_mfma_f32_16x16x32_bf16 v[52:55], a[224:227], a[84:87], v[52:55]\n"
  "ds_read_b128 a[248:251], v19 offset:0\n"
  "s_waitcnt lgkmcnt(4)\n"
  "v_mfma_f32_16x16x32_bf16 v[48:51], a[228:231], a[88:91], v[48:51]\n"
  "ds_read_b128 a[252:255], v19 offset:1024\n"
  "v_mul_f32 v62, v62, v12\n"
  "v_mfma_f32_16x16x32_bf16 v[52:55], a[228:231], a[92:95], v[52:55]\n"
  "v_mul_f32 v63, v63, v12\n"
  "v_mfma_f32_16x16x32_bf16 v[48:51], a[232:235], a[96:99], v[48:51]\n"
  "buffer_load_dwordx4 v17, s[56:59], s31 offen offset:1024 sc1 lds\n"
  "v_mfma_f32_16x16x32_bf16 v[52:55], a[232:235], a[100:103], v[52:55]\n"
  "ds_read_b128 a[192:195], v19 offset:2048\n"
  "s_waitcnt lgkmcnt(4)\n"
  "v_mfma_f32_16x16x32_bf16 v[48:51], a[236:239], a[104:107], v[48:51]\n"
  "ds_read_b128 a[196:199], v19 offset:3072\n"
  "v_mfma_f32_16x16x32_bf16 v[52:55], a[236:239], a[108:111], v[52:55]\n"
  "s_mul_i32 s29, s81, 1835008\n"
  "v_mfma_f32_16x16x32_bf16 v[48:51], a[240:243], a[112:115], v[48:51]\n"
  "buffer_load_dwordx4 v17, s[56:59], s31 offen offset:2048 sc1 lds\n"
  "v_mfma_f32_16x16x32_bf16 v[52:55], a[240:243], a[116:119], v[52:55]\n"
  "ds_read_b128 a[200:203], v19 offset:4096\n"
  "s_add_u32 s29, s29, s73\n"
  "s_waitcnt lgkmcnt(4)\n"
  "v_mfma_f32_16x16x32_bf16 v[48:51], a[244:247], a[120:123], v[48:51]\n"
  "ds_read_b128 a[204:207], v19 offset:5120\n"
  "v_mfma_f32_16x16x32_bf16 v[52:55], a[244:247], a[124:127], v[52:55]\n"
  "s_add_u32 s29, s29, 8192\n"
  "v_mfma_f32_16x16x32_bf16 v[56:59], a[248:251], a[0:3], v[56:59]\n"
  "buffer_load_dwordx4 v17, s[56:59], s31 offen offset:3072 sc1 lds\n"
  "v_mfma_f32_16x16x32_bf16 v[60:63], a[248:251], a[4:7], v[60:63]\n"
  "ds_read_b128 a[208:211], v19 offset:6144\n"
  "s_waitcnt lgkmcnt(4)\n"
  "v_mfma_f32_16x16x32_bf16 v[56:59], a[252:255], a[8:11], v[56:59]\n"
  "ds_read_b128 a[212:215], v19 offset:7168\n"
  "v_cvt_pk_bf16_f32 v48, v48, v49\n"
  "v_cvt_pk_bf16_f32 v49, v50, v51\n"
  "v_mfma_f32_16x16x32_bf16 v[60:63], a[252:255], a[12:15], v[60:63]\n"
  "v_cvt_pk_bf16_f32 v50, v52, v53\n"
  "v_cvt_pk_bf16_f32 v51, v54, v55\n"
  "v_mfma_f32_16x16x32_bf16 v[56:59], a[192:195], a[16:19], v[56:59]\n"
  "ds_read_b128 a[216:219], v19 offset:8192\n"
  "v_mfma_f32_16x16x32_bf16 v[60:63], a[192:195], a[20:23], v[60:63]\n"
  "buffer_store_dwordx4 v[48:51], v17, s[60:63], s29 offen offset:0 sc1\n"
  "s_waitcnt lgkmcnt(4)\n"
  "v_mfma_f32_16x16x32_bf16 v[56:59], a[196:199], a[24:27], v[56:59]\n"
  "ds_read_b128 a[220:223], v19 offset:9216\n"
  "v_mfma_f32_16x16x32_bf16 v[60:63], a[196:199], a[28:31], v[60:63]\n"
  "v_mfma_f32_16x16x32_bf16 v[56:59], a[200:203], a[32:35], v[56:59]\n"
  "ds_read_b128 a[224:227], v19 offset:10240\n"
  "v_mfma_f32_16x16x32_bf16 v[60:63], a[200:203], a[36:39], v[60:63]\n"
  "s_waitcnt lgkmcnt(4)\n"
  "v_mfma_f32_16x16x32_bf16 v[56:59], a[204:207], a[40:43], v[56:59]\n"
  "ds_read_b128 a[228:231], v19 offset:11264\n"
  "v_mfma_f32_16x16x32_bf16 v[60:63], a[204:207], a[44:47], v[60:63]\n"
  "v_mfma_f32_16x16x32_bf16 v[56:59], a[208:211], a[48:51], v[56:59]\n"
  "ds_read_b128 a[232:235], v19 offset:12288\n"
  "v_mul_f32 v64, v64, v11\n"
  "v_mfma_f32_16x16x32_bf16 v[60:63], a[208:211], a[52:55], v[60:63]\n"
  "v_mul_f32 v65, v65, v11\n"
  "s_waitcnt lgkmcnt(4)\n"
  "v_mfma_f32_16x16x32_bf16 v[56:59], a[212:215], a[56:59], v[56:59]\n"
  "ds_read_b128 a[236:239], v19 offset:13312\n"
  "v_mul_f32 v66, v66, v11\n"
  "v_mfma_f32_16x16x32_bf16 v[60:63], a[212:215], a[60:63], v[60:63]\n"
  "v_mfma_f32_16x16x32_bf16 v[56:59], a[216:219], a[64:67], v[56:59]\n"
  "ds_read_b128 a[240:243], v19 offset:14336\n"
  "v_mul_f32 v67, v67, v11\n"
  "v_mfma_f32_16x16x32_bf16 v[60:63], a[216:219], a[68:71], v[60:63]\n"
  "v_mul_f32 v68, v68, v12\n"
  "s_waitcnt lgkmcnt(4)\n"
  "v_mfma_f32_16x16x32_bf16 v[56:59], a[220:223], a[72:75], v[56:59]\n"
  "ds_read_b128 a[244:247], v19 offset:15360\n"
  "v_mul_f32 v69, v69, v12\n"
  "v_mfma_f32_16x16x32_bf16 v[60:63], a[220:223], a[76:79], v[60:63]\n"
  "s_waitcnt vmcnt(15)\n"
  "s_barrier\n"
  "s_add_u32 m0, s68, 49152\n"
  "s_lshl_b32 s31, s84, 17\n"
  "v_mfma_f32_16x16x32_bf16 v[56:59], a[224:227], a[80:83], v[56:59]\n"
  "buffer_load_dwordx4 v17, s[56:59], s31 offen offset:0 sc1 lds\n"
  "v_mfma_f32_16x16x32_bf16 v[60:63], a[224:227], a[84:87], v[60:63]\n"
  "ds_read_b128 a[248:251], v19 offset:16384\n"
  "s_waitcnt lgkmcnt(4)\n"
  "v_mfma_f32_16x16x32_bf16 v[56:59], a[228:231], a[88:91], v[56:59]\n"
  "ds_read_b128 a[252:255], v19 offset:17408\n"
  "v_mfma_f32_16x16x32_bf16 v[60:63], a[228:231], a[92:95], v[60:63]\n"
  "v_mul_f32 v70, v70, v12\n"
  "v_mfma_f32_16x16x32_bf16 v[56:59], a[232:235], a[96:99], v[56:59]\n"
  "buffer_load_dwordx4 v17, s[56:59], s31 offen offset:1024 sc1 lds\n"
  "v_mfma_f32_16x16x32_bf16 v[60:63], a[232:235], a[100:103], v[60:63]\n"
  "ds_read_b128 a[192:195], v19 offset:18432\n"
  "s_waitcnt lgkmcnt(4)\n"
  "v_mfma_f32_16x16x32_bf16 v[56:59], a[236:239], a[104:107], v[56:59]\n"
  "ds_read_b128 a[196:199], v19 offset:19456\n"
  "v_mul_f32 v71, v71, v12\n"
  "v_mfma_f32_16x16x32_bf16 v[60:63], a[236:239], a[108:111], v[60:63]\n"
  "v_mfma_f32_16x16x32_bf16 v[56:59], a[240:243], a[112:115], v[56:59]\n"
  "buffer_load_dwordx4 v17, s[56:59], s31 offen offset:2048 sc1 lds\n"
  "v_mfma_f32_16x16x32_bf16 v[60:63], a[240:243], a[116:119], v[60:63]\n"
  "ds_read_b128 a[200:203], v19 offset:20480\n"
  "s_waitcnt lgkmcnt(4)\n"
  "v_mfma_f32_16x16x32_bf16 v[56:59], a[244:247], a[120:123], v[56:59]\n"
  "ds_read_b128 a[204:207], v19 offset:21504\n"
  "v_mfma_f32_16x16x32_bf16 v[60:63], a[244:247], a[124:127], v[60:63]\n"
  "v_mfma_f32_16x16x32_bf16 v[64:67], a[248:251], a[0:3], v[64:67]\n"
  "buffer_load_dwordx4 v17, s[56:59], s31 offen offset:3072 sc1 lds\n"
  "v_mfma_f32_16x16x32_bf16 v[68:71], a[248:251], a[4:7], v[68:71]\n"
  "ds_read_b128 a[208:211], v19 offset:22528\n"
  "s_waitcnt lgkmcnt(4)\n"
  "v_mfma_f32_16x16x32_bf16 v[64:67], a[252:255], a[8:11], v[64:67]\n"
  "ds_read_b128 a[212:215], v19 offset:23552\n"
  "v_cvt_pk_bf16_f32 v56, v56, v57\n"
  "v_cvt_pk_bf16_f32 v57, v58, v59\n"
  "v_mfma_f32_16x16x32_bf16 v[68:71], a[252:255], a[12:15], v[68:71]\n"
  "v_cvt_pk_bf16_f32 v58, v60, v61\n"
  "v_cvt_pk_bf16_f32 v59, v62, v63\n"
  "v_mfma_f32_16x16x32_bf16 v[64:67], a[192:195], a[16:19], v[64:67]\n"
  "ds_read_b128 a[216:219], v19 offset:24576\n"
  "v_mfma_f32_16x16x32_bf16 v[68:71], a[192:195], a[20:23], v[68:71]\n"
  "buffer_store_dwordx4 v[56:59], v17, s[60:63], s29 offen offset:1024 sc1\n"
  "s_waitcnt lgkmcnt(4)\n"
  "v_mfma_f32_16x16x32_bf16 v[64:67], a[196:199], a[24:27], v[64:67]\n"
  "ds_read_b128 a[220:223], v19 offset:25600\n"
  "v_mfma_f32_16x16x32_bf16 v[68:71], a[196:199], a[28:31], v[68:71]\n"
  "v_mfma_f32_16x16x32_bf16 v[64:67], a[200:203], a[32:35], v[64:67]\n"
  "ds_read_b128 a[224:227], v19 offset:26624\n"
  "s_lshl_b32 s32, s80, 14\n"
  "v_mfma_f32_16x16x32_bf16 v[68:71], a[200:203], a[36:39], v[68:71]\n"
  "s_add_u32 s32, s32, s33\n"
  "s_waitcnt lgkmcnt(4)\n"
  "v_mfma_f32_16x16x32_bf16 v[64:67], a[204:207], a[40:43], v[64:67]\n"
  "ds_read_b128 a[228:231], v19 offset:27648\n"
  "s_waitcnt vmcnt(10)\n"
  "s_mov_b64 exec, 1\n"
  "buffer_store_dword v20, off, s[60:63], s32 offset:0 sc1\n"
  "s_mov_b64 exec, -1\n"
  "v_mfma_f32_16x16x32_bf16 v[68:71], a[204:207], a[44:47], v[68:71]\n"
  "v_mfma_f32_16x16x32_bf16 v[64:67], a[208:211], a[48:51], v[64:67]\n"
  "ds_read_b128 a[232:235], v19 offset:28672\n"
  "v_mul_f32 v72, v72, v11\n"
  "v_mfma_f32_16x16x32_bf16 v[68:71], a[208:211], a[52:55], v[68:71]\n"
  "v_mul_f32 v73, v73, v11\n"
  "s_waitcnt lgkmcnt(4)\n"
  "v_mfma_f32_16x16x32_bf16 v[64:67], a[212:215], a[56:59], v[64:67]\n"
  "ds_read_b128 a[236:239], v19 offset:29696\n"
  "v_mul_f32 v74, v74, v11\n"
  "v_mfma_f32_16x16x32_bf16 v[68:71], a[212:215], a[60:63], v[68:71]\n"
  "v_mul_f32 v75, v75, v11\n"
  "v_mfma_f32_16x16x32_bf16 v[64:67], a[216:219], a[64:67], v[64:67]\n"
  "ds_read_b128 a[240:243], v19 offset:30720\n"
  "v_mfma_f32_16x16x32_bf16 v[68:71], a[216:219], a[68:71], v[68:71]\n"
  "v_mul_f32 v76, v76, v12\n"
  "s_waitcnt lgkmcnt(4)\n"
  "v_mfma_f32_16x16x32_bf16 v[64:67], a[220:223], a[72:75], v[64:67]\n"
  "ds_read_b128 a[244:247], v19 offset:31744\n"
  "v_mul_f32 v77, v77, v12\n"
  "v_mfma_f32_16x16x32_bf16 v[68:71], a[220:223], a[76:79], v[68:71]\n"
  "s_barrier\n"
  "s_add_u32 m0, s68, 65536\n"
  "s_lshl_b32 s31, s84, 17\n"
  "s_add_u32 s31, s31, 0x10000\n"
  "v_mfma_f32_16x16x32_bf16 v[64:67], a[224:227], a[80:83], v[64:67]\n"
  "buffer_load_dwordx4 v17, s[56:59], s31 offen offset:0 sc1 lds\n"
  "v_mfma_f32_16x16x32_bf16 v[68:71], a[224:227], a[84:87], v[68:71]\n"
  "ds_read_b128 a[248:251], v18 offset:0\n"
  "s_waitcnt lgkmcnt(4)\n"
  "v_mfma_f32_16x16x32_bf16 v[64:67], a[228:231], a[88:91], v[64:67]\n"
  "ds_read_b128 a[252:255], v18 offset:1024\n"
  "v_mul_f32 v78, v78, v12\n"
  "v_mfma_f32_16x16x32_bf16 v[68:71], a[228:231], a[92:95], v[68:71]\n"
  "v_mul_f32 v79, v79, v12\n"
  "v_mfma_f32_16x16x32_bf16 v[64:67], a[232:235], a[96:99], v[64:67]\n"
  "buffer_load_dwordx4 v17, s[56:59], s31 offen offset:1024 sc1 lds\n"
  "v_mfma_f32_16x16x32_bf16 v[68:71], a[232:235], a[100:103], v[68:71]\n"
  "ds_read_b128 a[192:195], v18 offset:2048\n"
  "s_waitcnt lgkmcnt(4)\n"
  "v_mfma_f32_16x16x32_bf16 v[64:67], a[236:239], a[104:107], v[64:67]\n"
  "ds_read_b128 a[196:199], v18 offset:3072\n"
  "v_mfma_f32_16x16x32_bf16 v[68:71], a[236:239], a[108:111], v[68:71]\n"
  "s_mul_i32 s29, s82, 1835008\n"
  "v_mfma_f32_16x16x32_bf16 v[64:67], a[240:243], a[112:115], v[64:67]\n"
  "buffer_load_dwordx4 v17, s[56:59], s31 offen offset:2048 sc1 lds\n"
  "v_mfma_f32_16x16x32_bf16 v[68:71], a[240:243], a[116:119], v[68:71]\n"
  "ds_read_b128 a[200:203], v18 offset:4096\n"
  "s_add_u32 s29, s29, s73\n"
  "s_waitcnt lgkmcnt(4)\n"
  "v_mfma_f32_16x16x32_bf16 v[64:67], a[244:247], a[120:123], v[64:67]\n"
  "ds_read_b128 a[204:207], v18 offset:5120\n"
  "v_mfma_f32_16x16x32_bf16 v[68:71], a[244:247], a[124:127], v[68:71]\n"
  "s_add_u32 s29, s29, 16384\n"
  "v_mfma_f32_16x16x32_bf16 v[72:75], a[248:251], a[0:3], v[72:75]\n"
  "buffer_load_dwordx4 v17, s[56:59], s31 offen offset:3072 sc1 lds\n"
  "v_mfma_f32_16x16x32_bf16 v[76:79], a[248:251], a[4:7], v[76:79]\n"
  "ds_read_b128 a[208:211], v18 offset:6144\n"
  "s_waitcnt lgkmcnt(4)\n"
  "v_mfma_f32_16x16x32_bf16 v[72:75], a[252:255], a[8:11], v[72:75]\n"
  "ds_read_b128 a[212:215], v18 offset:7168\n"
  "v_cvt_pk_bf16_f32 v64, v64, v65\n"
  "v_cvt_pk_bf16_f32 v65, v66, v67\n"
  "v_mfma_f32_16x16x32_bf16 v[76:79], a[252:255], a[12:15], v[76:79]\n"
  "v_cvt_pk_bf16_f32 v66, v68, v69\n"
  "v_cvt_pk_bf16_f32 v67, v70, v71\n"
  "v_mfma_f32_16x16x32_bf16 v[72:75], a[192:195], a[16:19], v[72:75]\n"
  "ds_read_b128 a[216:219], v18 offset:8192\n"
  "v_mfma_f32_16x16x32_bf16 v[76:79], a[192:195], a[20:23], v[76:79]\n"
  "buffer_store_dwordx4 v[64:67], v17, s[60:63], s29 offen offset:0 sc1\n"
  "s_waitcnt lgkmcnt(4)\n"
  "v_mfma_f32_16x16x32_bf16 v[72:75], a[196:199], a[24:27], v[72:75]\n"
  "ds_read_b128 a[220:223], v18 offset:9216\n"
  "v_mfma_f32_16x16x32_bf16 v[76:79], a[196:199], a[28:31], v[76:79]\n"
  "v_mfma_f32_16x16x32_bf16 v[72:75], a[200:203], a[32:35], v[72:75]\n"
  "ds_read_b128 a[224:227], v18 offset:10240\n"
  "v_mfma_f32_16x16x32_bf16 v[76:79], a[200:203], a[36:39], v[76:79]\n"
  "s_waitcnt lgkmcnt(4)\n"
  "v_mfma_f32_16x16x32_bf16 v[72:75], a[204:207], a[40:43], v[72:75]\n"
  "ds_read_b128 a[228:231], v18 offset:11264\n"
  "v_mfma_f32_16x16x32_bf16 v[76:79], a[204:207], a[44:47], v[76:79]\n"
  "v_mfma_f32_16x16x32_bf16 v[72:75], a[208:211], a[48:51], v[72:75]\n"
  "ds_read_b128 a[232:235], v18 offset:12288\n"
  "v_mul_f32 v80, v80, v11\n"
  "v_mfma_f32_16x16x32_bf16 v[76:79], a[208:211], a[52:55], v[76:79]\n"
  "v_mul_f32 v81, v81, v11\n"
  "s_waitcnt lgkmcnt(4)\n"
  "v_mfma_f32_16x16x32_bf16 v[72:75], a[212:215], a[56:59], v[72:75]\n"
  "ds_read_b128 a[236:239], v18 offset:13312\n"
  "v_mul_f32 v82, v82, v11\n"
  "v_mfma_f32_16x16x32_bf16 v[76:79], a[212:215], a[60:63], v[76:79]\n"
  "v_mfma_f32_16x16x32_bf16 v[72:75], a[216:219], a[64:67], v[72:75]\n"
  "ds_read_b128 a[240:243], v18 offset:14336\n"
  "v_mul_f32 v83, v83, v11\n"
  "v_mfma_f32_16x16x32_bf16 v[76:79], a[216:219], a[68:71], v[76:79]\n"
  "v_mul_f32 v84, v84, v12\n"
  "s_waitcnt lgkmcnt(4)\n"
  "v_mfma_f32_16x16x32_bf16 v[72:75], a[220:223], a[72:75], v[72:75]\n"
  "ds_read_b128 a[244:247], v18 offset:15360\n"
  "v_mul_f32 v85, v85, v12\n"
  "v_mfma_f32_16x16x32_bf16 v[76:79], a[220:223], a[76:79], v[76:79]\n"
  "s_barrier\n"
  "s_add_u32 m0, s68, 81920\n"
  "s_lshl_b32 s31, s85, 17\n"
  "v_mfma_f32_16x16x32_bf16 v[72:75], a[224:227], a[80:83], v[72:75]\n"
  "buffer_load_dwordx4 v17, s[56:59], s31 offen offset:0 sc1 lds\n"
  "v_mfma_f32_16x16x32_bf16 v[76:79], a[224:227], a[84:87], v[76:79]\n"
  "ds_read_b128 a[248:251], v18 offset:16384\n"
  "s_waitcnt lgkmcnt(4)\n"
  "v_mfma_f32_16x16x32_bf16 v[72:75], a[228:231], a[88:91], v[72:75]\n"
  "ds_read_b128 a[252:255], v18 offset:17408\n"
  "v_mfma_f32_16x16x32_bf16 v[76:79], a[228:231], a[92:95], v[76:79]\n"
  "v_mul_f32 v86, v86, v12\n"
  "v_mfma_f32_16x16x32_bf16 v[72:75], a[232:235], a[96:99], v[72:75]\n"
  "buffer_load_dwordx4 v17, s[56:59], s31 offen offset:1024 sc1 lds\n"
  "v_mfma_f32_16x16x32_bf16 v[76:79], a[232:235], a[100:103], v[76:79]\n"
  "ds_read_b128 a[192:195], v18 offset:18432\n"
  "s_waitcnt lgkmcnt(4)\n"
  "v_mfma_f32_16x16x32_bf16 v[72:75], a[236:239], a[104:107], v[72:75]\n"
  "ds_read_b128 a[196:199], v18 offset:19456\n"
  "v_mul_f32 v87, v87, v12\n"
  "v_mfma_f32_16x16x32_bf16 v[76:79], a[236:239], a[108:111], v[76:79]\n"
  "v_mfma_f32_16x16x32_bf16 v[72:75], a[240:243], a[112:115], v[72:75]\n"
  "buffer_load_dwordx4 v17, s[56:59], s31 offen offset:2048 sc1 lds\n"
  "v_mfma_f32_16x16x32_bf16 v[76:79], a[240:243], a[116:119], v[76:79]\n"
  "ds_read_b128 a[200:203], v18 offset:20480\n"
  "s_waitcnt lgkmcnt(4)\n"
  "v_mfma_f32_16x16x32_bf16 v[72:75], a[244:247], a[120:123], v[72:75]\n"
  "ds_read_b128 a[204:207], v18 offset:21504\n"
  "v_mfma_f32_16x16x32_bf16 v[76:79], a[244:247], a[124:127], v[76:79]\n"
  "v_mfma_f32_16x16x32_bf16 v[80:83], a[248:251], a[0:3], v[80:83]\n"
  "buffer_load_dwordx4 v17, s[56:59], s31 offen offset:3072 sc1 lds\n"
  "v_mfma_f32_16x16x32_bf16 v[84:87], a[248:251], a[4:7], v[84:87]\n"
  "ds_read_b128 a[208:211], v18 offset:22528\n"
  "s_waitcnt lgkmcnt(4)\n"
  "v_mfma_f32_16x16x32_bf16 v[80:83], a[252:255], a[8:11], v[80:83]\n"
  "ds_read_b128 a[212:215], v18 offset:23552\n"
  "v_cvt_pk_bf16_f32 v72, v72, v73\n"
  "v_cvt_pk_bf16_f32 v73, v74, v75\n"
  "v_mfma_f32_16x16x32_bf16 v[84:87], a[252:255], a[12:15], v[84:87]\n"
  "v_cvt_pk_bf16_f32 v74, v76, v77\n"
  "v_cvt_pk_bf16_f32 v75, v78, v79\n"
  "v_mfma_f32_16x16x32_bf16 v[80:83], a[192:195], a[16:19], v[80:83]\n"
  "ds_read_b128 a[216:219], v18 offset:24576\n"
  "v_mfma_f32_16x16x32_bf16 v[84:87], a[192:195], a[20:23], v[84:87]\n"
  "buffer_store_dwordx4 v[72:75], v17, s[60:63], s29 offen offset:1024 sc1\n"
  "s_waitcnt lgkmcnt(4)\n"
  "v_mfma_f32_16x16x32_bf16 v[80:83], a[196:199], a[24:27], v[80:83]\n"
  "ds_read_b128 a[220:223], v18 offset:25600\n"
  "v_mfma_f32_16x16x32_bf16 v[84:87], a[196:199], a[28:31], v[84:87]\n"
  "v_mfma_f32_16x16x32_bf16 v[80:83], a[200:203], a[32:35], v[80:83]\n"
  "ds_read_b128 a[224:227], v18 offset:26624\n"
  "s_lshl_b32 s32, s81, 14\n"
  "v_mfma_f32_16x16x32_bf16 v[84:87], a[200:203], a[36:39], v[84:87]\n"
  "s_add_u32 s32, s32, s33\n"
  "s_waitcnt lgkmcnt(4)\n"
  "v_mfma_f32_16x16x32_bf16 v[80:83], a[204:207], a[40:43], v[80:83]\n"
  "ds_read_b128 a[228:231], v18 offset:27648\n"
  "s_waitcnt vmcnt(11)\n"
  "s_mov_b64 exec, 1\n"
  "buffer_store_dword v20, off, s[60:63], s32 offset:4 sc1\n"
  "s_mov_b64 exec, -1\n"
  "v_mfma_f32_16x16x32_bf16 v[84:87], a[204:207], a[44:47], v[84:87]\n"
  "v_mfma_f32_16x16x32_bf16 v[80:83], a[208:211], a[48:51], v[80:83]\n"
  "ds_read_b128 a[232:235], v18 offset:28672\n"
  "v_mul_f32 v88, v88, v11\n"
  "v_mfma_f32_16x16x32_bf16 v[84:87], a[208:211], a[52:55], v[84:87]\n"
  "v_mul_f32 v89, v89, v11\n"
  "s_waitcnt lgkmcnt(4)\n"
  "v_mfma_f32_16x16x32_bf16 v[80:83], a[212:215], a[56:59], v[80:83]\n"
  "ds_read_b128 a[236:239], v18 offset:29696\n"
  "v_mul_f32 v90, v90, v11\n"
  "v_mfma_f32_16x16x32_bf16 v[84:87], a[212:215], a[60:63], v[84:87]\n"
  "v_mul_f32 v91, v91, v11\n"
  "v_mfma_f32_16x16x32_bf16 v[80:83], a[216:219], a[64:67], v[80:83]\n"
  "ds_read_b128 a[240:243], v18 offset:30720\n"
  "v_mfma_f32_16x16x32_bf16 v[84:87], a[216:219], a[68:71], v[84:87]\n"
  "v_mul_f32 v92, v92, v12\n"
  "s_waitcnt lgkmcnt(4)\n"
  "v_mfma_f32_16x16x32_bf16 v[80:83], a[220:223], a[72:75], v[80:83]\n"
  "ds_read_b128 a[244:247], v18 offset:31744\n"
  "v_mul_f32 v93, v93, v12\n"
  "v_mfma_f32_16x16x32_bf16 v[84:87], a[220:223], a[76:79], v[84:87]\n"
  "s_barrier\n"
  "s_add_u32 m0, s68, 0\n"
  "s_lshl_b32 s31, s85, 17\n"
  "s_add_u32 s31, s31, 0x10000\n"
  "v_mfma_f32_16x16x32_bf16 v[80:83], a[224:227], a[80:83], v[80:83]\n"
  "buffer_load_dwordx4 v17, s[56:59], s31 offen offset:0 sc1 lds\n"
  "v_mfma_f32_16x16x32_bf16 v[84:87], a[224:227], a[84:87], v[84:87]\n"
  "ds_read_b128 a[248:251], v18 offset:32768\n"
  "s_waitcnt lgkmcnt(4)\n"
  "v_mfma_f32_16x16x32_bf16 v[80:83], a[228:231], a[88:91], v[80:83]\n"
  "ds_read_b128 a[252:255], v18 offset:33792\n"
  "v_mul_f32 v94, v94, v12\n"
  "v_mfma_f32_16x16x32_bf16 v[84:87], a[228:231], a[92:95], v[84:87]\n"
  "v_mul_f32 v95, v95, v12\n"
  "v_mfma_f32_16x16x32_bf16 v[80:83], a[232:235], a[96:99], v[80:83]\n"
  "buffer_load_dwordx4 v17, s[56:59], s31 offen offset:1024 sc1 lds\n"
  "v_mfma_f32_16x16x32_bf16 v[84:87], a[232:235], a[100:103], v[84:87]\n"
  "ds_read_b128 a[192:195], v18 offset:34816\n"
  "s_waitcnt lgkmcnt(4)\n"
  "v_mfma_f32_16x16x32_bf16 v[80:83], a[236:239], a[104:107], v[80:83]\n"
  "ds_read_b128 a[196:199], v18 offset:35840\n"
  "v_mfma_f32_16x16x32_bf16 v[84:87], a[236:239], a[108:111], v[84:87]\n"
  "s_mul_i32 s29, s83, 1835008\n"
  "v_mfma_f32_16x16x32_bf16 v[80:83], a[240:243], a[112:115], v[80:83]\n"
  "buffer_load_dwordx4 v17, s[56:59], s31 offen offset:2048 sc1 lds\n"
  "v_mfma_f32_16x16x32_bf16 v[84:87], a[240:243], a[116:119], v[84:87]\n"
  "ds_read_b128 a[200:203], v18 offset:36864\n"
  "s_add_u32 s29, s29, s73\n"
  "s_waitcnt lgkmcnt(4)\n"
  "v_mfma_f32_16x16x32_bf16 v[80:83], a[244:247], a[120:123], v[80:83]\n"
  "ds_read_b128 a[204:207], v18 offset:37888\n"
  "v_mfma_f32_16x16x32_bf16 v[84:87], a[244:247], a[124:127], v[84:87]\n"
  "s_add_u32 s29, s29, 24576\n"
  "v_mfma_f32_16x16x32_bf16 v[88:91], a[248:251], a[0:3], v[88:91]\n"
  "buffer_load_dwordx4 v17, s[56:59], s31 offen offset:3072 sc1 lds\n"
  "v_mfma_f32_16x16x32_bf16 v[92:95], a[248:251], a[4:7], v[92:95]\n"
  "ds_read_b128 a[208:211], v18 offset:38912\n"
  "s_waitcnt lgkmcnt(4)\n"
  "v_mfma_f32_16x16x32_bf16 v[88:91], a[252:255], a[8:11], v[88:91]\n"
  "ds_read_b128 a[212:215], v18 offset:39936\n"
  "v_cvt_pk_bf16_f32 v80, v80, v81\n"
  "v_cvt_pk_bf16_f32 v81, v82, v83\n"
  "v_mfma_f32_16x16x32_bf16 v[92:95], a[252:255], a[12:15], v[92:95]\n"
  "v_cvt_pk_bf16_f32 v82, v84, v85\n"
  "v_cvt_pk_bf16_f32 v83, v86, v87\n"
  "v_mfma_f32_16x16x32_bf16 v[88:91], a[192:195], a[16:19], v[88:91]\n"
  "ds_read_b128 a[216:219], v18 offset:40960\n"
  "v_mfma_f32_16x16x32_bf16 v[92:95], a[192:195], a[20:23], v[92:95]\n"
  "buffer_store_dwordx4 v[80:83], v17, s[60:63], s29 offen offset:0 sc1\n"
  "s_waitcnt lgkmcnt(4)\n"
  "v_mfma_f32_16x16x32_bf16 v[88:91], a[196:199], a[24:27], v[88:91]\n"
  "ds_read_b128 a[220:223], v18 offset:41984\n"
  "v_mfma_f32_16x16x32_bf16 v[92:95], a[196:199], a[28:31], v[92:95]\n"
  "v_mfma_f32_16x16x32_bf16 v[88:91], a[200:203], a[32:35], v[88:91]\n"
  "ds_read_b128 a[224:227], v18 offset:43008\n"
  "v_mfma_f32_16x16x32_bf16 v[92:95], a[200:203], a[36:39], v[92:95]\n"
  "s_waitcnt lgkmcnt(4)\n"
  "v_mfma_f32_16x16x32_bf16 v[88:91], a[204:207], a[40:43], v[88:91]\n"
  "ds_read_b128 a[228:231], v18 offset:44032\n"
  "v_mfma_f32_16x16x32_bf16 v[92:95], a[204:207], a[44:47], v[92:95]\n"
  "v_mfma_f32_16x16x32_bf16 v[88:91], a[208:211], a[48:51], v[88:91]\n"
  "ds_read_b128 a[232:235], v18 offset:45056\n"
  "v_mul_f32 v96, v96, v11\n"
  "v_mfma_f32_16x16x32_bf16 v[92:95], a[208:211], a[52:55], v[92:95]\n"
  "v_mul_f32 v97, v97, v11\n"
  "s_waitcnt lgkmcnt(4)\n"
  "v_mfma_f32_16x16x32_bf16 v[88:91], a[212:215], a[56:59], v[88:91]\n"
  "ds_read_b128 a[236:239], v18 offset:46080\n"
  "v_mul_f32 v98, v98, v11\n"
  "v_mfma_f32_16x16x32_bf16 v[92:95], a[212:215], a[60:63], v[92:95]\n"
  "v_mfma_f32_16x16x32_bf16 v[88:91], a[216:219], a[64:67], v[88:91]\n"
  "ds_read_b128 a[240:243], v18 offset:47104\n"
  "v_mul_f32 v99, v99, v11\n"
  "v_mfma_f32_16x16x32_bf16 v[92:95], a[216:219], a[68:71], v[92:95]\n"
  "v_mul_f32 v100, v100, v12\n"
  "s_waitcnt lgkmcnt(4)\n"
  "v_mfma_f32_16x16x32_bf16 v[88:91], a[220:223], a[72:75], v[88:91]\n"
  "ds_read_b128 a[244:247], v18 offset:48128\n"
  "v_mul_f32 v101, v101, v12\n"
  "v_mfma_f32_16x16x32_bf16 v[92:95], a[220:223], a[76:79], v[92:95]\n"
  "s_barrier\n"
  "s_add_u32 m0, s68, 16384\n"
  "s_lshl_b32 s31, s86, 17\n"
  "v_mfma_f32_16x16x32_bf16 v[88:91], a[224:227], a[80:83], v[88:91]\n"
  "buffer_load_dwordx4 v17, s[56:59], s31 offen offset:0 sc1 lds\n"
  "v_mfma_f32_16x16x32_bf16 v[92:95], a[224:227], a[84:87], v[92:95]\n"
  "ds_read_b128 a[248:251], v18 offset:49152\n"
  "s_waitcnt lgkmcnt(4)\n"
  "v_mfma_f32_16x16x32_bf16 v[88:91], a[228:231], a[88:91], v[88:91]\n"
  "ds_read_b128 a[252:255], v18 offset:50176\n"
  "v_mfma_f32_16x16x32_bf16 v[92:95], a[228:231], a[92:95], v[92:95]\n"
  "v_mul_f32 v102, v102, v12\n"
  "v_mfma_f32_16x16x32_bf16 v[88:91], a[232:235], a[96:99], v[88:91]\n"
  "buffer_load_dwordx4 v17, s[56:59], s31 offen offset:1024 sc1 lds\n"
  "v_mfma_f32_16x16x32_bf16 v[92:95], a[232:235], a[100:103], v[92:95]\n"
  "ds_read_b128 a[192:195], v18 offset:51200\n"
  "s_waitcnt lgkmcnt(4)\n"
  "v_mfma_f32_16x16x32_bf16 v[88:91], a[236:239], a[104:107], v[88:91]\n"
  "ds_read_b128 a[196:199], v18 offset:52224\n"
  "v_mul_f32 v103, v103, v12\n"
  "v_mfma_f32_16x16x32_bf16 v[92:95], a[236:239], a[108:111], v[92:95]\n"
  "v_mfma_f32_16x16x32_bf16 v[88:91], a[240:243], a[112:115], v[88:91]\n"
  "buffer_load_dwordx4 v17, s[56:59], s31 offen offset:2048 sc1 lds\n"
  "v_mfma_f32_16x16x32_bf16 v[92:95], a[240:243], a[116:119], v[92:95]\n"
  "ds_read_b128 a[200:203], v18 offset:53248\n"
  "s_waitcnt lgkmcnt(4)\n"
  "v_mfma_f32_16x16x32_bf16 v[88:91], a[244:247], a[120:123], v[88:91]\n"
  "ds_read_b128 a[204:207], v18 offset:54272\n"
  "v_mfma_f32_16x16x32_bf16 v[92:95], a[244:247], a[124:127], v[92:95]\n"
  "v_mfma_f32_16x16x32_bf16 v[96:99], a[248:251], a[0:3], v[96:99]\n"
  "buffer_load_dwordx4 v17, s[56:59], s31 offen offset:3072 sc1 lds\n"
  "v_mfma_f32_16x16x32_bf16 v[100:103], a[248:251], a[4:7], v[100:103]\n"
  "ds_read_b128 a[208:211], v18 offset:55296\n"
  "s_waitcnt lgkmcnt(4)\n"
  "v_mfma_f32_16x16x32_bf16 v[96:99], a[252:255], a[8:11], v[96:99]\n"
  "ds_read_b128 a[212:215], v18 offset:56320\n"
  "v_cvt_pk_bf16_f32 v88, v88, v89\n"
  "v_cvt_pk_bf16_f32 v89, v90, v91\n"
  "v_mfma_f32_16x16x32_bf16 v[100:103], a[252:255], a[12:15], v[100:103]\n"
  "v_cvt_pk_bf16_f32 v90, v92, v93\n"
  "v_cvt_pk_bf16_f32 v91, v94, v95\n"
  "v_mfma_f32_16x16x32_bf16 v[96:99], a[192:195], a[16:19], v[96:99]\n"
  "ds_read_b128 a[216:219], v18 offset:57344\n"
  "v_mfma_f32_16x16x32_bf16 v[100:103], a[192:195], a[20:23], v[100:103]\n"
  "buffer_store_dwordx4 v[88:91], v17, s[60:63], s29 offen offset:1024 sc1\n"
  "s_waitcnt lgkmcnt(4)\n"
  "v_mfma_f32_16x16x32_bf16 v[96:99], a[196:199], a[24:27], v[96:99]\n"
  "ds_read_b128 a[220:223], v18 offset:58368\n"
  "v_mfma_f32_16x16x32_bf16 v[100:103], a[196:199], a[28:31], v[100:103]\n"
  "v_mfma_f32_16x16x32_bf16 v[96:99], a[200:203], a[32:35], v[96:99]\n"
  "ds_read_b128 a[224:227], v18 offset:59392\n"
  "s_lshl_b32 s32, s82, 14\n"
  "v_mfma_f32_16x16x32_bf16 v[100:103], a[200:203], a[36:39], v[100:103]\n"
  "s_add_u32 s32, s32, s33\n"
  "s_waitcnt lgkmcnt(4)\n"
  "v_mfma_f32_16x16x32_bf16 v[96:99], a[204:207], a[40:43], v[96:99]\n"
  "ds_read_b128 a[228:231], v18 offset:60416\n"
  "s_waitcnt vmcnt(11)\n"
  "s_mov_b64 exec, 1\n"
  "buffer_store_dword v20, off, s[60:63], s32 offset:8 sc1\n"
  "s_mov_b64 exec, -1\n"
  "v_mfma_f32_16x16x32_bf16 v[100:103], a[204:207], a[44:47], v[100:103]\n"
  "v_mfma_f32_16x16x32_bf16 v[96:99], a[208:211], a[48:51], v[96:99]\n"
  "ds_read_b128 a[232:235], v18 offset:61440\n"
  "v_mul_f32 v104, v104, v11\n"
  "v_mfma_f32_16x16x32_bf16 v[100:103], a[208:211], a[52:55], v[100:103]\n"
  "v_mul_f32 v105, v105, v11\n"
  "s_waitcnt lgkmcnt(4)\n"
  "v_mfma_f32_16x16x32_bf16 v[96:99], a[212:215], a[56:59], v[96:99]\n"
  "ds_read_b128 a[236:239], v18 offset:62464\n"
  "v_mfma_f32_16x16x32_bf16 v[100:103], a[212:215], a[60:63], v[100:103]\n"
  "v_mul_f32 v106, v106, v11\n"
  "v_mfma_f32_16x16x32_bf16 v[96:99], a[216:219], a[64:67], v[96:99]\n"
  "ds_read_b128 a[240:243], v18 offset:63488\n"
  "v_mul_f32 v107, v107, v11\n"
  "v_mfma_f32_16x16x32_bf16 v[100:103], a[216:219], a[68:71], v[100:103]\n"
  "s_waitcnt lgkmcnt(4)\n"
  "v_mfma_f32_16x16x32_bf16 v[96:99], a[220:223], a[72:75], v[96:99]\n"
  "ds_read_b128 a[244:247], v18 offset:64512\n"
  "v_mul_f32 v108, v108, v12\n"
  "v_mfma_f32_16x16x32_bf16 v[100:103], a[220:223], a[76:79], v[100:103]\n"
  "s_barrier\n"
  "s_add_u32 m0, s68, 32768\n"
  "s_lshl_b32 s31, s86, 17\n"
  "s_add_u32 s31, s31, 0x10000\n"
  "v_mfma_f32_16x16x32_bf16 v[96:99], a[224:227], a[80:83], v[96:99]\n"
  "buffer_load_dwordx4 v17, s[56:59], s31 offen offset:0 sc1 lds\n"
  "v_mfma_f32_16x16x32_bf16 v[100:103], a[224:227], a[84:87], v[100:103]\n"
  "ds_read_b128 a[248:251], v19 offset:0\n"
  "s_waitcnt lgkmcnt(4)\n"
  "v_mfma_f32_16x16x32_bf16 v[96:99], a[228:231], a[88:91], v[96:99]\n"
  "ds_read_b128 a[252:255], v19 offset:1024\n"
  "v_mul_f32 v109, v109, v12\n"
  "v_mfma_f32_16x16x32_bf16 v[100:103], a[228:231], a[92:95], v[100:103]\n"
  "buffer_load_dwordx4 v17, s[56:59], s31 offen offset:1024 sc1 lds\n"
  "v_mfma_f32_16x16x32_bf16 v[96:99], a[232:235], a[96:99], v[96:99]\n"
  "ds_read_b128 a[192:195], v19 offset:2048\n"
  "v_mfma_f32_16x16x32_bf16 v[100:103], a[232:235], a[100:103], v[100:103]\n"
  "v_mul_f32 v110, v110, v12\n"
  "s_waitcnt lgkmcnt(4)\n"
  "v_mfma_f32_16x16x32_bf16 v[96:99], a[236:239], a[104:107], v[96:99]\n"
  "ds_read_b128 a[196:199], v19 offset:3072\n"
  "v_mul_f32 v111, v111, v12\n"
  "v_mfma_f32_16x16x32_bf16 v[100:103], a[236:239], a[108:111], v[100:103]\n"
  "buffer_load_dwordx4 v17, s[56:59], s31 offen offset:2048 sc1 lds\n"
  "s_mul_i32 s29, s84, 1835008\n"
  "v_mfma_f32_16x16x32_bf16 v[96:99], a[240:243], a[112:115], v[96:99]\n"
  "ds_read_b128 a[200:203], v19 offset:4096\n"
  "v_mfma_f32_16x16x32_bf16 v[100:103], a[240:243], a[116:119], v[100:103]\n"
  "s_add_u32 s29, s29, s73\n"
  "s_waitcnt lgkmcnt(4)\n"
  "v_mfma_f32_16x16x32_bf16 v[96:99], a[244:247], a[120:123], v[96:99]\n"
  "ds_read_b128 a[204:207], v19 offset:5120\n"
  "v_mfma_f32_16x16x32_bf16 v[100:103], a[244:247], a[124:127], v[100:103]\n"
  "buffer_load_dwordx4 v17, s[56:59], s31 offen offset:3072 sc1 lds\n"
  "s_add_u32 s29, s29, 32768\n"
  "v_mfma_f32_16x16x32_bf16 v[104:107], a[248:251], a[0:3], v[104:107]\n"
  "ds_read_b128 a[208:211], v19 offset:6144\n"
  "buffer_load_dword v21, v22, s[60:63], s79 offen sc1\n"
  "v_mfma_f32_16x16x32_bf16 v[108:111], a[248:251], a[4:7], v[108:111]\n"
  "s_waitcnt lgkmcnt(4)\n"
  "v_mfma_f32_16x16x32_bf16 v[104:107], a[252:255], a[8:11], v[104:107]\n"
  "ds_read_b128 a[212:215], v19 offset:7168\n"
  "v_cvt_pk_bf16_f32 v96, v96, v97\n"
  "v_cvt_pk_bf16_f32 v97, v98, v99\n"
  "v_mfma_f32_16x16x32_bf16 v[108:111], a[252:255], a[12:15], v[108:111]\n"
  "buffer_load_dwordx2 v[88:89], v24, s[36:39], s87 offen offset:0\n"
  "v_cvt_pk_bf16_f32 v98, v100, v101\n"
  "v_cvt_pk_bf16_f32 v99, v102, v103\n"
  "v_mfma_f32_16x16x32_bf16 v[104:107], a[192:195], a[16:19], v[104:107]\n"
  "ds_read_b128 a[216:219], v19 offset:8192\n"
  "v_mfma_f32_16x16x32_bf16 v[108:111], a[192:195], a[20:23], v[108:111]\n"
  "buffer_store_dwordx4 v[96:99], v17, s[60:63], s29 offen offset:0 sc1\n"
  "s_waitcnt lgkmcnt(4)\n"
  "v_mfma_f32_16x16x32_bf16 v[104:107], a[196:199], a[24:27], v[104:107]\n"
  "ds_read_b128 a[220:223], v19 offset:9216\n"
  "v_mfma_f32_16x16x32_bf16 v[108:111], a[196:199], a[28:31], v[108:111]\n"
  "buffer_load_dwordx2 v[90:91], v24, s[36:39], s87 offen offset:3584\n"
  "v_mfma_f32_16x16x32_bf16 v[104:107], a[200:203], a[32:35], v[104:107]\n"
  "ds_read_b128 a[224:227], v19 offset:10240\n"
  "v_mfma_f32_16x16x32_bf16 v[108:111], a[200:203], a[36:39], v[108:111]\n"
  "s_waitcnt lgkmcnt(4)\n"
  "v_mfma_f32_16x16x32_bf16 v[104:107], a[204:207], a[40:43], v[104:107]\n"
  "ds_read_b128 a[228:231], v19 offset:11264\n"
  "v_mfma_f32_16x16x32_bf16 v[108:111], a[204:207], a[44:47], v[108:111]\n"
  "buffer_load_dwordx2 v[92:93], v24, s[36:39], s87 offen offset:32\n"
  "v_mfma_f32_16x16x32_bf16 v[104:107], a[208:211], a[48:51], v[104:107]\n"
  "ds_read_b128 a[232:235], v19 offset:12288\n"
  "v_mul_f32 v112, v112, v11\n"
  "v_mfma_f32_16x16x32_bf16 v[108:111], a[208:211], a[52:55], v[108:111]\n"
  "v_mul_f32 v113, v113, v11\n"
  "s_waitcnt lgkmcnt(4)\n"
  "v_mfma_f32_16x16x32_bf16 v[104:107], a[212:215], a[56:59], v[104:107]\n"
  "ds_read_b128 a[236:239], v19 offset:13312\n"
  "v_mfma_f32_16x16x32_bf16 v[108:111], a[212:215], a[60:63], v[108:111]\n"
  "buffer_load_dwordx2 v[94:95], v24, s[36:39], s87 offen offset:3616\n"
  "v_mfma_f32_16x16x32_bf16 v[104:107], a[216:219], a[64:67], v[104:107]\n"
  "ds_read_b128 a[240:243], v19 offset:14336\n"
  "v_mul_f32 v114, v114, v11\n"
  "v_mfma_f32_16x16x32_bf16 v[108:111], a[216:219], a[68:71], v[108:111]\n"
  "s_waitcnt lgkmcnt(4)\n"
  "v_mfma_f32_16x16x32_bf16 v[104:107], a[220:223], a[72:75], v[104:107]\n"
  "ds_read_b128 a[244:247], v19 offset:15360\n"
  "v_mul_f32 v115, v115, v11\n"
  "v_mfma_f32_16x16x32_bf16 v[108:111], a[220:223], a[76:79], v[108:111]\n"
  "s_barrier\n"
  "v_mfma_f32_16x16x32_bf16 v[104:107], a[224:227], a[80:83], v[104:107]\n"
  "ds_read_b128 a[248:251], v19 offset:16384\n"
  "v_mfma_f32_16x16x32_bf16 v[108:111], a[224:227], a[84:87], v[108:111]\n"
  "v_mul_f32 v116, v116, v12\n"
  "s_waitcnt lgkmcnt(4)\n"
  "v_mfma_f32_16x16x32_bf16 v[104:107], a[228:231], a[88:91], v[104:107]\n"
  "ds_read_b128 a[252:255], v19 offset:17408\n"
  "v_mfma_f32_16x16x32_bf16 v[108:111], a[228:231], a[92:95], v[108:111]\n"
  "v_mul_f32 v117, v117, v12\n"
  "v_mfma_f32_16x16x32_bf16 v[104:107], a[232:235], a[96:99], v[104:107]\n"
  "ds_read_b128 a[192:195], v19 offset:18432\n"
  "v_mfma_f32_16x16x32_bf16 v[108:111], a[232:235], a[100:103], v[108:111]\n"
  "v_mul_f32 v118, v118, v12\n"
  "s_waitcnt lgkmcnt(4)\n"
  "v_mfma_f32_16x16x32_bf16 v[104:107], a[236:239], a[104:107], v[104:107]\n"
  "ds_read_b128 a[196:199], v19 offset:19456\n"
  "v_mfma_f32_16x16x32_bf16 v[108:111], a[236:239], a[108:111], v[108:111]\n"
  "v_mul_f32 v119, v119, v12\n"
  "v_mfma_f32_16x16x32_bf16 v[104:107], a[240:243], a[112:115], v[104:107]\n"
  "ds_read_b128 a[200:203], v19 offset:20480\n"
  "v_mfma_f32_16x16x32_bf16 v[108:111], a[240:243], a[116:119], v[108:111]\n"
  "s_waitcnt lgkmcnt(4)\n"
  "v_mfma_f32_16x16x32_bf16 v[104:107], a[244:247], a[120:123], v[104:107]\n"
  "ds_read_b128 a[204:207], v19 offset:21504\n"
  "v_mfma_f32_16x16x32_bf16 v[108:111], a[244:247], a[124:127], v[108:111]\n"
  "v_mfma_f32_16x16x32_bf16 v[112:115], a[248:251], a[0:3], v[112:115]\n"
  "ds_read_b128 a[208:211], v19 offset:22528\n"
  "v_mfma_f32_16x16x32_bf16 v[116:119], a[248:251], a[4:7], v[116:119]\n"
  "s_waitcnt lgkmcnt(4)\n"
  "v_mfma_f32_16x16x32_bf16 v[112:115], a[252:255], a[8:11], v[112:115]\n"
  "ds_read_b128 a[212:215], v19 offset:23552\n"
  "v_cvt_pk_bf16_f32 v104, v104, v105\n"
  "v_cvt_pk_bf16_f32 v105, v106, v107\n"
  "v_mfma_f32_16x16x32_bf16 v[116:119], a[252:255], a[12:15], v[116:119]\n"
  "v_cvt_pk_bf16_f32 v106, v108, v109\n"
  "v_cvt_pk_bf16_f32 v107, v110, v111\n"
  "v_mfma_f32_16x16x32_bf16 v[112:115], a[192:195], a[16:19], v[112:115]\n"
  "ds_read_b128 a[216:219], v19 offset:24576\n"
  "v_mfma_f32_16x16x32_bf16 v[116:119], a[192:195], a[20:23], v[116:119]\n"
  "buffer_store_dwordx4 v[104:107], v17, s[60:63], s29 offen offset:1024 sc1\n"
  "s_waitcnt lgkmcnt(4)\n"
  "v_mfma_f32_16x16x32_bf16 v[112:115], a[196:199], a[24:27], v[112:115]\n"
  "ds_read_b128 a[220:223], v19 offset:25600\n"
  "v_mfma_f32_16x16x32_bf16 v[116:119], a[196:199], a[28:31], v[116:119]\n"
  "v_mfma_f32_16x16x32_bf16 v[112:115], a[200:203], a[32:35], v[112:115]\n"
  "ds_read_b128 a[224:227], v19 offset:26624\n"
  "s_lshl_b32 s32, s83, 14\n"
  "v_mfma_f32_16x16x32_bf16 v[116:119], a[200:203], a[36:39], v[116:119]\n"
  "s_add_u32 s32, s32, s33\n"
  "s_waitcnt lgkmcnt(4)\n"
  "v_mfma_f32_16x16x32_bf16 v[112:115], a[204:207], a[40:43], v[112:115]\n"
  "ds_read_b128 a[228:231], v19 offset:27648\n"
  "s_waitcnt vmcnt(12)\n"
  "s_mov_b64 exec, 1\n"
  "buffer_store_dword v20, off, s[60:63], s32 offset:12 sc1\n"
  "s_mov_b64 exec, -1\n"
  "v_mfma_f32_16x16x32_bf16 v[116:119], a[204:207], a[44:47], v[116:119]\n"
  "v_mfma_f32_16x16x32_bf16 v[112:115], a[208:211], a[48:51], v[112:115]\n"
  "ds_read_b128 a[232:235], v19 offset:28672\n"
  "v_mul_f32 v120, v120, v11\n"
  "v_mfma_f32_16x16x32_bf16 v[116:119], a[208:211], a[52:55], v[116:119]\n"
  "v_mul_f32 v121, v121, v11\n"
  "s_waitcnt lgkmcnt(4)\n"
  "v_mfma_f32_16x16x32_bf16 v[112:115], a[212:215], a[56:59], v[112:115]\n"
  "ds_read_b128 a[236:239], v19 offset:29696\n"
  "v_mul_f32 v122, v122, v11\n"
  "v_mfma_f32_16x16x32_bf16 v[116:119], a[212:215], a[60:63], v[116:119]\n"
  "v_mfma_f32_16x16x32_bf16 v[112:115], a[216:219], a[64:67], v[112:115]\n"
  "ds_read_b128 a[240:243], v19 offset:30720\n"
  "s_waitcnt vmcnt(7)\n"
  "v_cmp_ne_u32 vcc, v20, v21\n"
  "s_nop 0\n"
  "s_and_b32 s26, vcc_lo, 3\n"
  "s_cbranch_scc1 Lspo0%=\n"
  "Lbpo0%=:\n"
  "v_mfma_f32_16x16x32_bf16 v[116:119], a[216:219], a[68:71], v[116:119]\n"
  "s_add_u32 s28, s74, 0\n"
  "buffer_load_dwordx4 v[32:35], v17, s[60:63], s28 offen offset:0 sc1\n"
  "s_waitcnt lgkmcnt(4)\n"
  "v_mfma_f32_16x16x32_bf16 v[112:115], a[220:223], a[72:75], v[112:115]\n"
  "ds_read_b128 a[244:247], v19 offset:31744\n"
  "buffer_load_dwordx4 v[36:39], v17, s[60:63], s28 offen offset:1024 sc1\n"
  "v_mfma_f32_16x16x32_bf16 v[116:119], a[220:223], a[76:79], v[116:119]\n"
  "s_barrier\n"
  "s_add_u32 s28, s74, 8192\n"
  "buffer_load_dwordx4 v[40:43], v17, s[60:63], s28 offen offset:0 sc1\n"
  "v_mfma_f32_16x16x32_bf16 v[112:115], a[224:227], a[80:83], v[112:115]\n"
  "ds_read_b128 a[248:251], v18 offset:0\n"
  "buffer_load_dwordx4 v[44:47], v17, s[60:63], s28 offen offset:1024 sc1\n"
  "v_mfma_f32_16x16x32_bf16 v[116:119], a[224:227], a[84:87], v[116:119]\n"
  "v_mul_f32 v123, v123, v11\n"
  "s_waitcnt lgkmcnt(4)\n"
  "v_mfma_f32_16x16x32_bf16 v[112:115], a[228:231], a[88:91], v[112:115]\n"
  "ds_read_b128 a[252:255], v18 offset:1024\n"
  "v_mul_f32 v124, v124, v12\n"
  "v_mfma_f32_16x16x32_bf16 v[116:119], a[228:231], a[92:95], v[116:119]\n"
  "v_mul_f32 v125, v125, v12\n"
  "v_mfma_f32_16x16x32_bf16 v[112:115], a[232:235], a[96:99], v[112:115]\n"
  "ds_read_b128 a[192:195], v18 offset:2048\n"
  "v_mfma_f32_16x16x32_bf16 v[116:119], a[232:235], a[100:103], v[116:119]\n"
  "v_mul_f32 v126, v126, v12\n"
  "s_waitcnt lgkmcnt(4)\n"
  "v_mfma_f32_16x16x32_bf16 v[112:115], a[236:239], a[104:107], v[112:115]\n"
  "ds_read_b128 a[196:199], v18 offset:3072\n"
  "v_mul_f32 v127, v127, v12\n"
  "v_mfma_f32_16x16x32_bf16 v[116:119], a[236:239], a[108:111], v[116:119]\n"
  "s_mul_i32 s29, s85, 1835008\n"
  "v_mfma_f32_16x16x32_bf16 v[112:115], a[240:243], a[112:115], v[112:115]\n"
  "ds_read_b128 a[200:203], v18 offset:4096\n"
  "v_mfma_f32_16x16x32_bf16 v[116:119], a[240:243], a[116:119], v[116:119]\n"
  "s_add_u32 s29, s29, s73\n"
  "s_waitcnt lgkmcnt(4)\n"
  "v_mfma_f32_16x16x32_bf16 v[112:115], a[244:247], a[120:123], v[112:115]\n"
  "ds_read_b128 a[204:207], v18 offset:5120\n"
  "v_mfma_f32_16x16x32_bf16 v[116:119], a[244:247], a[124:127], v[116:119]\n"
  "s_add_u32 s29, s29, 40960\n"
  "v_mfma_f32_16x16x32_bf16 v[120:123], a[248:251], a[0:3], v[120:123]\n"
  "ds_read_b128 a[208:211], v18 offset:6144\n"
  "v_mfma_f32_16x16x32_bf16 v[124:127], a[248:251], a[4:7], v[124:127]\n"
  "s_waitcnt lgkmcnt(4)\n"
  "v_mfma_f32_16x16x32_bf16 v[120:123], a[252:255], a[8:11], v[120:123]\n"
  "ds_read_b128 a[212:215], v18 offset:7168\n"
  "v_cvt_pk_bf16_f32 v112, v112, v113\n"
  "v_cvt_pk_bf16_f32 v113, v114, v115\n"
  "v_mfma_f32_16x16x32_bf16 v[124:127], a[252:255], a[12:15], v[124:127]\n"
  "v_cvt_pk_bf16_f32 v114, v116, v117\n"
  "v_cvt_pk_bf16_f32 v115, v118, v119\n"
  "v_mfma_f32_16x16x32_bf16 v[120:123], a[192:195], a[16:19], v[120:123]\n"
  "ds_read_b128 a[216:219], v18 offset:8192\n"
  "v_mfma_f32_16x16x32_bf16 v[124:127], a[192:195], a[20:23], v[124:127]\n"
  "buffer_store_dwordx4 v[112:115], v17, s[60:63], s29 offen offset:0 sc1\n"
  "s_waitcnt lgkmcnt(4)\n"
  "v_mfma_f32_16x16x32_bf16 v[120:123], a[196:199], a[24:27], v[120:123]\n"
  "ds_read_b128 a[220:223], v18 offset:9216\n"
  "v_mfma_f32_16x16x32_bf16 v[124:127], a[196:199], a[28:31], v[124:127]\n"
  "v_mfma_f32_16x16x32_bf16 v[120:123], a[200:203], a[32:35], v[120:123]\n"
  "ds_read_b128 a[224:227], v18 offset:10240\n"
  "v_mfma_f32_16x16x32_bf16 v[124:127], a[200:203], a[36:39], v[124:127]\n"
  "s_waitcnt lgkmcnt(4)\n"
  "v_mfma_f32_16x16x32_bf16 v[120:123], a[204:207], a[40:43], v[120:123]\n"
  "ds_read_b128 a[228:231], v18 offset:11264\n"
  "v_mfma_f32_16x16x32_bf16 v[124:127], a[204:207], a[44:47], v[124:127]\n"
  "v_mfma_f32_16x16x32_bf16 v[120:123], a[208:211], a[48:51], v[120:123]\n"
  "ds_read_b128 a[232:235], v18 offset:12288\n"
  "v_mfma_f32_16x16x32_bf16 v[124:127], a[208:211], a[52:55], v[124:127]\n"
  "s_waitcnt lgkmcnt(4)\n"
  "v_mfma_f32_16x16x32_bf16 v[120:123], a[212:215], a[56:59], v[120:123]\n"
  "ds_read_b128 a[236:239], v18 offset:13312\n"
  "v_mfma_f32_16x16x32_bf16 v[124:127], a[212:215], a[60:63], v[124:127]\n"
  "v_mfma_f32_16x16x32_bf16 v[120:123], a[216:219], a[64:67], v[120:123]\n"
  "ds_read_b128 a[240:243], v18 offset:14336\n"
  "buffer_load_dword v21, v22, s[60:63], s79 offen sc1\n"
  "v_mfma_f32_16x16x32_bf16 v[124:127], a[216:219], a[68:71], v[124:127]\n"
  "s_waitcnt lgkmcnt(4)\n"
  "v_mfma_f32_16x16x32_bf16 v[120:123], a[220:223], a[72:75], v[120:123]\n"
  "ds_read_b128 a[244:247], v18 offset:15360\n"
  "v_mfma_f32_16x16x32_bf16 v[124:127], a[220:223], a[76:79], v[124:127]\n"
  "v_mfma_f32_16x16x32_bf16 v[120:123], a[224:227], a[80:83], v[120:123]\n"
  "v_mfma_f32_16x16x32_bf16 v[124:127], a[224:227], a[84:87], v[124:127]\n"
  "s_waitcnt lgkmcnt(3)\n"
  "v_mfma_f32_16x16x32_bf16 v[120:123], a[228:231], a[88:91], v[120:123]\n"
  "v_mfma_f32_16x16x32_bf16 v[124:127], a[228:231], a[92:95], v[124:127]\n"
  "v_mfma_f32_16x16x32_bf16 v[120:123], a[232:235], a[96:99], v[120:123]\n"
  "v_mfma_f32_16x16x32_bf16 v[124:127], a[232:235], a[100:103], v[124:127]\n"
  "s_waitcnt lgkmcnt(1)\n"
  "v_mfma_f32_16x16x32_bf16 v[120:123], a[236:239], a[104:107], v[120:123]\n"
  "v_mfma_f32_16x16x32_bf16 v[124:127], a[236:239], a[108:111], v[124:127]\n"
  "v_mfma_f32_16x16x32_bf16 v[120:123], a[240:243], a[112:115], v[120:123]\n"
  "v_mfma_f32_16x16x32_bf16 v[124:127], a[240:243], a[116:119], v[124:127]\n"
  "s_waitcnt lgkmcnt(0)\n"
  "v_mfma_f32_16x16x32_bf16 v[120:123], a[244:247], a[120:123], v[120:123]\n"
  "v_mfma_f32_16x16x32_bf16 v[124:127], a[244:247], a[124:127], v[124:127]\n"
  "v_mfma_f32_16x16x32_bf16 v[128:131], v[152:155], v[144:147], 0\n"
  "v_mfma_f32_16x16x32_bf16 v[132:135], v[152:155], v[148:151], 0\n"
  "v_mfma_f32_16x16x32_bf16 v[128:131], v[168:171], v[160:163], v[128:131]\n"
  "s_nop 3\n"
  "v_cvt_pk_bf16_f32 v120, v120, v121\n"
  "v_cvt_pk_bf16_f32 v121, v122, v123\n"
  "v_mfma_f32_16x16x32_bf16 v[132:135], v[168:171], v[164:167], v[132:135]\n"
  "v_cvt_pk_bf16_f32 v122, v124, v125\n"
  "v_cvt_pk_bf16_f32 v123, v126, v127\n"
  "v_mfma_f32_16x16x32_bf16 v[128:131], v[184:187], v[176:179], v[128:131]\n"
  "v_mfma_f32_16x16x32_bf16 v[132:135], v[184:187], v[180:183], v[132:135]\n"
  "buffer_store_dwordx4 v[120:123], v17, s[60:63], s29 offen offset:1024 sc1\n"
  "v_mfma_f32_16x16x32_bf16 v[128:131], v[200:203], v[192:195], v[128:131]\n"
  "v_mfma_f32_16x16x32_bf16 v[132:135], v[200:203], v[196:199], v[132:135]\n"
  "v_mfma_f32_16x16x32_bf16 v[128:131], v[216:219], v[208:211], v[128:131]\n"
  "s_lshl_b32 s32, s84, 14\n"
  "v_mfma_f32_16x16x32_bf16 v[132:135], v[216:219], v[212:215], v[132:135]\n"
  "s_add_u32 s32, s32, s33\n"
  "v_mfma_f32_16x16x32_bf16 v[128:131], v[232:235], v[224:227], v[128:131]\n"
  "s_waitcnt vmcnt(8)\n"
  "s_mov_b64 exec, 1\n"
  "buffer_store_dword v20, off, s[60:63], s32 offset:16 sc1\n"
  "s_mov_b64 exec, -1\n"
  "v_mfma_f32_16x16x32_bf16 v[132:135], v[232:235], v[228:231], v[132:135]\n"
  "v_mfma_f32_16x16x32_bf16 v[128:131], v[248:251], v[240:243], v[128:131]\n"
  "v_mfma_f32_16x16x32_bf16 v[132:135], v[248:251], v[244:247], v[132:135]\n"
  "v_mfma_f32_16x16x32_bf16 v[136:139], v[156:159], v[144:147], 0\n"
  "v_mfma_f32_16x16x32_bf16 v[140:143], v[156:159], v[148:151], 0\n"
  "s_barrier\n"
  "v_mfma_f32_16x16x32_bf16 v[136:139], v[172:175], v[160:163], v[136:139]\n"
  "ds_read_b128 a[248:251], v18 offset:16384\n"
  "v_mfma_f32_16x16x32_bf16 v[140:143], v[172:175], v[164:167], v[140:143]\n"
  "v_lshlrev_b32 v26, 16, v88\n"
  "v_and_b32 v27, 0xffff0000, v88\n"
  "v_mfma_f32_16x16x32_bf16 v[136:139], v[188:191], v[176:179], v[136:139]\n"
  "ds_read_b128 a[252:255], v18 offset:17408\n"
  "v_fma_f32 v128, v128, v11, v26\n"
  "v_fma_f32 v129, v129, v11, v27\n"
  "v_mfma_f32_16x16x32_bf16 v[140:143], v[188:191], v[180:183], v[140:143]\n"
  "v_lshlrev_b32 v26, 16, v89\n"
  "v_and_b32 v27, 0xffff0000, v89\n"
  "v_mfma_f32_16x16x32_bf16 v[136:139], v[204:207], v[192:195], v[136:139]\n"
  "ds_read_b128 a[192:195], v18 offset:18432\n"
  "v_fma_f32 v130, v130, v11, v26\n"
  "v_mfma_f32_16x16x32_bf16 v[140:143], v[204:207], v[196:199], v[140:143]\n"
  "v_fma_f32 v131, v131, v11, v27\n"
  "v_lshlrev_b32 v26, 16, v90\n"
  "v_mfma_f32_16x16x32_bf16 v[136:139], v[220:223], v[208:211], v[136:139]\n"
  "ds_read_b128 a[196:199], v18 offset:19456\n"
  "v_and_b32 v27, 0xffff0000, v90\n"
  "v_mfma_f32_16x16x32_bf16 v[140:143], v[220:223], v[212:215], v[140:143]\n"
  "v_fma_f32 v132, v132, v12, v26\n"
  "v_fma_f32 v133, v133, v12, v27\n"
  "v_mfma_f32_16x16x32_bf16 v[136:139], v[236:239], v[224:227], v[136:139]\n"
  "ds_read_b128 a[200:203], v18 offset:20480\n"
  "v_lshlrev_b32 v26, 16, v91\n"
  "v_mfma_f32_16x16x32_bf16 v[140:143], v[236:239], v[228:231], v[140:143]\n"
  "v_and_b32 v27, 0xffff0000, v91\n"
  "v_fma_f32 v134, v134, v12, v26\n"
  "v_mfma_f32_16x16x32_bf16 v[136:139], v[252:255], v[240:243], v[136:139]\n"
  "ds_read_b128 a[204:207], v18 offset:21504\n"
  "v_fma_f32 v135, v135, v12, v27\n"
  "v_mfma_f32_16x16x32_bf16 v[140:143], v[252:255], v[244:247], v[140:143]\n"
  "s_waitcnt lgkmcnt(4)\n"
  "v_mfma_f32_16x16x32_bf16 v[128:131], a[248:251], a[0:3], v[128:131]\n"
  "ds_read_b128 a[208:211], v18 offset:22528\n"
  "v_mfma_f32_16x16x32_bf16 v[132:135], a[248:251], a[4:7], v[132:135]\n"
  "v_mfma_f32_16x16x32_bf16 v[128:131], a[252:255], a[8:11], v[128:131]\n"
  "ds_read_b128 a[212:215], v18 offset:23552\n"
  "s_lshl_b32 s32, s85, 14\n"
  "v_mfma_f32_16x16x32_bf16 v[132:135], a[252:255], a[12:15], v[132:135]\n"
  "s_add_u32 s32, s32, s33\n"
  "s_waitcnt lgkmcnt(4)\n"
  "v_mfma_f32_16x16x32_bf16 v[128:131], a[192:195], a[16:19], v[128:131]\n"
  "ds_read_b128 a[216:219], v18 offset:24576\n"
  "s_waitcnt vmcnt(1)\n"
  "s_mov_b64 exec, 1\n"
  "buffer_store_dword v20, off, s[60:63], s32 offset:20 sc1\n"
  "s_mov_b64 exec, -1\n"
  "v_cmp_ne_u32 vcc, v20, v21\n"
  "s_nop 0\n"
  "s_and_b32 s26, vcc_lo, 12\n"
  "s_cbranch_scc1 Lspo1%=\n"
  "Lbpo1%=:\n"
  "v_mfma_f32_16x16x32_bf16 v[132:135], a[192:195], a[20:23], v[132:135]\n"
  "s_add_u32 s28, s74, 16384\n"
  "buffer_load_dwordx4 v[48:51], v17, s[60:63], s28 offen offset:0 sc1\n"
  "v_mfma_f32_16x16x32_bf16 v[128:131], a[196:199], a[24:27], v[128:131]\n"
  "ds_read_b128 a[220:223], v18 offset:25600\n"
  "buffer_load_dwordx4 v[52:55], v17, s[60:63], s28 offen offset:1024 sc1\n"
  "v_mfma_f32_16x16x32_bf16 v[132:135], a[196:199], a[28:31], v[132:135]\n"
  "s_add_u32 s28, s74, 24576\n"
  "buffer_load_dwordx4 v[56:59], v17, s[60:63], s28 offen offset:0 sc1\n"
  "s_waitcnt lgkmcnt(3)\n"
  "v_mfma_f32_16x16x32_bf16 v[128:131], a[200:203], a[32:35], v[128:131]\n"
  "ds_read_b128 a[224:227], v18 offset:26624\n"
  "buffer_load_dwordx4 v[60:63], v17, s[60:63], s28 offen offset:1024 sc1\n"
  "v_mfma_f32_16x16x32_bf16 v[132:135], a[200:203], a[36:39], v[132:135]\n"
  "v_lshlrev_b32 v26, 16, v92\n"
  "v_and_b32 v27, 0xffff0000, v92\n"
  "v_mfma_f32_16x16x32_bf16 v[128:131], a[204:207], a[40:43], v[128:131]\n"
  "ds_read_b128 a[228:231], v18 offset:27648\n"
  "v_fma_f32 v136, v136, v11, v26\n"
  "v_mfma_f32_16x16x32_bf16 v[132:135], a[204:207], a[44:47], v[132:135]\n"
  "v_fma_f32 v137, v137, v11, v27\n"
  "v_mfma_f32_16x16x32_bf16 v[128:131], a[208:211], a[48:51], v[128:131]\n"
  "ds_read_b128 a[232:235], v18 offset:28672\n"
  "v_lshlrev_b32 v26, 16, v93\n"
  "v_mfma_f32_16x16x32_bf16 v[132:135], a[208:211], a[52:55], v[132:135]\n"
  "v_and_b32 v27, 0xffff0000, v93\n"
  "v_fma_f32 v138, v138, v11, v26\n"
  "s_waitcnt lgkmcnt(4)\n"
  "v_mfma_f32_16x16x32_bf16 v[128:131], a[212:215], a[56:59], v[128:131]\n"
  "ds_read_b128 a[236:239], v18 offset:29696\n"
  "v_mfma_f32_16x16x32_bf16 v[132:135], a[212:215], a[60:63], v[132:135]\n"
  "v_fma_f32 v139, v139, v11, v27\n"
  "v_lshlrev_b32 v26, 16, v94\n"
  "v_mfma_f32_16x16x32_bf16 v[128:131], a[216:219], a[64:67], v[128:131]\n"
  "ds_read_b128 a[240:243], v18 offset:30720\n"
  "v_and_b32 v27, 0xffff0000, v94\n"
  "v_mfma_f32_16x16x32_bf16 v[132:135], a[216:219], a[68:71], v[132:135]\n"
  "v_fma_f32 v140, v140, v12, v26\n"
  "s_waitcnt lgkmcnt(4)\n"
  "v_mfma_f32_16x16x32_bf16 v[128:131], a[220:223], a[72:75], v[128:131]\n"
  "ds_read_b128 a[244:247], v18 offset:31744\n"
  "v_fma_f32 v141, v141, v12, v27\n"
  "v_mfma_f32_16x16x32_bf16 v[132:135], a[220:223], a[76:79], v[132:135]\n"
  "s_barrier\n"
  "v_mfma_f32_16x16x32_bf16 v[128:131], a[224:227], a[80:83], v[128:131]\n"
  "ds_read_b128 a[248:251], v18 offset:32768\n"
  "v_lshlrev_b32 v26, 16, v95\n"
  "v_mfma_f32_16x16x32_bf16 v[132:135], a[224:227], a[84:87], v[132:135]\n"
  "v_and_b32 v27, 0xffff0000, v95\n"
  "s_waitcnt lgkmcnt(4)\n"
  "v_mfma_f32_16x16x32_bf16 v[128:131], a[228:231], a[88:91], v[128:131]\n"
  "ds_read_b128 a[252:255], v18 offset:33792\n"
  "v_fma_f32 v142, v142, v12, v26\n"
  "v_mfma_f32_16x16x32_bf16 v[132:135], a[228:231], a[92:95], v[132:135]\n"
  "buffer_load_dword v21, v22, s[60:63], s79 offen sc1\n"
  "v_fma_f32 v143, v143, v12, v27\n"
  "v_mfma_f32_16x16x32_bf16 v[128:131], a[232:235], a[96:99], v[128:131]\n"
  "ds_read_b128 a[192:195], v18 offset:34816\n"
  "v_mfma_f32_16x16x32_bf16 v[132:135], a[232:235], a[100:103], v[132:135]\n"
  "s_waitcnt lgkmcnt(4)\n"
  "v_mfma_f32_16x16x32_bf16 v[128:131], a[236:239], a[104:107], v[128:131]\n"
  "ds_read_b128 a[196:199], v18 offset:35840\n"
  "v_mfma_f32_16x16x32_bf16 v[132:135], a[236:239], a[108:111], v[132:135]\n"
  "v_mfma_f32_16x16x32_bf16 v[128:131], a[240:243], a[112:115], v[128:131]\n"
  "ds_read_b128 a[200:203], v18 offset:36864\n"
  "v_mfma_f32_16x16x32_bf16 v[132:135], a[240:243], a[116:119], v[132:135]\n"
  "s_waitcnt lgkmcnt(4)\n"
  "v_mfma_f32_16x16x32_bf16 v[128:131], a[244:247], a[120:123], v[128:131]\n"
  "ds_read_b128 a[204:207], v18 offset:37888\n"
  "v_mfma_f32_16x16x32_bf16 v[132:135], a[244:247], a[124:127], v[132:135]\n"
  "v_mfma_f32_16x16x32_bf16 v[136:139], a[248:251], a[0:3], v[136:139]\n"
  "ds_read_b128 a[208:211], v18 offset:38912\n"
  "v_mfma_f32_16x16x32_bf16 v[140:143], a[248:251], a[4:7], v[140:143]\n"
  "s_waitcnt lgkmcnt(4)\n"
  "v_mfma_f32_16x16x32_bf16 v[136:139], a[252:255], a[8:11], v[136:139]\n"
  "ds_read_b128 a[212:215], v18 offset:39936\n"
  "v_mfma_f32_16x16x32_bf16 v[140:143], a[252:255], a[12:15], v[140:143]\n"
  "v_mfma_f32_16x16x32_bf16 v[136:139], a[192:195], a[16:19], v[136:139]\n"
  "ds_read_b128 a[216:219], v18 offset:40960\n"
  "v_mfma_f32_16x16x32_bf16 v[140:143], a[192:195], a[20:23], v[140:143]\n"
  "s_waitcnt lgkmcnt(4)\n"
  "v_mfma_f32_16x16x32_bf16 v[136:139], a[196:199], a[24:27], v[136:139]\n"
  "ds_read_b128 a[220:223], v18 offset:41984\n"
  "v_mfma_f32_16x16x32_bf16 v[140:143], a[196:199], a[28:31], v[140:143]\n"
  "v_mfma_f32_16x16x32_bf16 v[136:139], a[200:203], a[32:35], v[136:139]\n"
  "ds_read_b128 a[224:227], v18 offset:43008\n"
  "v_mfma_f32_16x16x32_bf16 v[140:143], a[200:203], a[36:39], v[140:143]\n"
  "s_waitcnt lgkmcnt(4)\n"
  "v_mfma_f32_16x16x32_bf16 v[136:139], a[204:207], a[40:43], v[136:139]\n"
  "ds_read_b128 a[228:231], v18 offset:44032\n"
  "v_mfma_f32_16x16x32_bf16 v[140:143], a[204:207], a[44:47], v[140:143]\n"
  "v_mfma_f32_16x16x32_bf16 v[136:139], a[208:211], a[48:51], v[136:139]\n"
  "ds_read_b128 a[232:235], v18 offset:45056\n"
  "v_mfma_f32_16x16x32_bf16 v[140:143], a[208:211], a[52:55], v[140:143]\n"
  "s_waitcnt lgkmcnt(4)\n"
  "v_mfma_f32_16x16x32_bf16 v[136:139], a[212:215], a[56:59], v[136:139]\n"
  "ds_read_b128 a[236:239], v18 offset:46080\n"
  "v_mfma_f32_16x16x32_bf16 v[140:143], a[212:215], a[60:63], v[140:143]\n"
  "v_mfma_f32_16x16x32_bf16 v[136:139], a[216:219], a[64:67], v[136:139]\n"
  "ds_read_b128 a[240:243], v18 offset:47104\n"
  "v_mfma_f32_16x16x32_bf16 v[140:143], a[216:219], a[68:71], v[140:143]\n"
  "s_waitcnt vmcnt(0)\n"
  "v_cmp_ne_u32 vcc, v20, v21\n"
  "s_nop 0\n"
  "s_and_b32 s26, vcc_lo, 112\n"
  "s_cbranch_scc1 Lspo2%=\n"
  "Lbpo2%=:\n"
  "s_waitcnt lgkmcnt(4)\n"
  "v_mfma_f32_16x16x32_bf16 v[136:139], a[220:223], a[72:75], v[136:139]\n"
  "ds_read_b128 a[244:247], v18 offset:48128\n"
  "s_add_u32 s28, s74, 32768\n"
  "buffer_load_dwordx4 v[64:67], v17, s[60:63], s28 offen offset:0 sc1\n"
  "v_mfma_f32_16x16x32_bf16 v[140:143], a[220:223], a[76:79], v[140:143]\n"
  "buffer_load_dwordx4 v[68:71], v17, s[60:63], s28 offen offset:1024 sc1\n"
  "v_mfma_f32_16x16x32_bf16 v[136:139], a[224:227], a[80:83], v[136:139]\n"
  "s_add_u32 s28, s74, 49152\n"
  "buffer_load_dwordx4 v[80:83], v17, s[60:63], s28 offen offset:0 sc1\n"
  "v_mfma_f32_16x16x32_bf16 v[140:143], a[224:227], a[84:87], v[140:143]\n"
  "buffer_load_dwordx4 v[84:87], v17, s[60:63], s28 offen offset:1024 sc1\n"
  "s_waitcnt lgkmcnt(3)\n"
  "v_mfma_f32_16x16x32_bf16 v[136:139], a[228:231], a[88:91], v[136:139]\n"
  "s_add_u32 s28, s74, 40960\n"
  "buffer_load_dwordx4 v[72:75], v17, s[60:63], s28 offen offset:0 sc1\n"
  "v_mfma_f32_16x16x32_bf16 v[140:143], a[228:231], a[92:95], v[140:143]\n"
  "buffer_load_dwordx4 v[76:79], v17, s[60:63], s28 offen offset:1024 sc1\n"
  "v_mfma_f32_16x16x32_bf16 v[136:139], a[232:235], a[96:99], v[136:139]\n"
  "v_mfma_f32_16x16x32_bf16 v[140:143], a[232:235], a[100:103], v[140:143]\n"
  "s_waitcnt lgkmcnt(1)\n"
  "v_mfma_f32_16x16x32_bf16 v[136:139], a[236:239], a[104:107], v[136:139]\n"
  "v_mfma_f32_16x16x32_bf16 v[140:143], a[236:239], a[108:111], v[140:143]\n"
  "v_mfma_f32_16x16x32_bf16 v[136:139], a[240:243], a[112:115], v[136:139]\n"
  "v_mfma_f32_16x16x32_bf16 v[140:143], a[240:243], a[116:119], v[140:143]\n"
  "s_waitcnt lgkmcnt(0)\n"
  "v_mfma_f32_16x16x32_bf16 v[136:139], a[244:247], a[120:123], v[136:139]\n"
  "v_mfma_f32_16x16x32_bf16 v[140:143], a[244:247], a[124:127], v[140:143]\n"
  "v_mfma_f32_16x16x32_bf16 v[128:131], a[186:189], v[32:35], v[128:131]\n"
  "v_mfma_f32_16x16x32_bf16 v[132:135], a[184:187], v[32:35], v[132:135]\n"
  "v_mfma_f32_16x16x32_bf16 v[136:139], a[186:189], v[36:39], v[136:139]\n"
  "v_mfma_f32_16x16x32_bf16 v[140:143], a[184:187], v[36:39], v[140:143]\n"
  "v_mfma_f32_16x16x32_bf16 v[128:131], a[186:189], v[40:43], v[128:131]\n"
  "v_mfma_f32_16x16x32_bf16 v[132:135], a[184:187], v[40:43], v[132:135]\n"
  "v_mfma_f32_16x16x32_bf16 v[136:139], a[186:189], v[44:47], v[136:139]\n"
  "v_mfma_f32_16x16x32_bf16 v[140:143], a[184:187], v[44:47], v[140:143]\n"
  "v_mfma_f32_16x16x32_bf16 v[128:131], a[186:189], v[48:51], v[128:131]\n"
  "v_mfma_f32_16x16x32_bf16 v[132:135], a[184:187], v[48:51], v[132:135]\n"
  "v_mfma_f32_16x16x32_bf16 v[136:139], a[186:189], v[52:55], v[136:139]\n"
  "v_mfma_f32_16x16x32_bf16 v[140:143], a[184:187], v[52:55], v[140:143]\n"
  "v_mfma_f32_16x16x32_bf16 v[128:131], a[186:189], v[56:59], v[128:131]\n"
  "v_mfma_f32_16x16x32_bf16 v[132:135], a[184:187], v[56:59], v[132:135]\n"
  "v_mfma_f32_16x16x32_bf16 v[136:139], a[186:189], v[60:63], v[136:139]\n"
  "v_mfma_f32_16x16x32_bf16 v[140:143], a[184:187], v[60:63], v[140:143]\n"
  "s_waitcnt vmcnt(5)\n"
  "v_mfma_f32_16x16x32_bf16 v[128:131], a[186:189], v[64:67], v[128:131]\n"
  "v_mfma_f32_16x16x32_bf16 v[132:135], a[184:187], v[64:67], v[132:135]\n"
  "s_waitcnt vmcnt(4)\n"
  "v_mfma_f32_16x16x32_bf16 v[136:139], a[186:189], v[68:71], v[136:139]\n"
  "v_mfma_f32_16x16x32_bf16 v[140:143], a[184:187], v[68:71], v[140:143]\n"
  "s_waitcnt vmcnt(3)\n"
  "v_mfma_f32_16x16x32_bf16 v[128:131], a[186:189], v[80:83], v[128:131]\n"
  "v_mfma_f32_16x16x32_bf16 v[132:135], a[184:187], v[80:83], v[132:135]\n"
  "s_waitcnt vmcnt(2)\n"
  "v_mfma_f32_16x16x32_bf16 v[136:139], a[186:189], v[84:87], v[136:139]\n"
  "v_mfma_f32_16x16x32_bf16 v[140:143], a[184:187], v[84:87], v[140:143]\n"
  "s_waitcnt vmcnt(1)\n"
  "v_mfma_f32_16x16x32_bf16 v[128:131], a[186:189], v[72:75], v[128:131]\n"
  "v_mfma_f32_16x16x32_bf16 v[132:135], a[184:187], v[72:75], v[132:135]\n"
  "s_waitcnt vmcnt(0)\n"
  "v_mfma_f32_16x16x32_bf16 v[136:139], a[186:189], v[76:79], v[136:139]\n"
  "v_mfma_f32_16x16x32_bf16 v[140:143], a[184:187], v[76:79], v[140:143]\n"
  "s_nop 3\n"
  "v_cvt_pk_bf16_f32 v88, v128, v129\n"
  "v_cvt_pk_bf16_f32 v89, v130, v131\n"
  "s_nop 0\n"
  "v_cvt_pk_bf16_f32 v90, v136, v137\n"
  "v_cvt_pk_bf16_f32 v91, v138, v139\n"
  "v_cvt_pk_bf16_f32 v92, v132, v133\n"
  "v_cvt_pk_bf16_f32 v93, v134, v135\n"
  "v_cvt_pk_bf16_f32 v94, v140, v141\n"
  "v_cvt_pk_bf16_f32 v95, v142, v143\n"
  "v_permlane16_swap_b32 v88, v90\n"
  "v_permlane16_swap_b32 v89, v91\n"
  "v_permlane16_swap_b32 v92, v94\n"
  "v_permlane16_swap_b32 v93, v95\n"
  "buffer_store_dwordx4 v[88:91], v25, s[64:67], s87 offen offset:0\n"
  "buffer_store_dwordx4 v[92:95], v25, s[64:67], s87 offen offset:3584\n"
  "s_branch Lend%=\n"
  "Lsabo%=:\n"
  "s_mov_b32 s75, 0\n"
  "Llabo%=:\n"
  "s_sleep 1\n"
  "buffer_load_dword v21, v22, s[60:63], s87 offen sc1\n"
  "s_waitcnt vmcnt(0)\n"
  "v_cmp_ne_u32 vcc, v20, v21\n"
  "s_nop 1\n"
  "s_and_b64 s[26:27], vcc, exec\n"
  "s_cbranch_scc0 Lbabo%=\n"
  "s_add_u32 s75, s75, 1\n"
  "s_cmp_lt_u32 s75, 0x8000\n"
  "s_cbranch_scc1 Llabo%=\n"
  "s_branch Lbabo%=\n"
  "Lspo0%=:\n"
  "s_mov_b32 s75, 0\n"
  "Llpo0%=:\n"
  "s_sleep 1\n"
  "buffer_load_dword v21, v22, s[60:63], s79 offen sc1\n"
  "s_waitcnt vmcnt(0)\n"
  "v_cmp_ne_u32 vcc, v20, v21\n"
  "s_nop 1\n"
  "s_and_b32 s26, vcc_lo, 3\n"
  "s_cbranch_scc0 Lbpo0%=\n"
  "s_add_u32 s75, s75, 1\n"
  "s_cmp_lt_u32 s75, 0x8000\n"
  "s_cbranch_scc1 Llpo0%=\n"
  "s_branch Lbpo0%=\n"
  "Lspo1%=:\n"
  "s_mov_b32 s75, 0\n"
  "Llpo1%=:\n"
  "s_sleep 1\n"
  "buffer_load_dword v21, v22, s[60:63], s79 offen sc1\n"
  "s_waitcnt vmcnt(0)\n"
  "v_cmp_ne_u32 vcc, v20, v21\n"
  "s_nop 1\n"
  "s_and_b32 s26, vcc_lo, 12\n"
  "s_cbranch_scc0 Lbpo1%=\n"
  "s_add_u32 s75, s75, 1\n"
  "s_cmp_lt_u32 s75, 0x8000\n"
  "s_cbranch_scc1 Llpo1%=\n"
  "s_branch Lbpo1%=\n"
  "Lspo2%=:\n"
  "s_mov_b32 s75, 0\n"
  "Llpo2%=:\n"
  "s_sleep 1\n"
  "buffer_load_dword v21, v22, s[60:63], s79 offen sc1\n"
  "s_waitcnt vmcnt(0)\n"
  "v_cmp_ne_u32 vcc, v20, v21\n"
  "s_nop 1\n"
  "s_and_b32 s26, vcc_lo, 112\n"
  "s_cbranch_scc0 Lbpo2%=\n"
  "s_add_u32 s75, s75, 1\n"
  "s_cmp_lt_u32 s75, 0x8000\n"
  "s_cbranch_scc1 Llpo2%=\n"
  "s_branch Lbpo2%=\n"
  "Lsabn%=:\n"
  "s_mov_b32 s75, 0\n"
  "Llabn%=:\n"
  "s_sleep 1\n"
  "buffer_load_dword v21, v22, s[60:63], s87 offen sc1\n"
  "s_waitcnt vmcnt(0)\n"
  "v_cmp_ne_u32 vcc, v20, v21\n"
  "s_nop 1\n"
  "s_and_b64 s[26:27], vcc, exec\n"
  "s_cbranch_scc0 Lbabn%=\n"
  "s_add_u32 s75, s75, 1\n"
  "s_cmp_lt_u32 s75, 0x8000\n"
  "s_cbranch_scc1 Llabn%=\n"
  "s_branch Lbabn%=\n"
  "Lnon%=:\n"
  "v_add_u32 v30, -49152, v30\n"
  "v_add_u32 v31, -49152, v31\n"
  "s_add_u32 m0, s34, 12288\n"
  "s_mov_b32 s30, 384\n"
  "buffer_load_dwordx4 v2, s[36:39], s30 offen offset:0 lds\n"
  "s_mov_b32 s30, 13696\n"
  "buffer_load_dwordx4 v2, s[36:39], s30 offen offset:1024 lds\n"
  "s_mov_b32 s30, 1920\n"
  "buffer_load_dwordx4 v2, s[36:39], s30 offen offset:2048 lds\n"
  "s_mov_b32 s30, 15232\n"
  "buffer_load_dwordx4 v2, s[36:39], s30 offen offset:3072 lds\n"
  "v_lshl_add_u32 v24, v24, 4, s70\n"
  "v_lshrrev_b32 v26, 2, v25\n"
  "v_and_b32 v25, 3, v25\n"
  "v_lshl_add_u32 v25, v26, 4, v25\n"
  "v_add_u32 v25, s31, v25\n"
  "v_mad_u32_u24 v25, v25, s26, v24\n"
  "v_subrev_u32 v25, 2048, v25\n"
  "v_accvgpr_write_b32 a178, v25\n"
  "v_add_u32 v24, 192, v1\n"
  "v_mul_u32_u24 v25, 0x925, v24\n"
  "v_lshrrev_b32 v25, 16, v25\n"
  "v_mul_u32_u24 v26, 28, v25\n"
  "v_sub_u32 v24, v24, v26\n"
  "v_xor_b32 v24, s28, v24\n"
  "v_add_u32 v26, s27, v25\n"
  "v_lshlrev_b32 v27, 4, v24\n"
  "v_mad_u32_u24 v26, v26, s99, v27\n"
  "v_subrev_u32 v8, 3072, v26\n"
  "v_add_u32 v24, s29, v24\n"
  "v_subrev_u32 v26, 28, v24\n"
  "v_min_u32 v24, v24, v26\n"
  "v_lshl_add_u32 v24, v24, 4, s70\n"
  "v_lshrrev_b32 v26, 2, v25\n"
  "v_and_b32 v25, 3, v25\n"
  "v_lshl_add_u32 v25, v26, 4, v25\n"
  "v_add_u32 v25, s31, v25\n"
  "v_mad_u32_u24 v25, v25, s26, v24\n"
  "v_subrev_u32 v25, 3072, v25\n"
  "v_accvgpr_write_b32 a179, v25\n"
  "v_lshrrev_b32 v25, 2, v28\n"
  "v_lshlrev_b32 v24, 7, v28\n"
  "v_and_b32 v24, 0x180, v24\n"
  "v_mad_u32_u24 v27, v25, s99, v24\n"
  "v_lshlrev_b32 v23, 7, v28\n"
  "s_lshl_b32 s70, s22, 31\n"
  "s_mov_b32 s80, s98\n"
  "s_add_u32 s81, s80, 1\n"
  "s_cmp_eq_u32 s81, 7\n"
  "s_cselect_b32 s81, 0, s81\n"
  "s_add_u32 s82, s81, 1\n"
  "s_cmp_eq_u32 s82, 7\n"
  "s_cselect_b32 s82, 0, s82\n"
  "s_add_u32 s83, s82, 1\n"
  "s_cmp_eq_u32 s83, 7\n"
  "s_cselect_b32 s83, 0, s83\n"
  "s_add_u32 s84, s83, 1\n"
  "s_cmp_eq_u32 s84, 7\n"
  "s_cselect_b32 s84, 0, s84\n"
  "s_add_u32 s85, s84, 1\n"
  "s_cmp_eq_u32 s85, 7\n"
  "s_cselect_b32 s85, 0, s85\n"
  "s_add_u32 s86, s85, 1\n"
  "s_cmp_eq_u32 s86, 7\n"
  "s_cselect_b32 s86, 0, s86\n"
  "v_lshrrev_b32 v24, 4, v1\n"
  "v_bfe_u32 v25, v1, 2, 2\n"
  "v_xor_b32 v24, v24, v25\n"
  "v_min_u32 v24, 1, v24\n"
  "v_add_u32 v24, -1, v24\n"
  "v_and_b32 v24, 0x3f80, v24\n"
  "v_and_b32 v25, 1, v1\n"
  "v_lshlrev_b32 v25, 4, v25\n"
  "v_lshlrev_b32 v24, v25, v24\n"
  "v_bfe_i32 v25, v1, 1, 1\n"
  "v_and_b32 v25, v25, v24\n"
  "v_xor_b32 v24, v25, v24\n"
  "v_accvgpr_write_b32 a184, 0\n"
  "v_accvgpr_write_b32 a185, 0\n"
  "v_accvgpr_write_b32 a186, v24\n"
  "v_accvgpr_write_b32 a187, v25\n"
  "v_accvgpr_write_b32 a188, 0\n"
  "v_accvgpr_write_b32 a189, 0\n"
  "s_add_u32 s28, s69, 57344\n"
  "v_add_u32 v164, s28, v17\n"
  "v_and_b32 v24, 15, v1\n"
  "v_and_b32 v25, 12, v1\n"
  "v_lshl_add_u32 v25, v25, 2, v24\n"
  "v_and_b32 v26, 3, v1\n"
  "v_lshl_add_u32 v25, v25, 2, v26\n"
  "v_lshl_add_u32 v165, v25, 2, s28\n"
  "s_lshl_b32 s28, s24, 9\n"
  "s_add_u32 s28, s28, 145408\n"
  "v_lshl_add_u32 v22, v24, 2, s28\n"
  "v_and_b32 v25, 48, v1\n"
  "v_add_u32 v167, s28, v25\n"
  "v_lshlrev_b32 v24, 2, v24\n"
  "v_add_u32 v168, 145408, v24\n"
  "v_accvgpr_write_b32 a160, v167\n"
  "v_accvgpr_write_b32 a161, v168\n"
  "s_mov_b64 exec, 0\n"
  "s_cmp_eq_u32 s24, 1\n"
  "s_cbranch_scc1 Ltn1%=\n"
  "s_cmp_eq_u32 s24, 2\n"
  "s_cbranch_scc1 Ltn2%=\n"
  "s_cmp_eq_u32 s24, 3\n"
  "s_cbranch_scc1 Ltn3%=\n"
  "s_branch Ltn0%=\n"
  "Ltnr0%=:\n"
  "s_mov_b64 exec, -1\n"
  "s_waitcnt lgkmcnt(0)\n"
  "s_mul_i32 s25, s25, 0x9E3779B1\n"
  "s_add_u32 s25, s25, 0x7F4A7C15\n"
  "v_mov_b32 v20, s25\n"
  "s_waitcnt vmcnt(12)\n"
  "ds_read_b128 v[32:35], v9 offset:0\n"
  "ds_read_b128 v[36:39], v9 offset:2048\n"
  "ds_read_b128 v[40:43], v10 offset:0\n"
  "ds_read_b128 v[44:47], v10 offset:2048\n"
  "s_waitcnt vmcnt(8)\n"
  "ds_read_b128 v[48:51], v9 offset:4096\n"
  "ds_read_b128 v[52:55], v9 offset:6144\n"
  "s_add_u32 m0, s68, 32768\n"
  "s_mov_b32 s30, 512\n"
  "buffer_load_dwordx4 v2, s[36:39], s30 offen offset:0 lds\n"
  "s_mov_b32 s30, 13824\n"
  "buffer_load_dwordx4 v2, s[36:39], s30 offen offset:1024 lds\n"
  "s_mov_b32 s30, 2048\n"
  "buffer_load_dwordx4 v2, s[36:39], s30 offen offset:2048 lds\n"
  "s_mov_b32 s30, 15360\n"
  "buffer_load_dwordx4 v2, s[36:39], s30 offen offset:3072 lds\n"
  "s_waitcnt lgkmcnt(5)\n"
  "v_mfma_f32_16x16x32_bf16 v[144:147], a[180:183], v[32:35], 0\n"
  "ds_read_b128 v[56:59], v10 offset:4096\n"
  "s_add_u32 m0, s35, 65536\n"
  "s_mov_b32 s31, 0\n"
  "buffer_load_dwordx4 v5, s[44:47], s31 offen offset:0 lds\n"
  "buffer_load_dwordx4 v6, s[44:47], s31 offen offset:1024 lds\n"
  "v_mfma_f32_16x16x32_bf16 v[152:155], v[32:35], v[32:35], 0\n"
  "buffer_load_dwordx4 v7, s[44:47], s31 offen offset:2048 lds\n"
  "s_waitcnt lgkmcnt(5)\n"
  "v_mfma_f32_16x16x32_bf16 v[148:151], a[180:183], v[36:39], 0\n"
  "s_mov_b32 exec_hi, 0\n"
  "buffer_load_dwordx4 v8, s[44:47], s31 offen offset:3072 lds\n"
  "s_mov_b32 exec_hi, -1\n"
  "v_mfma_f32_16x16x32_bf16 v[156:159], v[36:39], v[36:39], 0\n"
  "ds_read_b128 v[60:63], v10 offset:6144\n"
  "s_waitcnt vmcnt(12)\n"
  "s_add_u32 m0, s34, 0\n"
  "s_mov_b32 s30, 640\n"
  "s_waitcnt lgkmcnt(5)\n"
  "v_mfma_f32_16x16x32_bf16 v[144:147], a[180:183], v[40:43], v[144:147]\n"
  "ds_read_b128 v[64:67], v9 offset:8192\n"
  "s_waitcnt lgkmcnt(4)\n"
  "buffer_load_dwordx4 v2, s[36:39], s30 offen offset:0 lds\n"
  "v_mfma_f32_16x16x32_bf16 v[152:155], v[40:43], v[40:43], v[152:155]\n"
  "s_mov_b32 s30, 13952\n"
  "buffer_load_dwordx4 v2, s[36:39], s30 offen offset:1024 lds\n"
  "v_mfma_f32_16x16x32_bf16 v[148:151], a[180:183], v[44:47], v[148:151]\n"
  "ds_read_b128 v[68:71], v9 offset:10240\n"
  "s_mov_b32 s30, 2176\n"
  "v_mfma_f32_16x16x32_bf16 v[156:159], v[44:47], v[44:47], v[156:159]\n"
  "buffer_load_dwordx4 v2, s[36:39], s30 offen offset:2048 lds\n"
  "v_mfma_f32_16x16x32_bf16 v[144:147], a[180:183], v[48:51], v[144:147]\n"
  "ds_read_b128 v[72:75], v10 offset:8192\n"
  "s_mov_b32 s30, 15488\n"
  "v_mfma_f32_16x16x32_bf16 v[152:155], v[48:51], v[48:51], v[152:155]\n"
  "buffer_load_dwordx4 v2, s[36:39], s30 offen offset:3072 lds\n"
  "s_waitcnt lgkmcnt(4)\n"
  "v_mfma_f32_16x16x32_bf16 v[148:151], a[180:183], v[52:55], v[148:151]\n"
  "ds_read_b128 v[76:79], v10 offset:10240\n"
  "v_mfma_f32_16x16x32_bf16 v[156:159], v[52:55], v[52:55], v[156:159]\n"
  "s_waitcnt vmcnt(12)\n"
  "s_add_u32 m0, s34, 4096\n"
  "s_mov_b32 s30, 768\n"
  "v_mfma_f32_16x16x32_bf16 v[144:147], a[180:183], v[56:59], v[144:147]\n"
  "ds_read_b128 v[80:83], v9 offset:12288\n"
  "s_waitcnt lgkmcnt(4)\n"
  "buffer_load_dwordx4 v2, s[36:39], s30 offen offset:0 lds\n"
  "v_mfma_f32_16x16x32_bf16 v[152:155], v[56:59], v[56:59], v[152:155]\n"
  "s_mov_b32 s30, 14080\n"
  "buffer_load_dwordx4 v2, s[36:39], s30 offen offset:1024 lds\n"
  "v_mfma_f32_16x16x32_bf16 v[148:151], a[180:183], v[60:63], v[148:151]\n"
  "ds_read_b128 v[84:87], v9 offset:14336\n"
  "s_mov_b32 s30, 2304\n"
  "v_mfma_f32_16x16x32_bf16 v[156:159], v[60:63], v[60:63], v[156:159]\n"
  "buffer_load_dwordx4 v2, s[36:39], s30 offen offset:2048 lds\n"
  "v_mfma_f32_16x16x32_bf16 v[144:147], a[180:183], v[64:67], v[144:147]\n"
  "ds_read_b128 v[88:91], v10 offset:12288\n"
  "s_mov_b32 s30, 15616\n"
  "v_mfma_f32_16x16x32_bf16 v[152:155], v[64:67], v[64:67], v[152:155]\n"
  "buffer_load_dwordx4 v2, s[36:39], s30 offen offset:3072 lds\n"
  "s_waitcnt lgkmcnt(4)\n"
  "v_mfma_f32_16x16x32_bf16 v[148:151], a[180:183], v[68:71], v[148:151]\n"
  "ds_read_b128 v[92:95], v10 offset:14336\n"
  "v_mfma_f32_16x16x32_bf16 v[156:159], v[68:71], v[68:71], v[156:159]\n"
  "s_waitcnt vmcnt(12)\n"
  "s_add_u32 m0, s35, 81920\n"
  "s_mov_b32 s31, 114688\n"
  "v_mfma_f32_16x16x32_bf16 v[144:147], a[180:183], v[72:75], v[144:147]\n"
  "buffer_load_dwordx4 v5, s[44:47], s31 offen offset:0 lds\n"
  "v_mfma_f32_16x16x32_bf16 v[152:155], v[72:75], v[72:75], v[152:155]\n"
  "ds_read_b128 v[96:99], v30 offset:0\n"
  "s_waitcnt lgkmcnt(4)\n"
  "v_mfma_f32_16x16x32_bf16 v[148:151], a[180:183], v[76:79], v[148:151]\n"
  "ds_read_b128 v[100:103], v30 offset:2048\n"
  "v_mfma_f32_16x16x32_bf16 v[156:159], v[76:79], v[76:79], v[156:159]\n"
  "buffer_load_dwordx4 v6, s[44:47], s31 offen offset:1024 lds\n"
  "v_mfma_f32_16x16x32_bf16 v[144:147], a[180:183], v[80:83], v[144:147]\n"
  "ds_read_b128 v[104:107], v31 offset:0\n"
  "v_mfma_f32_16x16x32_bf16 v[152:155], v[80:83], v[80:83], v[152:155]\n"
  "s_waitcnt lgkmcnt(4)\n"
  "v_mfma_f32_16x16x32_bf16 v[148:151], a[180:183], v[84:87], v[148:151]\n"
  "buffer_load_dwordx4 v7, s[44:47], s31 offen offset:2048 lds\n"
  "v_mfma_f32_16x16x32_bf16 v[156:159], v[84:87], v[84:87], v[156:159]\n"
  "ds_read_b128 v[108:111], v31 offset:2048\n"
  "s_waitcnt vmcnt(7)\n"
  "v_mfma_f32_16x16x32_bf16 v[144:147], a[180:183], v[88:91], v[144:147]\n"
  "ds_read_b128 v[112:115], v9 offset:0\n"
  "v_mfma_f32_16x16x32_bf16 v[152:155], v[88:91], v[88:91], v[152:155]\n"
  "s_mov_b32 exec_hi, 0\n"
  "buffer_load_dwordx4 v8, s[44:47], s31 offen offset:3072 lds\n"
  "s_mov_b32 exec_hi, -1\n"
  "s_waitcnt lgkmcnt(4)\n"
  "v_mfma_f32_16x16x32_bf16 v[148:151], a[180:183], v[92:95], v[148:151]\n"
  "ds_read_b128 v[116:119], v9 offset:2048\n"
  "v_mfma_f32_16x16x32_bf16 v[156:159], v[92:95], v[92:95], v[156:159]\n"
  "s_add_u32 m0, s35, 0\n"
  "s_mov_b32 s31, 229376\n"
  "v_mfma_f32_16x16x32_bf16 v[144:147], a[180:183], v[96:99], v[144:147]\n"
  "buffer_load_dwordx4 v5, s[44:47], s31 offen offset:0 lds\n"
  "v_mfma_f32_16x16x32_bf16 v[152:155], v[96:99], v[96:99], v[152:155]\n"
  "ds_read_b128 v[120:123], v10 offset:0\n"
  "s_waitcnt lgkmcnt(4)\n"
  "v_mfma_f32_16x16x32_bf16 v[148:151], a[180:183], v[100:103], v[148:151]\n"
  "ds_read_b128 v[124:127], v10 offset:2048\n"
  "v_mfma_f32_16x16x32_bf16 v[156:159], v[100:103], v[100:103], v[156:159]\n"
  "s_waitcnt vmcnt(5)\n"
  "buffer_load_dwordx4 v6, s[44:47], s31 offen offset:1024 lds\n"
  "v_mfma_f32_16x16x32_bf16 v[144:147], a[180:183], v[104:107], v[144:147]\n"
  "ds_read_b128 v[128:131], v9 offset:4096\n"
  "v_mfma_f32_16x16x32_bf16 v[152:155], v[104:107], v[104:107], v[152:155]\n"
  "s_waitcnt lgkmcnt(4)\n"
  "v_mfma_f32_16x16x32_bf16 v[148:151], a[180:183], v[108:111], v[148:151]\n"
  "buffer_load_dwordx4 v7, s[44:47], s31 offen offset:2048 lds\n"
  "v_mfma_f32_16x16x32_bf16 v[156:159], v[108:111], v[108:111], v[156:159]\n"
  "ds_read_b128 v[132:135], v9 offset:6144\n"
  "v_mfma_f32_16x16x32_bf16 v[144:147], a[180:183], v[112:115], v[144:147]\n"
  "ds_read_b128 v[136:139], v10 offset:4096\n"
  "v_mfma_f32_16x16x32_bf16 v[152:155], v[112:115], v[112:115], v[152:155]\n"
  "s_mov_b32 exec_hi, 0\n"
  "buffer_load_dwordx4 v8, s[44:47], s31 offen offset:3072 lds\n"
  "s_mov_b32 exec_hi, -1\n"
  "s_waitcnt lgkmcnt(4)\n"
  "v_mfma_f32_16x16x32_bf16 v[148:151], a[180:183], v[116:119], v[148:151]\n"
  "ds_read_b128 v[140:143], v10 offset:6144\n"
  "v_mfma_f32_16x16x32_bf16 v[156:159], v[116:119], v[116:119], v[156:159]\n"
  "s_add_u32 m0, s35, 16384\n"
  "s_mov_b32 s31, 344064\n"
  "v_mfma_f32_16x16x32_bf16 v[144:147], a[180:183], v[120:123], v[144:147]\n"
  "buffer_load_dwordx4 v5, s[44:47], s31 offen offset:0 lds\n"
  "v_mfma_f32_16x16x32_bf16 v[152:155], v[120:123], v[120:123], v[152:155]\n"
  "s_waitcnt lgkmcnt(3)\n"
  "v_mfma_f32_16x16x32_bf16 v[148:151], a[180:183], v[124:127], v[148:151]\n"
  "v_mfma_f32_16x16x32_bf16 v[156:159], v[124:127], v[124:127], v[156:159]\n"
  "buffer_load_dwordx4 v6, s[44:47], s31 offen offset:1024 lds\n"
  "v_mfma_f32_16x16x32_bf16 v[144:147], a[180:183], v[128:131], v[144:147]\n"
  "v_mfma_f32_16x16x32_bf16 v[152:155], v[128:131], v[128:131], v[152:155]\n"
  "s_waitcnt lgkmcnt(1)\n"
  "v_mfma_f32_16x16x32_bf16 v[148:151], a[180:183], v[132:135], v[148:151]\n"
  "buffer_load_dwordx4 v7, s[44:47], s31 offen offset:2048 lds\n"
  "v_mfma_f32_16x16x32_bf16 v[156:159], v[132:135], v[132:135], v[156:159]\n"
  "s_barrier\n"
  "v_mfma_f32_16x16x32_bf16 v[144:147], a[180:183], v[136:139], v[144:147]\n"
  "ds_read_b64_tr_b16 a[192:193], v15 offset:0\n"
  "v_mfma_f32_16x16x32_bf16 v[152:155], v[136:139], v[136:139], v[152:155]\n"
  "s_mov_b32 exec_hi, 0\n"
  "buffer_load_dwordx4 v8, s[44:47], s31 offen offset:3072 lds\n"
  "s_mov_b32 exec_hi, -1\n"
  "s_waitcnt lgkmcnt(1)\n"
  "v_mfma_f32_16x16x32_bf16 v[148:151], a[180:183], v[140:143], v[148:151]\n"
  "ds_read_b64_tr_b16 a[194:195], v15 offset:1792\n"
  "ds_read_b64_tr_b16 a[196:197], v16 offset:32\n"
  "v_mfma_f32_16x16x32_bf16 v[156:159], v[140:143], v[140:143], v[156:159]\n"
  "ds_read_b64_tr_b16 a[198:199], v16 offset:1824\n"
  "v_mul_f32 v24, 0xbb124925, v144\n"
  "v_mul_f32 v25, 0xbb124925, v145\n"
  "v_mul_f32 v26, 0xbb124925, v146\n"
  "v_mul_f32 v27, 0xbb124925, v147\n"
  "v_mul_f32 v28, 0xbb124925, v148\n"
  "v_mul_f32 v29, 0xbb124925, v149\n"
  "v_mul_f32 v30, 0xbb124925, v150\n"
  "v_mul_f32 v31, 0xbb124925, v151\n"
  "s_add_u32 m0, s34, 8192\n"
  "s_mov_b32 s30, 0\n"
  "v_mfma_f32_16x16x32_bf16 v[224:227], a[186:189], v[32:35], v[24:27]\n"
  "ds_read_b64_tr_b16 a[200:201], v15 offset:64\n"
  "buffer_load_dwordx4 v3, s[52:55], s30 offen offset:0 nt lds\n"
  "v_mfma_f32_16x16x32_bf16 v[228:231], a[184:187], v[32:35], v[24:27]\n"
  "ds_read_b64_tr_b16 a[202:203], v15 offset:1856\n"
  "ds_write_b128 v164, v[152:155] offset:0\n"
  "ds_write_b128 v164, v[156:159] offset:1024\n"
  "v_mfma_f32_16x16x32_bf16 v[232:235], a[186:189], v[36:39], v[28:31]\n"
  "ds_read_b64_tr_b16 a[204:205], v16 offset:96\n"
  "s_mov_b32 s30, 64512\n"
  "ds_read_b32 v11, v165 offset:0\n"
  "ds_read_b32 v12, v165 offset:1024\n"
  "v_mfma_f32_16x16x32_bf16 v[236:239], a[184:187], v[36:39], v[28:31]\n"
  "buffer_load_dwordx4 v3, s[52:55], s30 offen offset:1024 nt lds\n"
  "v_mfma_f32_16x16x32_bf16 v[240:243], a[186:189], v[40:43], v[24:27]\n"
  "ds_read_b64_tr_b16 a[206:207], v16 offset:1888\n"
  "ds_read_b64_tr_b16 a[208:209], v15 offset:128\n"
  "v_cvt_pk_bf16_f32 v32, v224, v225\n"
  "v_cvt_pk_bf16_f32 v33, v226, v227\n"
  "v_mfma_f32_16x16x32_bf16 v[244:247], a[184:187], v[40:43], v[24:27]\n"
  "ds_read_b64_tr_b16 a[210:211], v15 offset:1920\n"
  "v_cvt_pk_bf16_f32 v34, v228, v229\n"
  "v_cvt_pk_bf16_f32 v35, v230, v231\n"
  "s_mov_b32 s30, 14336\n"
  "v_mfma_f32_16x16x32_bf16 v[248:251], a[186:189], v[44:47], v[28:31]\n"
  "buffer_load_dwordx4 v3, s[52:55], s30 offen offset:2048 nt lds\n"
  "v_cvt_pk_bf16_f32 v36, v232, v233\n"
  "v_cvt_pk_bf16_f32 v37, v234, v235\n"
  "v_mfma_f32_16x16x32_bf16 v[252:255], a[184:187], v[44:47], v[28:31]\n"
  "ds_read_b64_tr_b16 a[212:213], v16 offset:160\n"
  "ds_read_b64_tr_b16 a[214:215], v16 offset:1952\n"
  "v_cvt_pk_bf16_f32 v38, v236, v237\n"
  "v_cvt_pk_bf16_f32 v39, v238, v239\n"
  "s_waitcnt lgkmcnt(11)\n"
  "v_mfma_f32_16x16x32_bf16 v[144:147], a[192:195], v[32:35], 0\n"
  "ds_read_b64_tr_b16 a[216:217], v15 offset:192\n"
  "v_cvt_pk_bf16_f32 v40, v240, v241\n"
  "v_cvt_pk_bf16_f32 v41, v242, v243\n"
  "s_mov_b32 s30, 78848\n"
  "v_mfma_f32_16x16x32_bf16 v[148:151], a[192:195], v[36:39], 0\n"
  "buffer_load_dwordx4 v3, s[52:55], s30 offen offset:3072 nt lds\n"
  "v_cvt_pk_bf16_f32 v42, v244, v245\n"
  "v_cvt_pk_bf16_f32 v43, v246, v247\n"
  "v_mfma_f32_16x16x32_bf16 v[152:155], a[196:199], v[32:35], 0\n"
  "ds_read_b64_tr_b16 a[218:219], v15 offset:1984\n"
  "ds_read_b64_tr_b16 a[220:221], v16 offset:224\n"
  "v_cvt_pk_bf16_f32 v44, v248, v249\n"
  "v_cvt_pk_bf16_f32 v45, v250, v251\n"
  "v_mfma_f32_16x16x32_bf16 v[156:159], a[196:199], v[36:39], 0\n"
  "ds_read_b64_tr_b16 a[222:223], v16 offset:2016\n"
  "v_cvt_pk_bf16_f32 v46, v252, v253\n"
  "v_cvt_pk_bf16_f32 v47, v254, v255\n"
  "s_add_u32 m0, s34, 12288\n"
  "s_mov_b32 s30, 128\n"
  "s_waitcnt lgkmcnt(7)\n"
  "v_mfma_f32_16x16x32_bf16 v[160:163], a[200:203], v[32:35], 0\n"
  "ds_read_b64_tr_b16 a[224:225], v15 offset:256\n"
  "buffer_load_dwordx4 v3, s[52:55], s30 offen offset:0 nt lds\n"
  "v_mfma_f32_16x16x32_bf16 v[164:167], a[200:203], v[36:39], 0\n"
  "ds_read_b64_tr_b16 a[226:227], v15 offset:2048\n"
  "v_mul_f32 v2, v24, v24\n"
  "v_mul_f32 v4, v28, v28\n"
  "v_mfma_f32_16x16x32_bf16 v[168:171], a[204:207], v[32:35], 0\n"
  "ds_read_b64_tr_b16 a[228:229], v16 offset:288\n"
  "s_mov_b32 s30, 64640\n"
  "v_mul_f32 v11, 0x3b124925, v11\n"
  "v_mul_f32 v12, 0x3b124925, v12\n"
  "v_mfma_f32_16x16x32_bf16 v[172:175], a[204:207], v[36:39], 0\n"
  "buffer_load_dwordx4 v3, s[52:55], s30 offen offset:1024 nt lds\n"
  "s_waitcnt lgkmcnt(6)\n"
  "v_mfma_f32_16x16x32_bf16 v[176:179], a[208:211], v[32:35], 0\n"
  "ds_read_b64_tr_b16 a[230:231], v16 offset:2080\n"
  "ds_read_b64_tr_b16 a[232:233], v15 offset:320\n"
  "v_sub_f32 v2, v11, v2\n"
  "v_sub_f32 v4, v12, v4\n"
  "v_mfma_f32_16x16x32_bf16 v[180:183], a[208:211], v[36:39], 0\n"
  "ds_read_b64_tr_b16 a[234:235], v15 offset:2112\n"
  "s_mov_b32 s30, 14464\n"
  "v_max_f32 v2, 0, v2\n"
  "v_mfma_f32_16x16x32_bf16 v[184:187], a[212:215], v[32:35], 0\n"
  "buffer_load_dwordx4 v3, s[52:55], s30 offen offset:2048 nt lds\n"
  "v_max_f32 v4, 0, v4\n"
  "v_mfma_f32_16x16x32_bf16 v[188:191], a[212:215], v[36:39], 0\n"
  "ds_read_b64_tr_b16 a[236:237], v16 offset:352\n"
  "ds_read_b64_tr_b16 a[238:239], v16 offset:2144\n"
  "v_add_f32 v2, 0x3727c5ac, v2\n"
  "s_waitcnt lgkmcnt(8)\n"
  "v_mfma_f32_16x16x32_bf16 v[192:195], a[216:219], v[32:35], 0\n"
  "ds_read_b64_tr_b16 a[240:241], v15 offset:384\n"
  "s_mov_b32 s30, 78976\n"
  "v_add_f32 v4, 0x3727c5ac, v4\n"
  "v_rsq_f32 v11, v2\n"
  "v_mfma_f32_16x16x32_bf16 v[196:199], a[216:219], v[36:39], 0\n"
  "buffer_load_dwordx4 v3, s[52:55], s30 offen offset:3072 nt lds\n"
  "v_mfma_f32_16x16x32_bf16 v[200:203], a[220:223], v[32:35], 0\n"
  "ds_read_b64_tr_b16 a[242:243], v15 offset:2176\n"
  "ds_read_b64_tr_b16 a[244:245], v16 offset:416\n"
  "v_rsq_f32 v12, v4\n"
  "v_mfma_f32_16x16x32_bf16 v[204:207], a[220:223], v[36:39], 0\n"
  "s_waitcnt vmcnt(16)\n"
  "s_barrier\n"
  "ds_read_b64_tr_b16 a[246:247], v16 offset:2208\n"
  "s_waitcnt lgkmcnt(9)\n"
  "v_mfma_f32_16x16x32_bf16 v[208:211], a[224:227], v[32:35], 0\n"
  "ds_read_b64_tr_b16 a[248:249], v15 offset:16384\n"
  "s_mov_b32 s29, s72\n"
  "s_mov_b64 exec, 1\n"
  "buffer_store_dword v20, off, s[60:63], s29 sc1\n"
  "s_mov_b64 exec, -1\n"
  "v_mfma_f32_16x16x32_bf16 v[212:215], a[224:227], v[36:39], 0\n"
  "ds_read_b64_tr_b16 a[250:251], v15 offset:18176\n"
  "s_waitcnt lgkmcnt(8)\n"
  "v_mfma_f32_16x16x32_bf16 v[216:219], a[228:231], v[32:35], 0\n"
  "ds_read_b64_tr_b16 a[252:253], v16 offset:16416\n"
  "v_mfma_f32_16x16x32_bf16 v[220:223], a[228:231], v[36:39], 0\n"
  "ds_read_b64_tr_b16 a[254:255], v16 offset:18208\n"
  "v_mfma_f32_16x16x32_bf16 v[224:227], a[232:235], v[32:35], 0\n"
  "ds_read_b64_tr_b16 a[192:193], v15 offset:16448\n"
  "v_mfma_f32_16x16x32_bf16 v[228:231], a[232:235], v[36:39], 0\n"
  "ds_read_b64_tr_b16 a[194:195], v15 offset:18240\n"
  "s_waitcnt lgkmcnt(9)\n"
  "v_mfma_f32_16x16x32_bf16 v[232:235], a[236:239], v[32:35], 0\n"
  "v_mfma_f32_16x16x32_bf16 v[236:239], a[236:239], v[36:39], 0\n"
  "s_waitcnt lgkmcnt(6)\n"
  "v_mfma_f32_16x16x32_bf16 v[240:243], a[240:243], v[32:35], 0\n"
  "ds_read_b64_tr_b16 a[196:197], v16 offset:16480\n"
  "v_mfma_f32_16x16x32_bf16 v[244:247], a[240:243], v[36:39], 0\n"
  "ds_read_b64_tr_b16 a[198:199], v16 offset:18272\n"
  "v_mfma_f32_16x16x32_bf16 v[248:251], a[244:247], v[32:35], 0\n"
  "ds_read_b64_tr_b16 a[200:201], v15 offset:16512\n"
  "v_mfma_f32_16x16x32_bf16 v[252:255], a[244:247], v[36:39], 0\n"
  "ds_read_b64_tr_b16 a[202:203], v15 offset:18304\n"
  "s_waitcnt lgkmcnt(6)\n"
  "v_mfma_f32_16x16x32_bf16 v[144:147], a[248:251], v[40:43], v[144:147]\n"
  "ds_read_b64_tr_b16 a[204:205], v16 offset:16544\n"
  "v_mfma_f32_16x16x32_bf16 v[148:151], a[248:251], v[44:47], v[148:151]\n"
  "ds_read_b64_tr_b16 a[206:207], v16 offset:18336\n"
  "v_mfma_f32_16x16x32_bf16 v[152:155], a[252:255], v[40:43], v[152:155]\n"
  "ds_read_b64_tr_b16 a[208:209], v15 offset:16576\n"
  "v_mfma_f32_16x16x32_bf16 v[156:159], a[252:255], v[44:47], v[156:159]\n"
  "ds_read_b64_tr_b16 a[210:211], v15 offset:18368\n"
  "s_waitcnt lgkmcnt(8)\n"
  "v_mfma_f32_16x16x32_bf16 v[160:163], a[192:195], v[40:43], v[160:163]\n"
  "ds_read_b64_tr_b16 a[212:213], v16 offset:16608\n"
  "v_mfma_f32_16x16x32_bf16 v[164:167], a[192:195], v[44:47], v[164:167]\n"
  "ds_read_b64_tr_b16 a[214:215], v16 offset:18400\n"
  "v_mfma_f32_16x16x32_bf16 v[32:35], a[186:189], v[48:51], v[24:27]\n"
  "ds_read_b64_tr_b16 a[216:217], v15 offset:16640\n"
  "v_mfma_f32_16x16x32_bf16 v[36:39], a[184:187], v[48:51], v[24:27]\n"
  "ds_read_b64_tr_b16 a[218:219], v15 offset:18432\n"
  "Ltn0%=:\n"
  "s_cbranch_execz Ltnr0%=\n"
  "s_waitcnt lgkmcnt(8)\n"
  "v_mfma_f32_16x16x32_bf16 v[168:171], a[196:199], v[40:43], v[168:171]\n"
  "v_mfma_f32_16x16x32_bf16 v[172:175], a[196:199], v[44:47], v[172:175]\n"
  "v_mfma_f32_16x16x32_bf16 v[176:179], a[200:203], v[40:43], v[176:179]\n"
  "ds_read_b64_tr_b16 a[220:221], v16 offset:16672\n"
  "v_cvt_pk_bf16_f32 v48, v32, v33\n"
  "v_mfma_f32_16x16x32_bf16 v[180:183], a[200:203], v[44:47], v[180:183]\n"
  "ds_read_b64_tr_b16 a[222:223], v16 offset:18464\n"
  "v_cvt_pk_bf16_f32 v49, v34, v35\n"
  "s_waitcnt lgkmcnt(6)\n"
  "v_mfma_f32_16x16x32_bf16 v[184:187], a[204:207], v[40:43], v[184:187]\n"
  "ds_read_b64_tr_b16 a[224:225], v15 offset:16704\n"
  "v_cvt_pk_bf16_f32 v50, v36, v37\n"
  "v_mfma_f32_16x16x32_bf16 v[188:191], a[204:207], v[44:47], v[188:191]\n"
  "ds_read_b64_tr_b16 a[226:227], v15 offset:18496\n"
  "v_cvt_pk_bf16_f32 v51, v38, v39\n"
  "v_mfma_f32_16x16x32_bf16 v[192:195], a[208:211], v[40:43], v[192:195]\n"
  "ds_read_b64_tr_b16 a[228:229], v16 offset:16736\n"
  "v_mfma_f32_16x16x32_bf16 v[196:199], a[208:211], v[44:47], v[196:199]\n"
  "ds_read_b64_tr_b16 a[230:231], v16 offset:18528\n"
  "s_waitcnt lgkmcnt(6)\n"
  "v_mfma_f32_16x16x32_bf16 v[200:203], a[212:215], v[40:43], v[200:203]\n"
  "ds_read_b64_tr_b16 a[232:233], v15 offset:16768\n"
  "v_mfma_f32_16x16x32_bf16 v[204:207], a[212:215], v[44:47], v[204:207]\n"
  "ds_read_b64_tr_b16 a[234:235], v15 offset:18560\n"
  "v_mfma_f32_16x16x32_bf16 v[208:211], a[216:219], v[40:43], v[208:211]\n"
  "ds_read_b64_tr_b16 a[236:237], v16 offset:16800\n"
  "v_mfma_f32_16x16x32_bf16 v[212:215], a[216:219], v[44:47], v[212:215]\n"
  "s_waitcnt vmcnt(13)\n"
  "s_barrier\n"
  "ds_read_b64_tr_b16 a[238:239], v16 offset:18592\n"
  "v_mfma_f32_16x16x32_bf16 v[32:35], a[186:189], v[52:55], v[28:31]\n"
  "ds_read_b64_tr_b16 a[240:241], v13 offset:0\n"
  "v_mfma_f32_16x16x32_bf16 v[36:39], a[184:187], v[52:55], v[28:31]\n"
  "ds_read_b64_tr_b16 a[242:243], v13 offset:1792\n"
  "s_waitcnt lgkmcnt(8)\n"
  "v_mfma_f32_16x16x32_bf16 v[216:219], a[220:223], v[40:43], v[216:219]\n"
  "ds_read_b64_tr_b16 a[244:245], v14 offset:32\n"
  "v_mfma_f32_16x16x32_bf16 v[220:223], a[220:223], v[44:47], v[220:223]\n"
  "ds_read_b64_tr_b16 a[246:247], v14 offset:1824\n"
  "v_mfma_f32_16x16x32_bf16 v[224:227], a[224:227], v[40:43], v[224:227]\n"
  "ds_read_b64_tr_b16 a[248:249], v13 offset:64\n"
  "v_cvt_pk_bf16_f32 v52, v32, v33\n"
  "v_mfma_f32_16x16x32_bf16 v[228:231], a[224:227], v[44:47], v[228:231]\n"
  "ds_read_b64_tr_b16 a[250:251], v13 offset:1856\n"
  "v_cvt_pk_bf16_f32 v53, v34, v35\n"
  "s_waitcnt lgkmcnt(8)\n"
  "v_mfma_f32_16x16x32_bf16 v[232:235], a[228:231], v[40:43], v[232:235]\n"
  "v_cvt_pk_bf16_f32 v54, v36, v37\n"
  "v_mfma_f32_16x16x32_bf16 v[236:239], a[228:231], v[44:47], v[236:239]\n"
  "v_cvt_pk_bf16_f32 v55, v38, v39\n"
  "v_mfma_f32_16x16x32_bf16 v[240:243], a[232:235], v[40:43], v[240:243]\n"
  "ds_read_b64_tr_b16 a[252:253], v14 offset:96\n"
  "v_mfma_f32_16x16x32_bf16 v[244:247], a[232:235], v[44:47], v[244:247]\n"
  "ds_read_b64_tr_b16 a[254:255], v14 offset:1888\n"
  "s_waitcnt lgkmcnt(6)\n"
  "v_mfma_f32_16x16x32_bf16 v[248:251], a[236:239], v[40:43], v[248:251]\n"
  "ds_read_b64_tr_b16 a[192:193], v13 offset:128\n"
  "v_mfma_f32_16x16x32_bf16 v[252:255], a[236:239], v[44:47], v[252:255]\n"
  "ds_read_b64_tr_b16 a[194:195], v13 offset:1920\n"
  "s_add_u32 m0, s35, 32768\n"
  "s_mov_b32 s31, 458752\n"
  "v_mfma_f32_16x16x32_bf16 v[144:147], a[240:243], v[48:51], v[144:147]\n"
  "buffer_load_dwordx4 v5, s[44:47], s31 offen offset:0 lds\n"
  "v_mfma_f32_16x16x32_bf16 v[148:151], a[240:243], v[52:55], v[148:151]\n"
  "ds_read_b64_tr_b16 a[196:197], v14 offset:160\n"
  "ds_read_b64_tr_b16 a[198:199], v14 offset:1952\n"
  "s_waitcnt lgkmcnt(6)\n"
  "v_mfma_f32_16x16x32_bf16 v[152:155], a[244:247], v[48:51], v[152:155]\n"
  "ds_read_b64_tr_b16 a[200:201], v13 offset:192\n"
  "v_mfma_f32_16x16x32_bf16 v[156:159], a[244:247], v[52:55], v[156:159]\n"
  "ds_read_b64_tr_b16 a[202:203], v13 offset:1984\n"
  "v_mfma_f32_16x16x32_bf16 v[160:163], a[248:251], v[48:51], v[160:163]\n"
  "buffer_load_dwordx4 v6, s[44:47], s31 offen offset:1024 lds\n"
  "v_mfma_f32_16x16x32_bf16 v[164:167], a[248:251], v[52:55], v[164:167]\n"
  "ds_read_b64_tr_b16 a[204:205], v14 offset:224\n"
  "ds_read_b64_tr_b16 a[206:207], v14 offset:2016\n"
  "v_mfma_f32_16x16x32_bf16 v[32:35], a[186:189], v[56:59], v[24:27]\n"
  "ds_read_b64_tr_b16 a[208:209], v13 offset:256\n"
  "v_mfma_f32_16x16x32_bf16 v[36:39], a[184:187], v[56:59], v[24:27]\n"
  "ds_read_b64_tr_b16 a[210:211], v13 offset:2048\n"
  "s_waitcnt lgkmcnt(8)\n"
  "v_mfma_f32_16x16x32_bf16 v[168:171], a[252:255], v[48:51], v[168:171]\n"
  "v_mfma_f32_16x16x32_bf16 v[172:175], a[252:255], v[52:55], v[172:175]\n"
  "buffer_load_dwordx4 v7, s[44:47], s31 offen offset:2048 lds\n"
  "v_mfma_f32_16x16x32_bf16 v[176:179], a[192:195], v[48:51], v[176:179]\n"
  "ds_read_b64_tr_b16 a[212:213], v14 offset:288\n"
  "v_cvt_pk_bf16_f32 v56, v32, v33\n"
  "v_mfma_f32_16x16x32_bf16 v[180:183], a[192:195], v[52:55], v[180:183]\n"
  "ds_read_b64_tr_b16 a[214:215], v14 offset:2080\n"
  "v_cvt_pk_bf16_f32 v57, v34, v35\n"
  "s_waitcnt lgkmcnt(6)\n"
  "v_mfma_f32_16x16x32_bf16 v[184:187], a[196:199], v[48:51], v[184:187]\n"
  "ds_read_b64_tr_b16 a[216:217], v13 offset:320\n"
  "v_cvt_pk_bf16_f32 v58, v36, v37\n"
  "v_mfma_f32_16x16x32_bf16 v[188:191], a[196:199], v[52:55], v[188:191]\n"
  "ds_read_b64_tr_b16 a[218:219], v13 offset:2112\n"
  "v_cvt_pk_bf16_f32 v59, v38, v39\n"
  "v_mfma_f32_16x16x32_bf16 v[192:195], a[200:203], v[48:51], v[192:195]\n"
  "s_mov_b32 exec_hi, 0\n"
  "buffer_load_dwordx4 v8, s[44:47], s31 offen offset:3072 lds\n"
  "s_mov_b32 exec_hi, -1\n"
  "v_mfma_f32_16x16x32_bf16 v[196:199], a[200:203], v[52:55], v[196:199]\n"
  "ds_read_b64_tr_b16 a[220:221], v14 offset:352\n"
  "ds_read_b64_tr_b16 a[222:223], v14 offset:2144\n"
  "s_waitcnt lgkmcnt(6)\n"
  "v_mfma_f32_16x16x32_bf16 v[200:203], a[204:207], v[48:51], v[200:203]\n"
  "ds_read_b64_tr_b16 a[224:225], v13 offset:384\n"
  "v_mfma_f32_16x16x32_bf16 v[204:207], a[204:207], v[52:55], v[204:207]\n"
  "ds_read_b64_tr_b16 a[226:227], v13 offset:2176\n"
  "v_mfma_f32_16x16x32_bf16 v[208:211], a[208:211], v[48:51], v[208:211]\n"
  "ds_read_b64_tr_b16 a[228:229], v14 offset:416\n"
  "v_mfma_f32_16x16x32_bf16 v[212:215], a[208:211], v[52:55], v[212:215]\n"
  "s_waitcnt vmcnt(13)\n"
  "s_barrier\n"
  "ds_read_b64_tr_b16 a[230:231], v14 offset:2208\n"
  "s_add_u32 m0, s35, 49152\n"
  "s_mov_b32 s31, 573440\n"
  "v_mfma_f32_16x16x32_bf16 v[32:35], a[186:189], v[60:63], v[28:31]\n"
  "buffer_load_dwordx4 v5, s[44:47], s31 offen offset:0 lds\n"
  "v_mfma_f32_16x16x32_bf16 v[36:39], a[184:187], v[60:63], v[28:31]\n"
  "ds_read_b64_tr_b16 a[232:233], v13 offset:16384\n"
  "ds_read_b64_tr_b16 a[234:235], v13 offset:18176\n"
  "s_waitcnt lgkmcnt(8)\n"
  "v_mfma_f32_16x16x32_bf16 v[216:219], a[212:215], v[48:51], v[216:219]\n"
  "ds_read_b64_tr_b16 a[236:237], v14 offset:16416\n"
  "v_mfma_f32_16x16x32_bf16 v[220:223], a[212:215], v[52:55], v[220:223]\n"
  "ds_read_b64_tr_b16 a[238:239], v14 offset:18208\n"
  "v_mfma_f32_16x16x32_bf16 v[224:227], a[216:219], v[48:51], v[224:227]\n"
  "buffer_load_dwordx4 v6, s[44:47], s31 offen offset:1024 lds\n"
  "v_cvt_pk_bf16_f32 v60, v32, v33\n"
  "v_mfma_f32_16x16x32_bf16 v[228:231], a[216:219], v[52:55], v[228:231]\n"
  "ds_read_b64_tr_b16 a[240:241], v13 offset:16448\n"
  "ds_read_b64_tr_b16 a[242:243], v13 offset:18240\n"
  "v_cvt_pk_bf16_f32 v61, v34, v35\n"
  "s_waitcnt lgkmcnt(8)\n"
  "v_mfma_f32_16x16x32_bf16 v[232:235], a[220:223], v[48:51], v[232:235]\n"
  "v_cvt_pk_bf16_f32 v62, v36, v37\n"
  "v_mfma_f32_16x16x32_bf16 v[236:239], a[220:223], v[52:55], v[236:239]\n"
  "v_cvt_pk_bf16_f32 v63, v38, v39\n"
  "v_mfma_f32_16x16x32_bf16 v[240:243], a[224:227], v[48:51], v[240:243]\n"
  "buffer_load_dwordx4 v7, s[44:47], s31 offen offset:2048 lds\n"
  "v_mfma_f32_16x16x32_bf16 v[244:247], a[224:227], v[52:55], v[244:247]\n"
  "ds_read_b64_tr_b16 a[244:245], v14 offset:16480\n"
  "ds_read_b64_tr_b16 a[246:247], v14 offset:18272\n"
  "s_waitcnt lgkmcnt(6)\n"
  "v_mfma_f32_16x16x32_bf16 v[248:251], a[228:231], v[48:51], v[248:251]\n"
  "ds_read_b64_tr_b16 a[248:249], v13 offset:16512\n"
  "v_mfma_f32_16x16x32_bf16 v[252:255], a[228:231], v[52:55], v[252:255]\n"
  "ds_read_b64_tr_b16 a[250:251], v13 offset:18304\n"
  "v_mfma_f32_16x16x32_bf16 v[144:147], a[232:235], v[56:59], v[144:147]\n"
  "s_mov_b32 exec_hi, 0\n"
  "buffer_load_dwordx4 v8, s[44:47], s31 offen offset:3072 lds\n"
  "s_mov_b32 exec_hi, -1\n"
  "v_mfma_f32_16x16x32_bf16 v[148:151], a[232:235], v[60:63], v[148:151]\n"
  "ds_read_b64_tr_b16 a[252:253], v14 offset:16544\n"
  "ds_read_b64_tr_b16 a[254:255], v14 offset:18336\n"
  "s_waitcnt lgkmcnt(6)\n"
  "v_mfma_f32_16x16x32_bf16 v[152:155], a[236:239], v[56:59], v[152:155]\n"
  "ds_read_b64_tr_b16 a[192:193], v13 offset:16576\n"
  "v_mfma_f32_16x16x32_bf16 v[156:159], a[236:239], v[60:63], v[156:159]\n"
  "ds_read_b64_tr_b16 a[194:195], v13 offset:18368\n"
  "s_add_u32 m0, s35, 65536\n"
  "s_mov_b32 s31, 688128\n"
  "v_mfma_f32_16x16x32_bf16 v[160:163], a[240:243], v[56:59], v[160:163]\n"
  "buffer_load_dwordx4 v5, s[44:47], s31 offen offset:0 lds\n"
  "v_mfma_f32_16x16x32_bf16 v[164:167], a[240:243], v[60:63], v[164:167]\n"
  "ds_read_b64_tr_b16 a[196:197], v14 offset:16608\n"
  "ds_read_b64_tr_b16 a[198:199], v14 offset:18400\n"
  "v_mfma_f32_16x16x32_bf16 v[32:35], a[186:189], v[64:67], v[24:27]\n"
  "ds_read_b64_tr_b16 a[200:201], v13 offset:16640\n"
  "v_mfma_f32_16x16x32_bf16 v[36:39], a[184:187], v[64:67], v[24:27]\n"
  "buffer_load_dwordx4 v6, s[44:47], s31 offen offset:1024 lds\n"
  "s_waitcnt lgkmcnt(7)\n"
  "v_mfma_f32_16x16x32_bf16 v[168:171], a[244:247], v[56:59], v[168:171]\n"
  "ds_read_b64_tr_b16 a[202:203], v13 offset:18432\n"
  "v_mfma_f32_16x16x32_bf16 v[172:175], a[244:247], v[60:63], v[172:175]\n"
  "v_mfma_f32_16x16x32_bf16 v[176:179], a[248:251], v[56:59], v[176:179]\n"
  "ds_read_b64_tr_b16 a[204:205], v14 offset:16672\n"
  "v_cvt_pk_bf16_f32 v64, v32, v33\n"
  "v_mfma_f32_16x16x32_bf16 v[180:183], a[248:251], v[60:63], v[180:183]\n"
  "buffer_load_dwordx4 v7, s[44:47], s31 offen offset:2048 lds\n"
  "v_cvt_pk_bf16_f32 v65, v34, v35\n"
  "s_waitcnt lgkmcnt(5)\n"
  "v_mfma_f32_16x16x32_bf16 v[184:187], a[252:255], v[56:59], v[184:187]\n"
  "ds_read_b64_tr_b16 a[206:207], v14 offset:18464\n"
  "ds_read_b64_tr_b16 a[208:209], v13 offset:16704\n"
  "v_cvt_pk_bf16_f32 v66, v36, v37\n"
  "v_mfma_f32_16x16x32_bf16 v[188:191], a[252:255], v[60:63], v[188:191]\n"
  "ds_read_b64_tr_b16 a[210:211], v13 offset:18496\n"
  "v_cvt_pk_bf16_f32 v67, v38, v39\n"
  "v_mfma_f32_16x16x32_bf16 v[192:195], a[192:195], v[56:59], v[192:195]\n"
  "ds_read_b64_tr_b16 a[212:213], v14 offset:16736\n"
  "v_mfma_f32_16x16x32_bf16 v[196:199], a[192:195], v[60:63], v[196:199]\n"
  "s_mov_b32 exec_hi, 0\n"
  "buffer_load_dwordx4 v8, s[44:47], s31 offen offset:3072 lds\n"
  "s_mov_b32 exec_hi, -1\n"
  "s_waitcnt lgkmcnt(6)\n"
  "v_mfma_f32_16x16x32_bf16 v[200:203], a[196:199], v[56:59], v[200:203]\n"
  "ds_read_b64_tr_b16 a[214:215], v14 offset:18528\n"
  "ds_read_b64_tr_b16 a[216:217], v13 offset:16768\n"
  "v_mfma_f32_16x16x32_bf16 v[204:207], a[196:199], v[60:63], v[204:207]\n"
  "ds_read_b64_tr_b16 a[218:219], v13 offset:18560\n"
  "s_waitcnt lgkmcnt(8)\n"
  "v_mfma_f32_16x16x32_bf16 v[208:211], a[200:203], v[56:59], v[208:211]\n"
  "ds_read_b64_tr_b16 a[220:221], v14 offset:16800\n"
  "v_mfma_f32_16x16x32_bf16 v[212:215], a[200:203], v[60:63], v[212:215]\n"
  "s_waitcnt vmcnt(8)\n"
  "s_barrier\n"
  "ds_read_b64_tr_b16 a[222:223], v14 offset:18592\n"
  "s_add_u32 m0, s35, 81920\n"
  "s_mov_b32 s31, 802816\n"
  "v_mfma_f32_16x16x32_bf16 v[32:35], a[186:189], v[68:71], v[28:31]\n"
  "buffer_load_dwordx4 v5, s[44:47], s31 offen offset:0 lds\n"
  "v_mfma_f32_16x16x32_bf16 v[36:39], a[184:187], v[68:71], v[28:31]\n"
  "ds_read_b64_tr_b16 a[224:225], v13 offset:32768\n"
  "ds_read_b64_tr_b16 a[226:227], v13 offset:34560\n"
  "ds_read_b128 a[0:3], v9 offset:8192\n"
  "s_waitcnt lgkmcnt(9)\n"
  "v_mfma_f32_16x16x32_bf16 v[216:219], a[204:207], v[56:59], v[216:219]\n"
  "ds_read_b64_tr_b16 a[228:229], v14 offset:32800\n"
  "v_mfma_f32_16x16x32_bf16 v[220:223], a[204:207], v[60:63], v[220:223]\n"
  "ds_read_b64_tr_b16 a[230:231], v14 offset:34592\n"
  "ds_read_b128 a[4:7], v9 offset:10240\n"
  "v_mfma_f32_16x16x32_bf16 v[224:227], a[208:211], v[56:59], v[224:227]\n"
  "ds_read_b64_tr_b16 a[232:233], v13 offset:32832\n"
  "v_cvt_pk_bf16_f32 v68, v32, v33\n"
  "v_mfma_f32_16x16x32_bf16 v[228:231], a[208:211], v[60:63], v[228:231]\n"
  "ds_read_b64_tr_b16 a[234:235], v13 offset:34624\n"
  "ds_read_b128 a[8:11], v10 offset:8192\n"
  "v_cvt_pk_bf16_f32 v69, v34, v35\n"
  "s_waitcnt lgkmcnt(11)\n"
  "v_mfma_f32_16x16x32_bf16 v[232:235], a[212:215], v[56:59], v[232:235]\n"
  "v_cvt_pk_bf16_f32 v70, v36, v37\n"
  "v_mfma_f32_16x16x32_bf16 v[236:239], a[212:215], v[60:63], v[236:239]\n"
  "buffer_load_dwordx4 v6, s[44:47], s31 offen offset:1024 lds\n"
  "v_cvt_pk_bf16_f32 v71, v38, v39\n"
  "v_mfma_f32_16x16x32_bf16 v[240:243], a[216:219], v[56:59], v[240:243]\n"
  "ds_read_b128 a[12:15], v10 offset:10240\n"
  "ds_read_b64_tr_b16 a[236:237], v14 offset:32864\n"
  "v_mfma_f32_16x16x32_bf16 v[244:247], a[216:219], v[60:63], v[244:247]\n"
  "ds_read_b64_tr_b16 a[238:239], v14 offset:34656\n"
  "ds_read_b128 a[16:19], v9 offset:12288\n"
  "s_waitcnt lgkmcnt(10)\n"
  "v_mfma_f32_16x16x32_bf16 v[248:251], a[220:223], v[56:59], v[248:251]\n"
  "ds_read_b64_tr_b16 a[240:241], v13 offset:32896\n"
  "v_mfma_f32_16x16x32_bf16 v[252:255], a[220:223], v[60:63], v[252:255]\n"
  "ds_read_b64_tr_b16 a[242:243], v13 offset:34688\n"
  "ds_read_b128 a[20:23], v9 offset:14336\n"
  "v_mfma_f32_16x16x32_bf16 v[144:147], a[224:227], v[64:67], v[144:147]\n"
  "ds_read_b64_tr_b16 a[244:245], v14 offset:32928\n"
  "v_mfma_f32_16x16x32_bf16 v[148:151], a[224:227], v[68:71], v[148:151]\n"
  "ds_read_b64_tr_b16 a[246:247], v14 offset:34720\n"
  "ds_read_b128 a[24:27], v10 offset:12288\n"
  "s_waitcnt lgkmcnt(10)\n"
  "v_mfma_f32_16x16x32_bf16 v[152:155], a[228:231], v[64:67], v[152:155]\n"
  "ds_read_b64_tr_b16 a[248:249], v13 offset:32960\n"
  "v_mfma_f32_16x16x32_bf16 v[156:159], a[228:231], v[68:71], v[156:159]\n"
  "buffer_load_dwordx4 v7, s[44:47], s31 offen offset:2048 lds\n"
  "v_mfma_f32_16x16x32_bf16 v[160:163], a[232:235], v[64:67], v[160:163]\n"
  "ds_read_b64_tr_b16 a[250:251], v13 offset:34752\n"
  "ds_read_b128 a[28:31], v10 offset:14336\n"
  "ds_read_b64_tr_b16 a[252:253], v14 offset:32992\n"
  "v_mfma_f32_16x16x32_bf16 v[164:167], a[232:235], v[68:71], v[164:167]\n"
  "ds_read_b64_tr_b16 a[254:255], v14 offset:34784\n"
  "v_mfma_f32_16x16x32_bf16 v[32:35], a[186:189], v[72:75], v[24:27]\n"
  "ds_read_b64_tr_b16 a[192:193], v13 offset:33024\n"
  "v_mfma_f32_16x16x32_bf16 v[36:39], a[184:187], v[72:75], v[24:27]\n"
  "ds_read_b64_tr_b16 a[194:195], v13 offset:34816\n"
  "s_waitcnt lgkmcnt(10)\n"
  "v_mfma_f32_16x16x32_bf16 v[168:171], a[236:239], v[64:67], v[168:171]\n"
  "v_mfma_f32_16x16x32_bf16 v[172:175], a[236:239], v[68:71], v[172:175]\n"
  "v_mfma_f32_16x16x32_bf16 v[176:179], a[240:243], v[64:67], v[176:179]\n"
  "ds_read_b64_tr_b16 a[196:197], v14 offset:33056\n"
  "v_cvt_pk_bf16_f32 v72, v32, v33\n"
  "v_mfma_f32_16x16x32_bf16 v[180:183], a[240:243], v[68:71], v[180:183]\n"
  "s_mov_b32 exec_hi, 0\n"
  "buffer_load_dwordx4 v8, s[44:47], s31 offen offset:3072 lds\n"
  "s_mov_b32 exec_hi, -1\n"
  "v_cvt_pk_bf16_f32 v73, v34, v35\n"
  "s_waitcnt lgkmcnt(7)\n"
  "v_mfma_f32_16x16x32_bf16 v[184:187], a[244:247], v[64:67], v[184:187]\n"
  "ds_read_b64_tr_b16 a[198:199], v14 offset:34848\n"
  "ds_read_b64_tr_b16 a[200:201], v13 offset:33088\n"
  "v_cvt_pk_bf16_f32 v74, v36, v37\n"
  "v_mfma_f32_16x16x32_bf16 v[188:191], a[244:247], v[68:71], v[188:191]\n"
  "ds_read_b64_tr_b16 a[202:203], v13 offset:34880\n"
  "v_cvt_pk_bf16_f32 v75, v38, v39\n"
  "s_waitcnt lgkmcnt(6)\n"
  "v_mfma_f32_16x16x32_bf16 v[192:195], a[248:251], v[64:67], v[192:195]\n"
  "ds_read_b64_tr_b16 a[204:205], v14 offset:33120\n"
  "v_mfma_f32_16x16x32_bf16 v[196:199], a[248:251], v[68:71], v[196:199]\n"
  "ds_read_b64_tr_b16 a[206:207], v14 offset:34912\n"
  "v_mfma_f32_16x16x32_bf16 v[200:203], a[252:255], v[64:67], v[200:203]\n"
  "ds_read_b64_tr_b16 a[208:209], v13 offset:33152\n"
  "v_mfma_f32_16x16x32_bf16 v[204:207], a[252:255], v[68:71], v[204:207]\n"
  "ds_read_b64_tr_b16 a[210:211], v13 offset:34944\n"
  "s_waitcnt lgkmcnt(8)\n"
  "v_mfma_f32_16x16x32_bf16 v[208:211], a[192:195], v[64:67], v[208:211]\n"
  "ds_read_b64_tr_b16 a[212:213], v14 offset:33184\n"
  "v_mfma_f32_16x16x32_bf16 v[212:215], a[192:195], v[68:71], v[212:215]\n"
  "s_waitcnt vmcnt(8)\n"
  "s_barrier\n"
  "ds_read_b64_tr_b16 a[214:215], v14 offset:34976\n"
  "s_add_u32 m0, s35, 0\n"
  "s_mov_b32 s31, 917504\n"
  "v_mfma_f32_16x16x32_bf16 v[32:35], a[186:189], v[76:79], v[28:31]\n"
  "buffer_load_dwordx4 v5, s[44:47], s31 offen offset:0 lds\n"
  "v_mfma_f32_16x16x32_bf16 v[36:39], a[184:187], v[76:79], v[28:31]\n"
  "ds_read_b64_tr_b16 a[216:217], v13 offset:49152\n"
  "ds_read_b64_tr_b16 a[218:219], v13 offset:50944\n"
  "s_waitcnt lgkmcnt(8)\n"
  "v_mfma_f32_16x16x32_bf16 v[216:219], a[196:199], v[64:67], v[216:219]\n"
  "ds_read_b64_tr_b16 a[220:221], v14 offset:49184\n"
  "v_mfma_f32_16x16x32_bf16 v[220:223], a[196:199], v[68:71], v[220:223]\n"
  "buffer_load_dwordx4 v6, s[44:47], s31 offen offset:1024 lds\n"
  "v_mfma_f32_16x16x32_bf16 v[224:227], a[200:203], v[64:67], v[224:227]\n"
  "ds_read_b64_tr_b16 a[222:223], v14 offset:50976\n"
  "ds_read_b64_tr_b16 a[224:225], v13 offset:49216\n"
  "v_cvt_pk_bf16_f32 v76, v32, v33\n"
  "v_mfma_f32_16x16x32_bf16 v[228:231], a[200:203], v[68:71], v[228:231]\n"
  "ds_read_b64_tr_b16 a[226:227], v13 offset:51008\n"
  "v_cvt_pk_bf16_f32 v77, v34, v35\n"
  "s_waitcnt lgkmcnt(8)\n"
  "v_mfma_f32_16x16x32_bf16 v[232:235], a[204:207], v[64:67], v[232:235]\n"
  "v_cvt_pk_bf16_f32 v78, v36, v37\n"
  "v_mfma_f32_16x16x32_bf16 v[236:239], a[204:207], v[68:71], v[236:239]\n"
  "buffer_load_dwordx4 v7, s[44:47], s31 offen offset:2048 lds\n"
  "v_cvt_pk_bf16_f32 v79, v38, v39\n"
  "v_mfma_f32_16x16x32_bf16 v[240:243], a[208:211], v[64:67], v[240:243]\n"
  "ds_read_b64_tr_b16 a[228:229], v14 offset:49248\n"
  "v_mfma_f32_16x16x32_bf16 v[244:247], a[208:211], v[68:71], v[244:247]\n"
  "ds_read_b64_tr_b16 a[230:231], v14 offset:51040\n"
  "s_waitcnt lgkmcnt(6)\n"
  "v_mfma_f32_16x16x32_bf16 v[248:251], a[212:215], v[64:67], v[248:251]\n"
  "ds_read_b64_tr_b16 a[232:233], v13 offset:49280\n"
  "v_mfma_f32_16x16x32_bf16 v[252:255], a[212:215], v[68:71], v[252:255]\n"
  "s_mov_b32 exec_hi, 0\n"
  "buffer_load_dwordx4 v8, s[44:47], s31 offen offset:3072 lds\n"
  "s_mov_b32 exec_hi, -1\n"
  "v_mfma_f32_16x16x32_bf16 v[144:147], a[216:219], v[72:75], v[144:147]\n"
  "ds_read_b64_tr_b16 a[234:235], v13 offset:51072\n"
  "ds_read_b64_tr_b16 a[236:237], v14 offset:49312\n"
  "v_mfma_f32_16x16x32_bf16 v[148:151], a[216:219], v[76:79], v[148:151]\n"
  "ds_read_b64_tr_b16 a[238:239], v14 offset:51104\n"
  "s_waitcnt lgkmcnt(6)\n"
  "v_mfma_f32_16x16x32_bf16 v[152:155], a[220:223], v[72:75], v[152:155]\n"
  "ds_read_b64_tr_b16 a[240:241], v13 offset:49344\n"
  "s_add_u32 m0, s35, 16384\n"
  "s_mov_b32 s31, 1032192\n"
  "v_mfma_f32_16x16x32_bf16 v[156:159], a[220:223], v[76:79], v[156:159]\n"
  "buffer_load_dwordx4 v5, s[44:47], s31 offen offset:0 lds\n"
  "v_mfma_f32_16x16x32_bf16 v[160:163], a[224:227], v[72:75], v[160:163]\n"
  "ds_read_b64_tr_b16 a[242:243], v13 offset:51136\n"
  "ds_read_b64_tr_b16 a[244:245], v14 offset:49376\n"
  "v_mfma_f32_16x16x32_bf16 v[164:167], a[224:227], v[76:79], v[164:167]\n"
  "ds_read_b64_tr_b16 a[246:247], v14 offset:51168\n"
  "v_mfma_f32_16x16x32_bf16 v[32:35], a[186:189], v[80:83], v[24:27]\n"
  "ds_read_b64_tr_b16 a[248:249], v13 offset:49408\n"
  "v_mfma_f32_16x16x32_bf16 v[36:39], a[184:187], v[80:83], v[24:27]\n"
  "buffer_load_dwordx4 v6, s[44:47], s31 offen offset:1024 lds\n"
  "s_waitcnt lgkmcnt(8)\n"
  "v_mfma_f32_16x16x32_bf16 v[168:171], a[228:231], v[72:75], v[168:171]\n"
  "ds_read_b64_tr_b16 a[250:251], v13 offset:51200\n"
  "v_mfma_f32_16x16x32_bf16 v[172:175], a[228:231], v[76:79], v[172:175]\n"
  "s_waitcnt lgkmcnt(6)\n"
  "v_mfma_f32_16x16x32_bf16 v[176:179], a[232:235], v[72:75], v[176:179]\n"
  "ds_read_b64_tr_b16 a[252:253], v14 offset:49440\n"
  "v_cvt_pk_bf16_f32 v80, v32, v33\n"
  "v_mfma_f32_16x16x32_bf16 v[180:183], a[232:235], v[76:79], v[180:183]\n"
  "buffer_load_dwordx4 v7, s[44:47], s31 offen offset:2048 lds\n"
  "v_cvt_pk_bf16_f32 v81, v34, v35\n"
  "v_mfma_f32_16x16x32_bf16 v[184:187], a[236:239], v[72:75], v[184:187]\n"
  "ds_read_b64_tr_b16 a[254:255], v14 offset:51232\n"
  "ds_read_b64_tr_b16 a[192:193], v13 offset:49472\n"
  "v_cvt_pk_bf16_f32 v82, v36, v37\n"
  "v_mfma_f32_16x16x32_bf16 v[188:191], a[236:239], v[76:79], v[188:191]\n"
  "ds_read_b64_tr_b16 a[194:195], v13 offset:51264\n"
  "v_cvt_pk_bf16_f32 v83, v38, v39\n"
  "s_waitcnt lgkmcnt(6)\n"
  "v_mfma_f32_16x16x32_bf16 v[192:195], a[240:243], v[72:75], v[192:195]\n"
  "ds_read_b64_tr_b16 a[196:197], v14 offset:49504\n"
  "v_mfma_f32_16x16x32_bf16 v[196:199], a[240:243], v[76:79], v[196:199]\n"
  "s_mov_b32 exec_hi, 0\n"
  "buffer_load_dwordx4 v8, s[44:47], s31 offen offset:3072 lds\n"
  "s_mov_b32 exec_hi, -1\n"
  "v_mfma_f32_16x16x32_bf16 v[200:203], a[244:247], v[72:75], v[200:203]\n"
  "ds_read_b64_tr_b16 a[198:199], v14 offset:51296\n"
  "ds_read_b64_tr_b16 a[200:201], v13 offset:49536\n"
  "v_mfma_f32_16x16x32_bf16 v[204:207], a[244:247], v[76:79], v[204:207]\n"
  "ds_read_b64_tr_b16 a[202:203], v13 offset:51328\n"
  "s_waitcnt lgkmcnt(8)\n"
  "v_mfma_f32_16x16x32_bf16 v[208:211], a[248:251], v[72:75], v[208:211]\n"
  "ds_read_b64_tr_b16 a[204:205], v14 offset:49568\n"
  "v_mfma_f32_16x16x32_bf16 v[212:215], a[248:251], v[76:79], v[212:215]\n"
  "s_waitcnt vmcnt(12)\n"
  "s_barrier\n"
  "ds_read_b64_tr_b16 a[206:207], v14 offset:51360\n"
  "s_add_u32 m0, s35, 32768\n"
  "s_mov_b32 s31, 1146880\n"
  "v_mfma_f32_16x16x32_bf16 v[32:35], a[186:189], v[84:87], v[28:31]\n"
  "buffer_load_dwordx4 v5, s[44:47], s31 offen offset:0 lds\n"
  "v_mfma_f32_16x16x32_bf16 v[36:39], a[184:187], v[84:87], v[28:31]\n"
  "ds_read_b64_tr_b16 a[208:209], v15 offset:0\n"
  "ds_read_b64_tr_b16 a[210:211], v15 offset:1792\n"
  "s_waitcnt lgkmcnt(8)\n"
  "v_mfma_f32_16x16x32_bf16 v[216:219], a[252:255], v[72:75], v[216:219]\n"
  "ds_read_b64_tr_b16 a[212:213], v16 offset:32\n"
  "v_mfma_f32_16x16x32_bf16 v[220:223], a[252:255], v[76:79], v[220:223]\n"
  "buffer_load_dwordx4 v6, s[44:47], s31 offen offset:1024 lds\n"
  "v_mfma_f32_16x16x32_bf16 v[224:227], a[192:195], v[72:75], v[224:227]\n"
  "ds_read_b64_tr_b16 a[214:215], v16 offset:1824\n"
  "ds_read_b64_tr_b16 a[216:217], v15 offset:64\n"
  "v_cvt_pk_bf16_f32 v84, v32, v33\n"
  "v_mfma_f32_16x16x32_bf16 v[228:231], a[192:195], v[76:79], v[228:231]\n"
  "ds_read_b64_tr_b16 a[218:219], v15 offset:1856\n"
  "v_cvt_pk_bf16_f32 v85, v34, v35\n"
  "s_waitcnt lgkmcnt(8)\n"
  "v_mfma_f32_16x16x32_bf16 v[232:235], a[196:199], v[72:75], v[232:235]\n"
  "buffer_load_dwordx4 v7, s[44:47], s31 offen offset:2048 lds\n"
  "v_cvt_pk_bf16_f32 v86, v36, v37\n"
  "v_mfma_f32_16x16x32_bf16 v[236:239], a[196:199], v[76:79], v[236:239]\n"
  "v_cvt_pk_bf16_f32 v87, v38, v39\n"
  "v_mfma_f32_16x16x32_bf16 v[240:243], a[200:203], v[72:75], v[240:243]\n"
  "ds_read_b64_tr_b16 a[220:221], v16 offset:96\n"
  "v_mfma_f32_16x16x32_bf16 v[244:247], a[200:203], v[76:79], v[244:247]\n"
  "s_mov_b32 exec_hi, 0\n"
  "buffer_load_dwordx4 v8, s[44:47], s31 offen offset:3072 lds\n"
  "s_mov_b32 exec_hi, -1\n"
  "s_waitcnt lgkmcnt(5)\n"
  "v_mfma_f32_16x16x32_bf16 v[248:251], a[204:207], v[72:75], v[248:251]\n"
  "ds_read_b64_tr_b16 a[222:223], v16 offset:1888\n"
  "ds_read_b64_tr_b16 a[224:225], v15 offset:128\n"
  "v_mfma_f32_16x16x32_bf16 v[252:255], a[204:207], v[76:79], v[252:255]\n"
  "ds_read_b64_tr_b16 a[226:227], v15 offset:1920\n"
  "s_add_u32 m0, s34, 0\n"
  "s_mov_b32 s30, 0\n"
  "v_mfma_f32_16x16x32_bf16 v[144:147], a[208:211], v[80:83], v[144:147]\n"
  "ds_read_b64_tr_b16 a[228:229], v16 offset:160\n"
  "v_xor_b32 v3, 0x100, v3\n"
  "buffer_load_dwordx4 v3, s[52:55], s30 offen offset:0 nt lds\n"
  "v_mfma_f32_16x16x32_bf16 v[148:151], a[208:211], v[84:87], v[148:151]\n"
  "ds_read_b64_tr_b16 a[230:231], v16 offset:1952\n"
  "s_waitcnt lgkmcnt(6)\n"
  "v_mfma_f32_16x16x32_bf16 v[152:155], a[212:215], v[80:83], v[152:155]\n"
  "ds_read_b64_tr_b16 a[232:233], v15 offset:192\n"
  "s_mov_b32 s30, 64512\n"
  "v_mfma_f32_16x16x32_bf16 v[156:159], a[212:215], v[84:87], v[156:159]\n"
  "buffer_load_dwordx4 v3, s[52:55], s30 offen offset:1024 nt lds\n"
  "v_mfma_f32_16x16x32_bf16 v[160:163], a[216:219], v[80:83], v[160:163]\n"
  "ds_read_b64_tr_b16 a[234:235], v15 offset:1984\n"
  "ds_read_b64_tr_b16 a[236:237], v16 offset:224\n"
  "v_mfma_f32_16x16x32_bf16 v[164:167], a[216:219], v[84:87], v[164:167]\n"
  "ds_read_b64_tr_b16 a[238:239], v16 offset:2016\n"
  "s_mov_b32 s30, 14336\n"
  "v_mfma_f32_16x16x32_bf16 v[32:35], a[186:189], v[88:91], v[24:27]\n"
  "buffer_load_dwordx4 v3, s[52:55], s30 offen offset:2048 nt lds\n"
  "v_mfma_f32_16x16x32_bf16 v[36:39], a[184:187], v[88:91], v[24:27]\n"
  "ds_read_b64_tr_b16 a[240:241], v15 offset:256\n"
  "ds_read_b64_tr_b16 a[242:243], v15 offset:2048\n"
  "s_waitcnt lgkmcnt(8)\n"
  "v_mfma_f32_16x16x32_bf16 v[168:171], a[220:223], v[80:83], v[168:171]\n"
  "s_mov_b32 s30, 78848\n"
  "v_mfma_f32_16x16x32_bf16 v[172:175], a[220:223], v[84:87], v[172:175]\n"
  "buffer_load_dwordx4 v3, s[52:55], s30 offen offset:3072 nt lds\n"
  "v_mfma_f32_16x16x32_bf16 v[176:179], a[224:227], v[80:83], v[176:179]\n"
  "ds_read_b64_tr_b16 a[244:245], v16 offset:288\n"
  "v_cvt_pk_bf16_f32 v88, v32, v33\n"
  "v_mfma_f32_16x16x32_bf16 v[180:183], a[224:227], v[84:87], v[180:183]\n"
  "ds_read_b64_tr_b16 a[246:247], v16 offset:2080\n"
  "v_cvt_pk_bf16_f32 v89, v34, v35\n"
  "s_waitcnt lgkmcnt(7)\n"
  "v_mfma_f32_16x16x32_bf16 v[184:187], a[228:231], v[80:83], v[184:187]\n"
  "ds_read_b64_tr_b16 a[248:249], v15 offset:320\n"
  "s_bfm_b64 exec, 28, 0\n"
  "buffer_load_dword a163, v23, s[48:51], 0 offen\n"
  "s_mov_b64 exec, -1\n"
  "v_cvt_pk_bf16_f32 v90, v36, v37\n"
  "v_mfma_f32_16x16x32_bf16 v[188:191], a[228:231], v[84:87], v[188:191]\n"
  "ds_read_b64_tr_b16 a[250:251], v15 offset:2112\n"
  "v_cvt_pk_bf16_f32 v91, v38, v39\n"
  "s_waitcnt lgkmcnt(6)\n"
  "v_mfma_f32_16x16x32_bf16 v[192:195], a[232:235], v[80:83], v[192:195]\n"
  "ds_read_b64_tr_b16 a[252:253], v16 offset:352\n"
  "v_mfma_f32_16x16x32_bf16 v[196:199], a[232:235], v[84:87], v[196:199]\n"
  "ds_read_b64_tr_b16 a[254:255], v16 offset:2144\n"
  "v_mfma_f32_16x16x32_bf16 v[200:203], a[236:239], v[80:83], v[200:203]\n"
  "ds_read_b64_tr_b16 a[192:193], v15 offset:384\n"
  "v_mfma_f32_16x16x32_bf16 v[204:207], a[236:239], v[84:87], v[204:207]\n"
  "ds_read_b64_tr_b16 a[194:195], v15 offset:2176\n"
  "s_waitcnt lgkmcnt(8)\n"
  "v_mfma_f32_16x16x32_bf16 v[208:211], a[240:243], v[80:83], v[208:211]\n"
  "ds_read_b64_tr_b16 a[196:197], v16 offset:416\n"
  "v_mfma_f32_16x16x32_bf16 v[212:215], a[240:243], v[84:87], v[212:215]\n"
  "s_waitcnt vmcnt(17)\n"
  "s_barrier\n"
  "ds_read_b64_tr_b16 a[198:199], v16 offset:2208\n"
  "s_add_u32 m0, s35, 49152\n"
  "s_mov_b32 s31, 1261568\n"
  "v_mfma_f32_16x16x32_bf16 v[32:35], a[186:189], v[92:95], v[28:31]\n"
  "buffer_load_dwordx4 v5, s[44:47], s31 offen offset:0 lds\n"
  "v_mfma_f32_16x16x32_bf16 v[36:39], a[184:187], v[92:95], v[28:31]\n"
  "ds_read_b64_tr_b16 a[200:201], v15 offset:16384\n"
  "ds_read_b64_tr_b16 a[202:203], v15 offset:18176\n"
  "s_waitcnt lgkmcnt(8)\n"
  "v_mfma_f32_16x16x32_bf16 v[216:219], a[244:247], v[80:83], v[216:219]\n"
  "ds_read_b64_tr_b16 a[204:205], v16 offset:16416\n"
  "v_mfma_f32_16x16x32_bf16 v[220:223], a[244:247], v[84:87], v[220:223]\n"
  "ds_read_b64_tr_b16 a[206:207], v16 offset:18208\n"
  "v_mfma_f32_16x16x32_bf16 v[224:227], a[248:251], v[80:83], v[224:227]\n"
  "ds_read_b64_tr_b16 a[208:209], v15 offset:16448\n"
  "v_cvt_pk_bf16_f32 v92, v32, v33\n"
  "v_mfma_f32_16x16x32_bf16 v[228:231], a[248:251], v[84:87], v[228:231]\n"
  "ds_read_b64_tr_b16 a[210:211], v15 offset:18240\n"
  "v_cvt_pk_bf16_f32 v93, v34, v35\n"
  "s_waitcnt lgkmcnt(8)\n"
  "v_mfma_f32_16x16x32_bf16 v[232:235], a[252:255], v[80:83], v[232:235]\n"
  "v_cvt_pk_bf16_f32 v94, v36, v37\n"
  "v_mfma_f32_16x16x32_bf16 v[236:239], a[252:255], v[84:87], v[236:239]\n"
  "buffer_load_dwordx4 v6, s[44:47], s31 offen offset:1024 lds\n"
  "v_cvt_pk_bf16_f32 v95, v38, v39\n"
  "v_mfma_f32_16x16x32_bf16 v[240:243], a[192:195], v[80:83], v[240:243]\n"
  "ds_read_b64_tr_b16 a[212:213], v16 offset:16480\n"
  "v_mfma_f32_16x16x32_bf16 v[244:247], a[192:195], v[84:87], v[244:247]\n"
  "ds_read_b64_tr_b16 a[214:215], v16 offset:18272\n"
  "s_waitcnt lgkmcnt(6)\n"
  "v_mfma_f32_16x16x32_bf16 v[248:251], a[196:199], v[80:83], v[248:251]\n"
  "ds_read_b64_tr_b16 a[216:217], v15 offset:16512\n"
  "v_mfma_f32_16x16x32_bf16 v[252:255], a[196:199], v[84:87], v[252:255]\n"
  "ds_read_b64_tr_b16 a[218:219], v15 offset:18304\n"
  "v_mfma_f32_16x16x32_bf16 v[144:147], a[200:203], v[88:91], v[144:147]\n"
  "ds_read_b64_tr_b16 a[220:221], v16 offset:16544\n"
  "v_mfma_f32_16x16x32_bf16 v[148:151], a[200:203], v[92:95], v[148:151]\n"
  "ds_read_b64_tr_b16 a[222:223], v16 offset:18336\n"
  "s_waitcnt lgkmcnt(6)\n"
  "v_mfma_f32_16x16x32_bf16 v[152:155], a[204:207], v[88:91], v[152:155]\n"
  "ds_read_b64_tr_b16 a[224:225], v15 offset:16576\n"
  "v_mfma_f32_16x16x32_bf16 v[156:159], a[204:207], v[92:95], v[156:159]\n"
  "buffer_load_dwordx4 v7, s[44:47], s31 offen offset:2048 lds\n"
  "v_mfma_f32_16x16x32_bf16 v[160:163], a[208:211], v[88:91], v[160:163]\n"
  "ds_read_b64_tr_b16 a[226:227], v15 offset:18368\n"
  "ds_read_b64_tr_b16 a[228:229], v16 offset:16608\n"
  "v_mfma_f32_16x16x32_bf16 v[164:167], a[208:211], v[92:95], v[164:167]\n"
  "ds_read_b64_tr_b16 a[230:231], v16 offset:18400\n"
  "v_mfma_f32_16x16x32_bf16 v[32:35], a[186:189], v[96:99], v[24:27]\n"
  "ds_read_b64_tr_b16 a[232:233], v15 offset:16640\n"
  "v_mfma_f32_16x16x32_bf16 v[36:39], a[184:187], v[96:99], v[24:27]\n"
  "ds_read_b64_tr_b16 a[234:235], v15 offset:18432\n"
  "s_waitcnt lgkmcnt(8)\n"
  "v_mfma_f32_16x16x32_bf16 v[168:171], a[212:215], v[88:91], v[168:171]\n"
  "v_mfma_f32_16x16x32_bf16 v[172:175], a[212:215], v[92:95], v[172:175]\n"
  "Ltn1%=:\n"
  "s_cbranch_execz Ltnr0%=\n"
  "v_mfma_f32_16x16x32_bf16 v[176:179], a[216:219], v[88:91], v[176:179]\n"
  "ds_read_b64_tr_b16 a[236:237], v16 offset:16672\n"
  "v_cvt_pk_bf16_f32 v96, v32, v33\n"
  "v_mfma_f32_16x16x32_bf16 v[180:183], a[216:219], v[92:95], v[180:183]\n"
  "s_mov_b32 exec_hi, 0\n"
  "buffer_load_dwordx4 v8, s[44:47], s31 offen offset:3072 lds\n"
  "s_mov_b32 exec_hi, -1\n"
  "v_cvt_pk_bf16_f32 v97, v34, v35\n"
  "s_waitcnt lgkmcnt(6)\n"
  "v_mfma_f32_16x16x32_bf16 v[184:187], a[220:223], v[88:91], v[184:187]\n"
  "ds_read_b64_tr_b16 a[238:239], v16 offset:18464\n"
  "ds_read_b64_tr_b16 a[240:241], v15 offset:16704\n"
  "v_cvt_pk_bf16_f32 v98, v36, v37\n"
  "v_mfma_f32_16x16x32_bf16 v[188:191], a[220:223], v[92:95], v[188:191]\n"
  "ds_read_b64_tr_b16 a[242:243], v15 offset:18496\n"
  "v_cvt_pk_bf16_f32 v99, v38, v39\n"
  "s_waitcnt lgkmcnt(6)\n"
  "v_mfma_f32_16x16x32_bf16 v[192:195], a[224:227], v[88:91], v[192:195]\n"
  "ds_read_b64_tr_b16 a[244:245], v16 offset:16736\n"
  "v_mfma_f32_16x16x32_bf16 v[196:199], a[224:227], v[92:95], v[196:199]\n"
  "ds_read_b64_tr_b16 a[246:247], v16 offset:18528\n"
  "v_mfma_f32_16x16x32_bf16 v[200:203], a[228:231], v[88:91], v[200:203]\n"
  "ds_read_b64_tr_b16 a[248:249], v15 offset:16768\n"
  "v_mfma_f32_16x16x32_bf16 v[204:207], a[228:231], v[92:95], v[204:207]\n"
  "ds_read_b64_tr_b16 a[250:251], v15 offset:18560\n"
  "s_waitcnt lgkmcnt(8)\n"
  "v_mfma_f32_16x16x32_bf16 v[208:211], a[232:235], v[88:91], v[208:211]\n"
  "ds_read_b64_tr_b16 a[252:253], v16 offset:16800\n"
  "v_mfma_f32_16x16x32_bf16 v[212:215], a[232:235], v[92:95], v[212:215]\n"
  "s_waitcnt vmcnt(17)\n"
  "s_barrier\n"
  "ds_read_b64_tr_b16 a[254:255], v16 offset:18592\n"
  "s_add_u32 m0, s34, 4096\n"
  "s_mov_b32 s30, 128\n"
  "v_mfma_f32_16x16x32_bf16 v[32:35], a[186:189], v[100:103], v[28:31]\n"
  "ds_read_b64_tr_b16 a[192:193], v13 offset:0\n"
  "buffer_load_dwordx4 v3, s[52:55], s30 offen offset:0 nt lds\n"
  "v_mfma_f32_16x16x32_bf16 v[36:39], a[184:187], v[100:103], v[28:31]\n"
  "ds_read_b64_tr_b16 a[194:195], v13 offset:1792\n"
  "s_waitcnt lgkmcnt(8)\n"
  "v_mfma_f32_16x16x32_bf16 v[216:219], a[236:239], v[88:91], v[216:219]\n"
  "ds_read_b64_tr_b16 a[196:197], v14 offset:32\n"
  "v_mfma_f32_16x16x32_bf16 v[220:223], a[236:239], v[92:95], v[220:223]\n"
  "ds_read_b64_tr_b16 a[198:199], v14 offset:1824\n"
  "v_mfma_f32_16x16x32_bf16 v[224:227], a[240:243], v[88:91], v[224:227]\n"
  "ds_read_b64_tr_b16 a[200:201], v13 offset:64\n"
  "v_cvt_pk_bf16_f32 v100, v32, v33\n"
  "v_mfma_f32_16x16x32_bf16 v[228:231], a[240:243], v[92:95], v[228:231]\n"
  "ds_read_b64_tr_b16 a[202:203], v13 offset:1856\n"
  "v_cvt_pk_bf16_f32 v101, v34, v35\n"
  "s_waitcnt lgkmcnt(8)\n"
  "v_mfma_f32_16x16x32_bf16 v[232:235], a[244:247], v[88:91], v[232:235]\n"
  "v_cvt_pk_bf16_f32 v102, v36, v37\n"
  "s_mov_b32 s30, 64640\n"
  "v_mfma_f32_16x16x32_bf16 v[236:239], a[244:247], v[92:95], v[236:239]\n"
  "buffer_load_dwordx4 v3, s[52:55], s30 offen offset:1024 nt lds\n"
  "v_cvt_pk_bf16_f32 v103, v38, v39\n"
  "v_mfma_f32_16x16x32_bf16 v[240:243], a[248:251], v[88:91], v[240:243]\n"
  "ds_read_b64_tr_b16 a[204:205], v14 offset:96\n"
  "v_mfma_f32_16x16x32_bf16 v[244:247], a[248:251], v[92:95], v[244:247]\n"
  "ds_read_b64_tr_b16 a[206:207], v14 offset:1888\n"
  "s_waitcnt lgkmcnt(6)\n"
  "v_mfma_f32_16x16x32_bf16 v[248:251], a[252:255], v[88:91], v[248:251]\n"
  "ds_read_b64_tr_b16 a[208:209], v13 offset:128\n"
  "v_mfma_f32_16x16x32_bf16 v[252:255], a[252:255], v[92:95], v[252:255]\n"
  "ds_read_b64_tr_b16 a[210:211], v13 offset:1920\n"
  "v_mfma_f32_16x16x32_bf16 v[144:147], a[192:195], v[96:99], v[144:147]\n"
  "ds_read_b64_tr_b16 a[212:213], v14 offset:160\n"
  "v_mfma_f32_16x16x32_bf16 v[148:151], a[192:195], v[100:103], v[148:151]\n"
  "ds_read_b64_tr_b16 a[214:215], v14 offset:1952\n"
  "s_waitcnt lgkmcnt(6)\n"
  "v_mfma_f32_16x16x32_bf16 v[152:155], a[196:199], v[96:99], v[152:155]\n"
  "ds_read_b64_tr_b16 a[216:217], v13 offset:192\n"
  "s_mov_b32 s30, 14464\n"
  "v_mfma_f32_16x16x32_bf16 v[156:159], a[196:199], v[100:103], v[156:159]\n"
  "buffer_load_dwordx4 v3, s[52:55], s30 offen offset:2048 nt lds\n"
  "v_mfma_f32_16x16x32_bf16 v[160:163], a[200:203], v[96:99], v[160:163]\n"
  "ds_read_b64_tr_b16 a[218:219], v13 offset:1984\n"
  "ds_read_b64_tr_b16 a[220:221], v14 offset:224\n"
  "v_mfma_f32_16x16x32_bf16 v[164:167], a[200:203], v[100:103], v[164:167]\n"
  "ds_read_b64_tr_b16 a[222:223], v14 offset:2016\n"
  "v_mfma_f32_16x16x32_bf16 v[32:35], a[186:189], v[104:107], v[24:27]\n"
  "ds_read_b64_tr_b16 a[224:225], v13 offset:256\n"
  "v_mfma_f32_16x16x32_bf16 v[36:39], a[184:187], v[104:107], v[24:27]\n"
  "ds_read_b64_tr_b16 a[226:227], v13 offset:2048\n"
  "s_waitcnt lgkmcnt(8)\n"
  "v_mfma_f32_16x16x32_bf16 v[168:171], a[204:207], v[96:99], v[168:171]\n"
  "v_mfma_f32_16x16x32_bf16 v[172:175], a[204:207], v[100:103], v[172:175]\n"
  "v_mfma_f32_16x16x32_bf16 v[176:179], a[208:211], v[96:99], v[176:179]\n"
  "ds_read_b64_tr_b16 a[228:229], v14 offset:288\n"
  "v_cvt_pk_bf16_f32 v104, v32, v33\n"
  "s_mov_b32 s30, 78976\n"
  "v_mfma_f32_16x16x32_bf16 v[180:183], a[208:211], v[100:103], v[180:183]\n"
  "buffer_load_dwordx4 v3, s[52:55], s30 offen offset:3072 nt lds\n"
  "v_cvt_pk_bf16_f32 v105, v34, v35\n"
  "s_waitcnt lgkmcnt(6)\n"
  "v_mfma_f32_16x16x32_bf16 v[184:187], a[212:215], v[96:99], v[184:187]\n"
  "ds_read_b64_tr_b16 a[230:231], v14 offset:2080\n"
  "ds_read_b64_tr_b16 a[232:233], v13 offset:320\n"
  "v_cvt_pk_bf16_f32 v106, v36, v37\n"
  "v_mfma_f32_16x16x32_bf16 v[188:191], a[212:215], v[100:103], v[188:191]\n"
  "ds_read_b64_tr_b16 a[234:235], v13 offset:2112\n"
  "v_cvt_pk_bf16_f32 v107, v38, v39\n"
  "s_waitcnt lgkmcnt(6)\n"
  "v_mfma_f32_16x16x32_bf16 v[192:195], a[216:219], v[96:99], v[192:195]\n"
  "ds_read_b64_tr_b16 a[236:237], v14 offset:352\n"
  "v_mfma_f32_16x16x32_bf16 v[196:199], a[216:219], v[100:103], v[196:199]\n"
  "ds_read_b64_tr_b16 a[238:239], v14 offset:2144\n"
  "v_mfma_f32_16x16x32_bf16 v[200:203], a[220:223], v[96:99], v[200:203]\n"
  "ds_read_b64_tr_b16 a[240:241], v13 offset:384\n"
  "v_mfma_f32_16x16x32_bf16 v[204:207], a[220:223], v[100:103], v[204:207]\n"
  "ds_read_b64_tr_b16 a[242:243], v13 offset:2176\n"
  "s_waitcnt lgkmcnt(8)\n"
  "v_mfma_f32_16x16x32_bf16 v[208:211], a[224:227], v[96:99], v[208:211]\n"
  "ds_read_b64_tr_b16 a[244:245], v14 offset:416\n"
  "v_mfma_f32_16x16x32_bf16 v[212:215], a[224:227], v[100:103], v[212:215]\n"
  "s_waitcnt vmcnt(17)\n"
  "s_barrier\n"
  "ds_read_b64_tr_b16 a[246:247], v14 offset:2208\n"
  "s_add_u32 m0, s35, 65536\n"
  "s_mov_b32 s31, 1376256\n"
  "v_mfma_f32_16x16x32_bf16 v[32:35], a[186:189], v[108:111], v[28:31]\n"
  "buffer_load_dwordx4 v5, s[44:47], s31 offen offset:0 lds\n"
  "v_mfma_f32_16x16x32_bf16 v[36:39], a[184:187], v[108:111], v[28:31]\n"
  "ds_read_b64_tr_b16 a[248:249], v13 offset:16384\n"
  "ds_read_b64_tr_b16 a[250:251], v13 offset:18176\n"
  "s_waitcnt vmcnt(10)\n"
  "ds_read_b128 a[32:35], v9 offset:0\n"
  "s_waitcnt lgkmcnt(9)\n"
  "v_mfma_f32_16x16x32_bf16 v[216:219], a[228:231], v[96:99], v[216:219]\n"
  "ds_read_b64_tr_b16 a[252:253], v14 offset:16416\n"
  "v_mfma_f32_16x16x32_bf16 v[220:223], a[228:231], v[100:103], v[220:223]\n"
  "buffer_load_dwordx4 v6, s[44:47], s31 offen offset:1024 lds\n"
  "v_mfma_f32_16x16x32_bf16 v[224:227], a[232:235], v[96:99], v[224:227]\n"
  "ds_read_b64_tr_b16 a[254:255], v14 offset:18208\n"
  "ds_read_b128 a[36:39], v9 offset:2048\n"
  "ds_read_b64_tr_b16 a[192:193], v13 offset:16448\n"
  "v_cvt_pk_bf16_f32 v108, v32, v33\n"
  "v_mfma_f32_16x16x32_bf16 v[228:231], a[232:235], v[100:103], v[228:231]\n"
  "ds_read_b64_tr_b16 a[194:195], v13 offset:18240\n"
  "ds_read_b128 a[40:43], v10 offset:0\n"
  "v_cvt_pk_bf16_f32 v109, v34, v35\n"
  "s_waitcnt lgkmcnt(11)\n"
  "v_mfma_f32_16x16x32_bf16 v[232:235], a[236:239], v[96:99], v[232:235]\n"
  "v_cvt_pk_bf16_f32 v110, v36, v37\n"
  "v_mfma_f32_16x16x32_bf16 v[236:239], a[236:239], v[100:103], v[236:239]\n"
  "buffer_load_dwordx4 v7, s[44:47], s31 offen offset:2048 lds\n"
  "v_cvt_pk_bf16_f32 v111, v38, v39\n"
  "v_mfma_f32_16x16x32_bf16 v[240:243], a[240:243], v[96:99], v[240:243]\n"
  "ds_read_b128 a[44:47], v10 offset:2048\n"
  "ds_read_b64_tr_b16 a[196:197], v14 offset:16480\n"
  "v_mfma_f32_16x16x32_bf16 v[244:247], a[240:243], v[100:103], v[244:247]\n"
  "ds_read_b64_tr_b16 a[198:199], v14 offset:18272\n"
  "s_waitcnt lgkmcnt(9)\n"
  "v_mfma_f32_16x16x32_bf16 v[248:251], a[244:247], v[96:99], v[248:251]\n"
  "ds_read_b64_tr_b16 a[200:201], v13 offset:16512\n"
  "v_mfma_f32_16x16x32_bf16 v[252:255], a[244:247], v[100:103], v[252:255]\n"
  "s_mov_b32 exec_hi, 0\n"
  "buffer_load_dwordx4 v8, s[44:47], s31 offen offset:3072 lds\n"
  "s_mov_b32 exec_hi, -1\n"
  "v_mfma_f32_16x16x32_bf16 v[144:147], a[248:251], v[104:107], v[144:147]\n"
  "ds_read_b64_tr_b16 a[202:203], v13 offset:18304\n"
  "ds_read_b64_tr_b16 a[204:205], v14 offset:16544\n"
  "v_mfma_f32_16x16x32_bf16 v[148:151], a[248:251], v[108:111], v[148:151]\n"
  "ds_read_b64_tr_b16 a[206:207], v14 offset:18336\n"
  "s_waitcnt lgkmcnt(7)\n"
  "v_mfma_f32_16x16x32_bf16 v[152:155], a[252:255], v[104:107], v[152:155]\n"
  "ds_read_b64_tr_b16 a[208:209], v13 offset:16576\n"
  "s_add_u32 m0, s35, 81920\n"
  "s_mov_b32 s31, 1490944\n"
  "v_mfma_f32_16x16x32_bf16 v[156:159], a[252:255], v[108:111], v[156:159]\n"
  "buffer_load_dwordx4 v5, s[44:47], s31 offen offset:0 lds\n"
  "v_mfma_f32_16x16x32_bf16 v[160:163], a[192:195], v[104:107], v[160:163]\n"
  "ds_read_b64_tr_b16 a[210:211], v13 offset:18368\n"
  "ds_read_b64_tr_b16 a[212:213], v14 offset:16608\n"
  "v_mfma_f32_16x16x32_bf16 v[164:167], a[192:195], v[108:111], v[164:167]\n"
  "ds_read_b64_tr_b16 a[214:215], v14 offset:18400\n"
  "v_mfma_f32_16x16x32_bf16 v[32:35], a[186:189], v[112:115], v[24:27]\n"
  "ds_read_b64_tr_b16 a[216:217], v13 offset:16640\n"
  "v_mfma_f32_16x16x32_bf16 v[36:39], a[184:187], v[112:115], v[24:27]\n"
  "buffer_load_dwordx4 v6, s[44:47], s31 offen offset:1024 lds\n"
  "s_waitcnt lgkmcnt(8)\n"
  "v_mfma_f32_16x16x32_bf16 v[168:171], a[196:199], v[104:107], v[168:171]\n"
  "ds_read_b64_tr_b16 a[218:219], v13 offset:18432\n"
  "v_mfma_f32_16x16x32_bf16 v[172:175], a[196:199], v[108:111], v[172:175]\n"
  "s_waitcnt lgkmcnt(6)\n"
  "v_mfma_f32_16x16x32_bf16 v[176:179], a[200:203], v[104:107], v[176:179]\n"
  "ds_read_b64_tr_b16 a[220:221], v14 offset:16672\n"
  "v_cvt_pk_bf16_f32 v112, v32, v33\n"
  "v_mfma_f32_16x16x32_bf16 v[180:183], a[200:203], v[108:111], v[180:183]\n"
  "buffer_load_dwordx4 v7, s[44:47], s31 offen offset:2048 lds\n"
  "v_cvt_pk_bf16_f32 v113, v34, v35\n"
  "v_mfma_f32_16x16x32_bf16 v[184:187], a[204:207], v[104:107], v[184:187]\n"
  "ds_read_b64_tr_b16 a[222:223], v14 offset:18464\n"
  "ds_read_b64_tr_b16 a[224:225], v13 offset:16704\n"
  "v_cvt_pk_bf16_f32 v114, v36, v37\n"
  "v_mfma_f32_16x16x32_bf16 v[188:191], a[204:207], v[108:111], v[188:191]\n"
  "ds_read_b64_tr_b16 a[226:227], v13 offset:18496\n"
  "v_cvt_pk_bf16_f32 v115, v38, v39\n"
  "s_waitcnt lgkmcnt(6)\n"
  "v_mfma_f32_16x16x32_bf16 v[192:195], a[208:211], v[104:107], v[192:195]\n"
  "ds_read_b64_tr_b16 a[228:229], v14 offset:16736\n"
  "v_mfma_f32_16x16x32_bf16 v[196:199], a[208:211], v[108:111], v[196:199]\n"
  "s_mov_b32 exec_hi, 0\n"
  "buffer_load_dwordx4 v8, s[44:47], s31 offen offset:3072 lds\n"
  "s_mov_b32 exec_hi, -1\n"
  "v_mfma_f32_16x16x32_bf16 v[200:203], a[212:215], v[104:107], v[200:203]\n"
  "ds_read_b64_tr_b16 a[230:231], v14 offset:18528\n"
  "ds_read_b64_tr_b16 a[232:233], v13 offset:16768\n"
  "v_mfma_f32_16x16x32_bf16 v[204:207], a[212:215], v[108:111], v[204:207]\n"
  "ds_read_b64_tr_b16 a[234:235], v13 offset:18560\n"
  "s_waitcnt lgkmcnt(8)\n"
  "v_mfma_f32_16x16x32_bf16 v[208:211], a[216:219], v[104:107], v[208:211]\n"
  "ds_read_b64_tr_b16 a[236:237], v14 offset:16800\n"
  "v_mfma_f32_16x16x32_bf16 v[212:215], a[216:219], v[108:111], v[212:215]\n"
  "s_barrier\n"
  "ds_read_b64_tr_b16 a[238:239], v14 offset:18592\n"
  "s_add_u32 m0, s35, 0\n"
  "s_mov_b32 s31, 0\n"
  "v_mfma_f32_16x16x32_bf16 v[32:35], a[186:189], v[116:119], v[28:31]\n"
  "v_accvgpr_read_b32 v5, a176\n"
  "v_accvgpr_read_b32 v6, a177\n"
  "v_accvgpr_read_b32 v7, a178\n"
  "v_accvgpr_read_b32 v8, a179\n"
  "buffer_load_dwordx4 v5, s[48:51], s31 offen offset:0 lds\n"
  "v_mfma_f32_16x16x32_bf16 v[36:39], a[184:187], v[116:119], v[28:31]\n"
  "ds_read_b64_tr_b16 a[240:241], v13 offset:32768\n"
  "ds_read_b64_tr_b16 a[242:243], v13 offset:34560\n"
  "s_waitcnt lgkmcnt(8)\n"
  "v_mfma_f32_16x16x32_bf16 v[216:219], a[220:223], v[104:107], v[216:219]\n"
  "ds_read_b64_tr_b16 a[244:245], v14 offset:32800\n"
  "v_mfma_f32_16x16x32_bf16 v[220:223], a[220:223], v[108:111], v[220:223]\n"
  "ds_read_b64_tr_b16 a[246:247], v14 offset:34592\n"
  "v_mfma_f32_16x16x32_bf16 v[224:227], a[224:227], v[104:107], v[224:227]\n"
  "buffer_load_dwordx4 v6, s[48:51], s31 offen offset:1024 lds\n"
  "v_cvt_pk_bf16_f32 v116, v32, v33\n"
  "v_mfma_f32_16x16x32_bf16 v[228:231], a[224:227], v[108:111], v[228:231]\n"
  "ds_read_b64_tr_b16 a[248:249], v13 offset:32832\n"
  "ds_read_b64_tr_b16 a[250:251], v13 offset:34624\n"
  "v_cvt_pk_bf16_f32 v117, v34, v35\n"
  "s_waitcnt lgkmcnt(8)\n"
  "v_mfma_f32_16x16x32_bf16 v[232:235], a[228:231], v[104:107], v[232:235]\n"
  "v_cvt_pk_bf16_f32 v118, v36, v37\n"
  "v_mfma_f32_16x16x32_bf16 v[236:239], a[228:231], v[108:111], v[236:239]\n"
  "v_cvt_pk_bf16_f32 v119, v38, v39\n"
  "v_mfma_f32_16x16x32_bf16 v[240:243], a[232:235], v[104:107], v[240:243]\n"
  "ds_read_b64_tr_b16 a[252:253], v14 offset:32864\n"
  "v_mfma_f32_16x16x32_bf16 v[244:247], a[232:235], v[108:111], v[244:247]\n"
  "buffer_load_dwordx4 v7, s[48:51], s31 offen offset:2048 lds\n"
  "s_waitcnt lgkmcnt(5)\n"
  "v_mfma_f32_16x16x32_bf16 v[248:251], a[236:239], v[104:107], v[248:251]\n"
  "ds_read_b64_tr_b16 a[254:255], v14 offset:34656\n"
  "ds_read_b64_tr_b16 a[192:193], v13 offset:32896\n"
  "v_mfma_f32_16x16x32_bf16 v[252:255], a[236:239], v[108:111], v[252:255]\n"
  "ds_read_b64_tr_b16 a[194:195], v13 offset:34688\n"
  "v_mfma_f32_16x16x32_bf16 v[144:147], a[240:243], v[112:115], v[144:147]\n"
  "ds_read_b64_tr_b16 a[196:197], v14 offset:32928\n"
  "v_mfma_f32_16x16x32_bf16 v[148:151], a[240:243], v[116:119], v[148:151]\n"
  "ds_read_b64_tr_b16 a[198:199], v14 offset:34720\n"
  "s_waitcnt lgkmcnt(6)\n"
  "v_mfma_f32_16x16x32_bf16 v[152:155], a[244:247], v[112:115], v[152:155]\n"
  "s_mov_b32 exec_hi, 0\n"
  "buffer_load_dwordx4 v8, s[48:51], s31 offen offset:3072 lds\n"
  "s_mov_b32 exec_hi, -1\n"
  "v_mfma_f32_16x16x32_bf16 v[156:159], a[244:247], v[116:119], v[156:159]\n"
  "ds_read_b64_tr_b16 a[200:201], v13 offset:32960\n"
  "ds_read_b64_tr_b16 a[202:203], v13 offset:34752\n"
  "v_mfma_f32_16x16x32_bf16 v[160:163], a[248:251], v[112:115], v[160:163]\n"
  "ds_read_b64_tr_b16 a[204:205], v14 offset:32992\n"
  "v_mfma_f32_16x16x32_bf16 v[164:167], a[248:251], v[116:119], v[164:167]\n"
  "ds_read_b64_tr_b16 a[206:207], v14 offset:34784\n"
  "v_mfma_f32_16x16x32_bf16 v[32:35], a[186:189], v[120:123], v[24:27]\n"
  "ds_read_b64_tr_b16 a[208:209], v13 offset:33024\n"
  "s_add_u32 m0, s34, 8192\n"
  "s_mov_b32 s30, 0\n"
  "v_mfma_f32_16x16x32_bf16 v[36:39], a[184:187], v[120:123], v[24:27]\n"
  "ds_read_b64_tr_b16 a[210:211], v13 offset:34816\n"
  "v_xor_b32 v3, 0x300, v3\n"
  "buffer_load_dwordx4 v3, s[52:55], s30 offen offset:0 nt lds\n"
  "s_waitcnt lgkmcnt(8)\n"
  "v_mfma_f32_16x16x32_bf16 v[168:171], a[252:255], v[112:115], v[168:171]\n"
  "v_mfma_f32_16x16x32_bf16 v[172:175], a[252:255], v[116:119], v[172:175]\n"
  "s_mov_b32 s30, 64512\n"
  "v_mfma_f32_16x16x32_bf16 v[176:179], a[192:195], v[112:115], v[176:179]\n"
  "buffer_load_dwordx4 v3, s[52:55], s30 offen offset:1024 nt lds\n"
  "v_cvt_pk_bf16_f32 v120, v32, v33\n"
  "v_mfma_f32_16x16x32_bf16 v[180:183], a[192:195], v[116:119], v[180:183]\n"
  "ds_read_b64_tr_b16 a[212:213], v14 offset:33056\n"
  "ds_read_b64_tr_b16 a[214:215], v14 offset:34848\n"
  "v_cvt_pk_bf16_f32 v121, v34, v35\n"
  "s_waitcnt lgkmcnt(6)\n"
  "v_mfma_f32_16x16x32_bf16 v[184:187], a[196:199], v[112:115], v[184:187]\n"
  "ds_read_b64_tr_b16 a[216:217], v13 offset:33088\n"
  "v_cvt_pk_bf16_f32 v122, v36, v37\n"
  "s_mov_b32 s30, 14336\n"
  "v_mfma_f32_16x16x32_bf16 v[188:191], a[196:199], v[116:119], v[188:191]\n"
  "buffer_load_dwordx4 v3, s[52:55], s30 offen offset:2048 nt lds\n"
  "v_cvt_pk_bf16_f32 v123, v38, v39\n"
  "v_mfma_f32_16x16x32_bf16 v[192:195], a[200:203], v[112:115], v[192:195]\n"
  "ds_read_b64_tr_b16 a[218:219], v13 offset:34880\n"
  "ds_read_b64_tr_b16 a[220:221], v14 offset:33120\n"
  "v_mfma_f32_16x16x32_bf16 v[196:199], a[200:203], v[116:119], v[196:199]\n"
  "ds_read_b64_tr_b16 a[222:223], v14 offset:34912\n"
  "s_mov_b32 s30, 78848\n"
  "s_waitcnt lgkmcnt(6)\n"
  "v_mfma_f32_16x16x32_bf16 v[200:203], a[204:207], v[112:115], v[200:203]\n"
  "buffer_load_dwordx4 v3, s[52:55], s30 offen offset:3072 nt lds\n"
  "v_mfma_f32_16x16x32_bf16 v[204:207], a[204:207], v[116:119], v[204:207]\n"
  "ds_read_b64_tr_b16 a[224:225], v13 offset:33152\n"
  "ds_read_b64_tr_b16 a[226:227], v13 offset:34944\n"
  "v_mfma_f32_16x16x32_bf16 v[208:211], a[208:211], v[112:115], v[208:211]\n"
  "ds_read_b64_tr_b16 a[228:229], v14 offset:33184\n"
  "v_mfma_f32_16x16x32_bf16 v[212:215], a[208:211], v[116:119], v[212:215]\n"
  "s_waitcnt vmcnt(20)\n"
  "s_barrier\n"
  "ds_read_b64_tr_b16 a[230:231], v14 offset:34976\n"
  "s_add_u32 m0, s34, 12288\n"
  "s_mov_b32 s30, 128\n"
  "v_mfma_f32_16x16x32_bf16 v[32:35], a[186:189], v[124:127], v[28:31]\n"
  "ds_read_b64_tr_b16 a[232:233], v13 offset:49152\n"
  "buffer_load_dwordx4 v3, s[52:55], s30 offen offset:0 nt lds\n"
  "v_mfma_f32_16x16x32_bf16 v[36:39], a[184:187], v[124:127], v[28:31]\n"
  "ds_read_b64_tr_b16 a[234:235], v13 offset:50944\n"
  "s_waitcnt vmcnt(17)\n"
  "ds_read_b128 a[48:51], v9 offset:4096\n"
  "s_waitcnt lgkmcnt(10)\n"
  "v_mfma_f32_16x16x32_bf16 v[216:219], a[212:215], v[112:115], v[216:219]\n"
  "ds_read_b64_tr_b16 a[236:237], v14 offset:49184\n"
  "v_mfma_f32_16x16x32_bf16 v[220:223], a[212:215], v[116:119], v[220:223]\n"
  "ds_read_b64_tr_b16 a[238:239], v14 offset:50976\n"
  "ds_read_b128 a[52:55], v9 offset:6144\n"
  "s_waitcnt lgkmcnt(10)\n"
  "v_mfma_f32_16x16x32_bf16 v[224:227], a[216:219], v[112:115], v[224:227]\n"
  "ds_read_b64_tr_b16 a[240:241], v13 offset:49216\n"
  "v_cvt_pk_bf16_f32 v124, v32, v33\n"
  "v_mfma_f32_16x16x32_bf16 v[228:231], a[216:219], v[116:119], v[228:231]\n"
  "ds_read_b64_tr_b16 a[242:243], v13 offset:51008\n"
  "ds_read_b128 a[56:59], v10 offset:4096\n"
  "v_cvt_pk_bf16_f32 v125, v34, v35\n"
  "v_mfma_f32_16x16x32_bf16 v[232:235], a[220:223], v[112:115], v[232:235]\n"
  "v_cvt_pk_bf16_f32 v126, v36, v37\n"
  "s_mov_b32 s30, 64640\n"
  "v_mfma_f32_16x16x32_bf16 v[236:239], a[220:223], v[116:119], v[236:239]\n"
  "buffer_load_dwordx4 v3, s[52:55], s30 offen offset:1024 nt lds\n"
  "v_cvt_pk_bf16_f32 v127, v38, v39\n"
  "s_waitcnt lgkmcnt(9)\n"
  "v_mfma_f32_16x16x32_bf16 v[240:243], a[224:227], v[112:115], v[240:243]\n"
  "ds_read_b128 a[60:63], v10 offset:6144\n"
  "ds_read_b64_tr_b16 a[244:245], v14 offset:49248\n"
  "v_mfma_f32_16x16x32_bf16 v[244:247], a[224:227], v[116:119], v[244:247]\n"
  "ds_read_b64_tr_b16 a[246:247], v14 offset:51040\n"
  "v_mfma_f32_16x16x32_bf16 v[248:251], a[228:231], v[112:115], v[248:251]\n"
  "ds_read_b64_tr_b16 a[248:249], v13 offset:49280\n"
  "v_mfma_f32_16x16x32_bf16 v[252:255], a[228:231], v[116:119], v[252:255]\n"
  "ds_read_b64_tr_b16 a[250:251], v13 offset:51072\n"
  "s_waitcnt lgkmcnt(8)\n"
  "v_mfma_f32_16x16x32_bf16 v[144:147], a[232:235], v[120:123], v[144:147]\n"
  "ds_read_b64_tr_b16 a[252:253], v14 offset:49312\n"
  "v_mfma_f32_16x16x32_bf16 v[148:151], a[232:235], v[124:127], v[148:151]\n"
  "ds_read_b64_tr_b16 a[254:255], v14 offset:51104\n"
  "v_mfma_f32_16x16x32_bf16 v[152:155], a[236:239], v[120:123], v[152:155]\n"
  "ds_read_b64_tr_b16 a[192:193], v13 offset:49344\n"
  "s_mov_b32 s30, 14464\n"
  "v_mfma_f32_16x16x32_bf16 v[156:159], a[236:239], v[124:127], v[156:159]\n"
  "buffer_load_dwordx4 v3, s[52:55], s30 offen offset:2048 nt lds\n"
  "s_waitcnt lgkmcnt(8)\n"
  "v_mfma_f32_16x16x32_bf16 v[160:163], a[240:243], v[120:123], v[160:163]\n"
  "ds_read_b64_tr_b16 a[194:195], v13 offset:51136\n"
  "ds_read_b64_tr_b16 a[196:197], v14 offset:49376\n"
  "v_mfma_f32_16x16x32_bf16 v[164:167], a[240:243], v[124:127], v[164:167]\n"
  "ds_read_b64_tr_b16 a[198:199], v14 offset:51168\n"
  "v_mfma_f32_16x16x32_bf16 v[32:35], a[186:189], v[128:131], v[24:27]\n"
  "ds_read_b64_tr_b16 a[200:201], v13 offset:49408\n"
  "v_mfma_f32_16x16x32_bf16 v[36:39], a[184:187], v[128:131], v[24:27]\n"
  "ds_read_b64_tr_b16 a[202:203], v13 offset:51200\n"
  "s_waitcnt lgkmcnt(8)\n"
  "v_mfma_f32_16x16x32_bf16 v[168:171], a[244:247], v[120:123], v[168:171]\n"
  "v_mfma_f32_16x16x32_bf16 v[172:175], a[244:247], v[124:127], v[172:175]\n"
  "v_mfma_f32_16x16x32_bf16 v[176:179], a[248:251], v[120:123], v[176:179]\n"
  "ds_read_b64_tr_b16 a[204:205], v14 offset:49440\n"
  "v_cvt_pk_bf16_f32 v128, v32, v33\n"
  "s_mov_b32 s30, 78976\n"
  "v_mfma_f32_16x16x32_bf16 v[180:183], a[248:251], v[124:127], v[180:183]\n"
  "buffer_load_dwordx4 v3, s[52:55], s30 offen offset:3072 nt lds\n"
  "v_cvt_pk_bf16_f32 v129, v34, v35\n"
  "s_waitcnt lgkmcnt(6)\n"
  "v_mfma_f32_16x16x32_bf16 v[184:187], a[252:255], v[120:123], v[184:187]\n"
  "ds_read_b64_tr_b16 a[206:207], v14 offset:51232\n"
  "ds_read_b64_tr_b16 a[208:209], v13 offset:49472\n"
  "v_cvt_pk_bf16_f32 v130, v36, v37\n"
  "v_mfma_f32_16x16x32_bf16 v[188:191], a[252:255], v[124:127], v[188:191]\n"
  "ds_read_b64_tr_b16 a[210:211], v13 offset:51264\n"
  "v_cvt_pk_bf16_f32 v131, v38, v39\n"
  "s_waitcnt lgkmcnt(6)\n"
  "v_mfma_f32_16x16x32_bf16 v[192:195], a[192:195], v[120:123], v[192:195]\n"
  "ds_read_b64_tr_b16 a[212:213], v14 offset:49504\n"
  "v_mfma_f32_16x16x32_bf16 v[196:199], a[192:195], v[124:127], v[196:199]\n"
  "ds_read_b64_tr_b16 a[214:215], v14 offset:51296\n"
  "v_mfma_f32_16x16x32_bf16 v[200:203], a[196:199], v[120:123], v[200:203]\n"
  "ds_read_b64_tr_b16 a[216:217], v13 offset:49536\n"
  "v_mfma_f32_16x16x32_bf16 v[204:207], a[196:199], v[124:127], v[204:207]\n"
  "ds_read_b64_tr_b16 a[218:219], v13 offset:51328\n"
  "s_waitcnt lgkmcnt(8)\n"
  "v_mfma_f32_16x16x32_bf16 v[208:211], a[200:203], v[120:123], v[208:211]\n"
  "ds_read_b64_tr_b16 a[220:221], v14 offset:49568\n"
  "v_mfma_f32_16x16x32_bf16 v[212:215], a[200:203], v[124:127], v[212:215]\n"
  "s_waitcnt vmcnt(16)\n"
  "s_barrier\n"
  "ds_read_b64_tr_b16 a[222:223], v14 offset:51360\n"
  "v_mfma_f32_16x16x32_bf16 v[32:35], a[186:189], v[132:135], v[28:31]\n"
  "ds_read_b64_tr_b16 a[224:225], v15 offset:0\n"
  "v_mfma_f32_16x16x32_bf16 v[36:39], a[184:187], v[132:135], v[28:31]\n"
  "ds_read_b64_tr_b16 a[226:227], v15 offset:1792\n"
  "s_waitcnt lgkmcnt(8)\n"
  "v_mfma_f32_16x16x32_bf16 v[216:219], a[204:207], v[120:123], v[216:219]\n"
  "ds_read_b64_tr_b16 a[228:229], v16 offset:32\n"
  "v_mfma_f32_16x16x32_bf16 v[220:223], a[204:207], v[124:127], v[220:223]\n"
  "ds_read_b64_tr_b16 a[230:231], v16 offset:1824\n"
  "v_mfma_f32_16x16x32_bf16 v[224:227], a[208:211], v[120:123], v[224:227]\n"
  "ds_read_b64_tr_b16 a[232:233], v15 offset:64\n"
  "v_cvt_pk_bf16_f32 v132, v32, v33\n"
  "v_mfma_f32_16x16x32_bf16 v[228:231], a[208:211], v[124:127], v[228:231]\n"
  "ds_read_b64_tr_b16 a[234:235], v15 offset:1856\n"
  "v_cvt_pk_bf16_f32 v133, v34, v35\n"
  "s_waitcnt lgkmcnt(8)\n"
  "v_mfma_f32_16x16x32_bf16 v[232:235], a[212:215], v[120:123], v[232:235]\n"
  "v_cvt_pk_bf16_f32 v134, v36, v37\n"
  "v_mfma_f32_16x16x32_bf16 v[236:239], a[212:215], v[124:127], v[236:239]\n"
  "v_cvt_pk_bf16_f32 v135, v38, v39\n"
  "v_mfma_f32_16x16x32_bf16 v[240:243], a[216:219], v[120:123], v[240:243]\n"
  "ds_read_b64_tr_b16 a[236:237], v16 offset:96\n"
  "v_mfma_f32_16x16x32_bf16 v[244:247], a[216:219], v[124:127], v[244:247]\n"
  "ds_read_b64_tr_b16 a[238:239], v16 offset:1888\n"
  "s_waitcnt lgkmcnt(6)\n"
  "v_mfma_f32_16x16x32_bf16 v[248:251], a[220:223], v[120:123], v[248:251]\n"
  "ds_read_b64_tr_b16 a[240:241], v15 offset:128\n"
  "v_mfma_f32_16x16x32_bf16 v[252:255], a[220:223], v[124:127], v[252:255]\n"
  "ds_read_b64_tr_b16 a[242:243], v15 offset:1920\n"
  "v_mfma_f32_16x16x32_bf16 v[144:147], a[224:227], v[128:131], v[144:147]\n"
  "ds_read_b64_tr_b16 a[244:245], v16 offset:160\n"
  "v_mfma_f32_16x16x32_bf16 v[148:151], a[224:227], v[132:135], v[148:151]\n"
  "ds_read_b64_tr_b16 a[246:247], v16 offset:1952\n"
  "s_waitcnt lgkmcnt(6)\n"
  "v_mfma_f32_16x16x32_bf16 v[152:155], a[228:231], v[128:131], v[152:155]\n"
  "ds_read_b64_tr_b16 a[248:249], v15 offset:192\n"
  "v_mfma_f32_16x16x32_bf16 v[156:159], a[228:231], v[132:135], v[156:159]\n"
  "ds_read_b64_tr_b16 a[250:251], v15 offset:1984\n"
  "v_mfma_f32_16x16x32_bf16 v[160:163], a[232:235], v[128:131], v[160:163]\n"
  "ds_read_b64_tr_b16 a[252:253], v16 offset:224\n"
  "v_mfma_f32_16x16x32_bf16 v[164:167], a[232:235], v[132:135], v[164:167]\n"
  "ds_read_b64_tr_b16 a[254:255], v16 offset:2016\n"
  "v_mfma_f32_16x16x32_bf16 v[32:35], a[186:189], v[136:139], v[24:27]\n"
  "ds_read_b64_tr_b16 a[192:193], v15 offset:256\n"
  "v_mfma_f32_16x16x32_bf16 v[36:39], a[184:187], v[136:139], v[24:27]\n"
  "ds_read_b64_tr_b16 a[194:195], v15 offset:2048\n"
  "s_waitcnt lgkmcnt(8)\n"
  "v_mfma_f32_16x16x32_bf16 v[168:171], a[236:239], v[128:131], v[168:171]\n"
  "v_mfma_f32_16x16x32_bf16 v[172:175], a[236:239], v[132:135], v[172:175]\n"
  "v_mfma_f32_16x16x32_bf16 v[176:179], a[240:243], v[128:131], v[176:179]\n"
  "ds_read_b64_tr_b16 a[196:197], v16 offset:288\n"
  "v_cvt_pk_bf16_f32 v136, v32, v33\n"
  "v_mfma_f32_16x16x32_bf16 v[180:183], a[240:243], v[132:135], v[180:183]\n"
  "ds_read_b64_tr_b16 a[198:199], v16 offset:2080\n"
  "v_cvt_pk_bf16_f32 v137, v34, v35\n"
  "s_waitcnt lgkmcnt(6)\n"
  "v_mfma_f32_16x16x32_bf16 v[184:187], a[244:247], v[128:131], v[184:187]\n"
  "ds_read_b64_tr_b16 a[200:201], v15 offset:320\n"
  "v_cvt_pk_bf16_f32 v138, v36, v37\n"
  "v_mfma_f32_16x16x32_bf16 v[188:191], a[244:247], v[132:135], v[188:191]\n"
  "ds_read_b64_tr_b16 a[202:203], v15 offset:2112\n"
  "v_cvt_pk_bf16_f32 v139, v38, v39\n"
  "v_mfma_f32_16x16x32_bf16 v[192:195], a[248:251], v[128:131], v[192:195]\n"
  "ds_read_b64_tr_b16 a[204:205], v16 offset:352\n"
  "v_mfma_f32_16x16x32_bf16 v[196:199], a[248:251], v[132:135], v[196:199]\n"
  "ds_read_b64_tr_b16 a[206:207], v16 offset:2144\n"
  "s_waitcnt lgkmcnt(6)\n"
  "v_mfma_f32_16x16x32_bf16 v[200:203], a[252:255], v[128:131], v[200:203]\n"
  "ds_read_b64_tr_b16 a[208:209], v15 offset:384\n"
  "v_mfma_f32_16x16x32_bf16 v[204:207], a[252:255], v[132:135], v[204:207]\n"
  "ds_read_b64_tr_b16 a[210:211], v15 offset:2176\n"
  "v_mfma_f32_16x16x32_bf16 v[208:211], a[192:195], v[128:131], v[208:211]\n"
  "ds_read_b64_tr_b16 a[212:213], v16 offset:416\n"
  "v_mfma_f32_16x16x32_bf16 v[212:215], a[192:195], v[132:135], v[212:215]\n"
  "s_waitcnt vmcnt(12)\n"
  "s_barrier\n"
  "ds_read_b64_tr_b16 a[214:215], v16 offset:2208\n"
  "v_mfma_f32_16x16x32_bf16 v[32:35], a[186:189], v[140:143], v[28:31]\n"
  "ds_read_b64_tr_b16 a[216:217], v15 offset:16384\n"
  "v_mfma_f32_16x16x32_bf16 v[36:39], a[184:187], v[140:143], v[28:31]\n"
  "ds_read_b64_tr_b16 a[218:219], v15 offset:18176\n"
  "s_waitcnt lgkmcnt(8)\n"
  "v_mfma_f32_16x16x32_bf16 v[216:219], a[196:199], v[128:131], v[216:219]\n"
  "ds_read_b64_tr_b16 a[220:221], v16 offset:16416\n"
  "v_mfma_f32_16x16x32_bf16 v[220:223], a[196:199], v[132:135], v[220:223]\n"
  "ds_read_b64_tr_b16 a[222:223], v16 offset:18208\n"
  "v_mfma_f32_16x16x32_bf16 v[224:227], a[200:203], v[128:131], v[224:227]\n"
  "ds_read_b64_tr_b16 a[224:225], v15 offset:16448\n"
  "v_cvt_pk_bf16_f32 v140, v32, v33\n"
  "v_mfma_f32_16x16x32_bf16 v[228:231], a[200:203], v[132:135], v[228:231]\n"
  "ds_read_b64_tr_b16 a[226:227], v15 offset:18240\n"
  "v_cvt_pk_bf16_f32 v141, v34, v35\n"
  "s_waitcnt lgkmcnt(8)\n"
  "v_mfma_f32_16x16x32_bf16 v[232:235], a[204:207], v[128:131], v[232:235]\n"
  "ds_read_b64_tr_b16 a[228:229], v16 offset:16480\n"
  "v_cvt_pk_bf16_f32 v142, v36, v37\n"
  "v_mfma_f32_16x16x32_bf16 v[236:239], a[204:207], v[132:135], v[236:239]\n"
  "ds_read_b64_tr_b16 a[230:231], v16 offset:18272\n"
  "v_cvt_pk_bf16_f32 v143, v38, v39\n"
  "v_mfma_f32_16x16x32_bf16 v[240:243], a[208:211], v[128:131], v[240:243]\n"
  "ds_read_b64_tr_b16 a[232:233], v15 offset:16512\n"
  "v_mfma_f32_16x16x32_bf16 v[244:247], a[208:211], v[132:135], v[244:247]\n"
  "ds_read_b64_tr_b16 a[234:235], v15 offset:18304\n"
  "s_waitcnt lgkmcnt(8)\n"
  "v_mfma_f32_16x16x32_bf16 v[248:251], a[212:215], v[128:131], v[248:251]\n"
  "ds_read_b64_tr_b16 a[236:237], v16 offset:16544\n"
  "s_add_u32 m0, s35, 16384\n"
  "s_mov_b32 s31, 28672\n"
  "v_mfma_f32_16x16x32_bf16 v[252:255], a[212:215], v[132:135], v[252:255]\n"
  "buffer_load_dwordx4 v5, s[48:51], s31 offen offset:0 lds\n"
  "v_mfma_f32_16x16x32_bf16 v[144:147], a[216:219], v[136:139], v[144:147]\n"
  "ds_read_b64_tr_b16 a[238:239], v16 offset:18336\n"
  "ds_read_b64_tr_b16 a[240:241], v15 offset:16576\n"
  "v_mfma_f32_16x16x32_bf16 v[148:151], a[216:219], v[140:143], v[148:151]\n"
  "ds_read_b64_tr_b16 a[242:243], v15 offset:18368\n"
  "s_waitcnt lgkmcnt(8)\n"
  "v_mfma_f32_16x16x32_bf16 v[152:155], a[220:223], v[136:139], v[152:155]\n"
  "ds_read_b64_tr_b16 a[244:245], v16 offset:16608\n"
  "v_mfma_f32_16x16x32_bf16 v[156:159], a[220:223], v[140:143], v[156:159]\n"
  "buffer_load_dwordx4 v6, s[48:51], s31 offen offset:1024 lds\n"
  "v_mfma_f32_16x16x32_bf16 v[160:163], a[224:227], v[136:139], v[160:163]\n"
  "ds_read_b64_tr_b16 a[246:247], v16 offset:18400\n"
  "ds_read_b64_tr_b16 a[248:249], v15 offset:16640\n"
  "v_mfma_f32_16x16x32_bf16 v[164:167], a[224:227], v[140:143], v[164:167]\n"
  "ds_read_b64_tr_b16 a[250:251], v15 offset:18432\n"
  "s_waitcnt lgkmcnt(8)\n"
  "v_mfma_f32_16x16x32_bf16 v[168:171], a[228:231], v[136:139], v[168:171]\n"
  "ds_read_b64_tr_b16 a[252:253], v16 offset:16672\n"
  "v_mfma_f32_16x16x32_bf16 v[172:175], a[228:231], v[140:143], v[172:175]\n"
  "buffer_load_dwordx4 v7, s[48:51], s31 offen offset:2048 lds\n"
  "v_mfma_f32_16x16x32_bf16 v[176:179], a[232:235], v[136:139], v[176:179]\n"
  "ds_read_b64_tr_b16 a[254:255], v16 offset:18464\n"
  "ds_read_b64_tr_b16 a[192:193], v15 offset:16704\n"
  "v_cvt_pk_bf16_f32 v144, v144, v145\n"
  "v_mfma_f32_16x16x32_bf16 v[180:183], a[232:235], v[140:143], v[180:183]\n"
  "ds_read_b64_tr_b16 a[194:195], v15 offset:18496\n"
  "v_cvt_pk_bf16_f32 v145, v146, v147\n"
  "v_cvt_pk_bf16_f32 v146, v152, v153\n"
  "Ltn2%=:\n"
  "s_cbranch_execz Ltnr0%=\n"
  "s_waitcnt lgkmcnt(8)\n"
  "v_mfma_f32_16x16x32_bf16 v[184:187], a[236:239], v[136:139], v[184:187]\n"
  "ds_read_b64_tr_b16 a[196:197], v16 offset:16736\n"
  "v_cvt_pk_bf16_f32 v147, v154, v155\n"
  "v_mfma_f32_16x16x32_bf16 v[188:191], a[236:239], v[140:143], v[188:191]\n"
  "s_mov_b32 exec_hi, 0\n"
  "buffer_load_dwordx4 v8, s[48:51], s31 offen offset:3072 lds\n"
  "s_mov_b32 exec_hi, -1\n"
  "v_mfma_f32_16x16x32_bf16 v[192:195], a[240:243], v[136:139], v[192:195]\n"
  "ds_read_b64_tr_b16 a[198:199], v16 offset:18528\n"
  "ds_read_b64_tr_b16 a[200:201], v15 offset:16768\n"
  "v_pk_max_i16 v144, v144, 0\n"
  "v_mfma_f32_16x16x32_bf16 v[196:199], a[240:243], v[140:143], v[196:199]\n"
  "ds_read_b64_tr_b16 a[202:203], v15 offset:18560\n"
  "v_pk_max_i16 v145, v145, 0\n"
  "v_pk_max_i16 v146, v146, 0\n"
  "s_waitcnt lgkmcnt(8)\n"
  "v_mfma_f32_16x16x32_bf16 v[200:203], a[244:247], v[136:139], v[200:203]\n"
  "ds_read_b64_tr_b16 a[204:205], v16 offset:16800\n"
  "v_pk_max_i16 v147, v147, 0\n"
  "v_mfma_f32_16x16x32_bf16 v[204:207], a[244:247], v[140:143], v[204:207]\n"
  "s_waitcnt vmcnt(12)\n"
  "s_barrier\n"
  "ds_read_b64_tr_b16 a[206:207], v16 offset:18592\n"
  "s_add_u32 m0, s35, 32768\n"
  "s_mov_b32 s31, 57344\n"
  "v_mfma_f32_16x16x32_bf16 v[208:211], a[248:251], v[136:139], v[208:211]\n"
  "buffer_load_dwordx4 v5, s[48:51], s31 offen offset:0 lds\n"
  "v_mfma_f32_16x16x32_bf16 v[212:215], a[248:251], v[140:143], v[212:215]\n"
  "ds_read_b64_tr_b16 a[208:209], v13 offset:0\n"
  "ds_read_b64_tr_b16 a[210:211], v13 offset:1792\n"
  "v_cvt_pk_bf16_f32 v148, v148, v149\n"
  "s_waitcnt lgkmcnt(8)\n"
  "v_mfma_f32_16x16x32_bf16 v[216:219], a[252:255], v[136:139], v[216:219]\n"
  "ds_read_b64_tr_b16 a[212:213], v14 offset:32\n"
  "v_cvt_pk_bf16_f32 v149, v150, v151\n"
  "v_mfma_f32_16x16x32_bf16 v[220:223], a[252:255], v[140:143], v[220:223]\n"
  "buffer_load_dwordx4 v6, s[48:51], s31 offen offset:1024 lds\n"
  "v_mfma_f32_16x16x32_bf16 v[224:227], a[192:195], v[136:139], v[224:227]\n"
  "ds_read_b64_tr_b16 a[214:215], v14 offset:1824\n"
  "ds_read_b64_tr_b16 a[216:217], v13 offset:64\n"
  "v_cvt_pk_bf16_f32 v150, v156, v157\n"
  "v_mfma_f32_16x16x32_bf16 v[228:231], a[192:195], v[140:143], v[228:231]\n"
  "ds_read_b64_tr_b16 a[218:219], v13 offset:1856\n"
  "v_cvt_pk_bf16_f32 v151, v158, v159\n"
  "v_pk_max_i16 v148, v148, 0\n"
  "s_waitcnt lgkmcnt(8)\n"
  "v_mfma_f32_16x16x32_bf16 v[232:235], a[196:199], v[136:139], v[232:235]\n"
  "buffer_load_dwordx4 v7, s[48:51], s31 offen offset:2048 lds\n"
  "v_mfma_f32_16x16x32_bf16 v[236:239], a[196:199], v[140:143], v[236:239]\n"
  "ds_read_b64_tr_b16 a[220:221], v14 offset:96\n"
  "ds_read_b64_tr_b16 a[222:223], v14 offset:1888\n"
  "v_mfma_f32_16x16x32_bf16 v[240:243], a[200:203], v[136:139], v[240:243]\n"
  "ds_read_b64_tr_b16 a[224:225], v13 offset:128\n"
  "v_pk_max_i16 v149, v149, 0\n"
  "v_pk_max_i16 v150, v150, 0\n"
  "v_mfma_f32_16x16x32_bf16 v[244:247], a[200:203], v[140:143], v[244:247]\n"
  "ds_read_b64_tr_b16 a[226:227], v13 offset:1920\n"
  "v_pk_max_i16 v151, v151, 0\n"
  "s_waitcnt lgkmcnt(8)\n"
  "v_mfma_f32_16x16x32_bf16 v[248:251], a[204:207], v[136:139], v[248:251]\n"
  "s_mov_b32 exec_hi, 0\n"
  "buffer_load_dwordx4 v8, s[48:51], s31 offen offset:3072 lds\n"
  "s_mov_b32 exec_hi, -1\n"
  "v_mfma_f32_16x16x32_bf16 v[252:255], a[204:207], v[140:143], v[252:255]\n"
  "ds_read_b64_tr_b16 a[228:229], v14 offset:160\n"
  "ds_read_b64_tr_b16 a[230:231], v14 offset:1952\n"
  "v_mfma_f32_16x16x32_bf16 v[32:35], a[208:211], v[144:147], 0\n"
  "ds_read_b64_tr_b16 a[232:233], v13 offset:192\n"
  "s_add_u32 m0, s35, 49152\n"
  "s_mov_b32 s31, 86016\n"
  "v_mfma_f32_16x16x32_bf16 v[36:39], a[208:211], v[148:151], 0\n"
  "buffer_load_dwordx4 v5, s[48:51], s31 offen offset:0 lds\n"
  "s_waitcnt lgkmcnt(7)\n"
  "v_mfma_f32_16x16x32_bf16 v[40:43], a[212:215], v[144:147], 0\n"
  "ds_read_b64_tr_b16 a[234:235], v13 offset:1984\n"
  "ds_read_b64_tr_b16 a[236:237], v14 offset:224\n"
  "v_cvt_pk_bf16_f32 v160, v160, v161\n"
  "v_mfma_f32_16x16x32_bf16 v[44:47], a[212:215], v[148:151], 0\n"
  "ds_read_b64_tr_b16 a[238:239], v14 offset:2016\n"
  "v_cvt_pk_bf16_f32 v161, v162, v163\n"
  "v_cvt_pk_bf16_f32 v162, v168, v169\n"
  "v_mfma_f32_16x16x32_bf16 v[48:51], a[216:219], v[144:147], 0\n"
  "buffer_load_dwordx4 v6, s[48:51], s31 offen offset:1024 lds\n"
  "v_mfma_f32_16x16x32_bf16 v[52:55], a[216:219], v[148:151], 0\n"
  "ds_read_b64_tr_b16 a[240:241], v13 offset:256\n"
  "ds_read_b64_tr_b16 a[242:243], v13 offset:2048\n"
  "v_cvt_pk_bf16_f32 v163, v170, v171\n"
  "s_waitcnt lgkmcnt(8)\n"
  "v_mfma_f32_16x16x32_bf16 v[56:59], a[220:223], v[144:147], 0\n"
  "ds_read_b64_tr_b16 a[244:245], v14 offset:288\n"
  "v_pk_max_i16 v160, v160, 0\n"
  "v_mfma_f32_16x16x32_bf16 v[60:63], a[220:223], v[148:151], 0\n"
  "ds_read_b64_tr_b16 a[246:247], v14 offset:2080\n"
  "v_pk_max_i16 v161, v161, 0\n"
  "v_pk_max_i16 v162, v162, 0\n"
  "v_mfma_f32_16x16x32_bf16 v[64:67], a[224:227], v[144:147], 0\n"
  "buffer_load_dwordx4 v7, s[48:51], s31 offen offset:2048 lds\n"
  "v_mfma_f32_16x16x32_bf16 v[68:71], a[224:227], v[148:151], 0\n"
  "ds_read_b64_tr_b16 a[248:249], v13 offset:320\n"
  "ds_read_b64_tr_b16 a[250:251], v13 offset:2112\n"
  "s_waitcnt lgkmcnt(9)\n"
  "v_mfma_f32_16x16x32_bf16 v[72:75], a[228:231], v[144:147], 0\n"
  "ds_read_b64_tr_b16 a[252:253], v14 offset:352\n"
  "v_pk_max_i16 v163, v163, 0\n"
  "v_cvt_pk_bf16_f32 v164, v164, v165\n"
  "v_mfma_f32_16x16x32_bf16 v[76:79], a[228:231], v[148:151], 0\n"
  "s_mov_b32 exec_hi, 0\n"
  "buffer_load_dwordx4 v8, s[48:51], s31 offen offset:3072 lds\n"
  "s_mov_b32 exec_hi, -1\n"
  "s_waitcnt lgkmcnt(7)\n"
  "v_mfma_f32_16x16x32_bf16 v[80:83], a[232:235], v[144:147], 0\n"
  "ds_read_b64_tr_b16 a[254:255], v14 offset:2144\n"
  "ds_read_b64_tr_b16 a[192:193], v13 offset:384\n"
  "v_cvt_pk_bf16_f32 v165, v166, v167\n"
  "v_mfma_f32_16x16x32_bf16 v[84:87], a[232:235], v[148:151], 0\n"
  "ds_read_b64_tr_b16 a[194:195], v13 offset:2176\n"
  "v_cvt_pk_bf16_f32 v166, v172, v173\n"
  "v_mfma_f32_16x16x32_bf16 v[88:91], a[236:239], v[144:147], 0\n"
  "ds_read_b64_tr_b16 a[196:197], v14 offset:416\n"
  "v_cvt_pk_bf16_f32 v167, v174, v175\n"
  "v_pk_max_i16 v164, v164, 0\n"
  "v_mfma_f32_16x16x32_bf16 v[92:95], a[236:239], v[148:151], 0\n"
  "s_waitcnt vmcnt(8)\n"
  "s_barrier\n"
  "ds_read_b64_tr_b16 a[198:199], v14 offset:2208\n"
  "s_add_u32 m0, s35, 65536\n"
  "s_mov_b32 s31, 114688\n"
  "s_waitcnt lgkmcnt(8)\n"
  "v_mfma_f32_16x16x32_bf16 v[96:99], a[240:243], v[144:147], 0\n"
  "buffer_load_dwordx4 v5, s[48:51], s31 offen offset:0 lds\n"
  "v_mfma_f32_16x16x32_bf16 v[100:103], a[240:243], v[148:151], 0\n"
  "ds_read_b64_tr_b16 a[200:201], v13 offset:16384\n"
  "ds_read_b64_tr_b16 a[202:203], v13 offset:18176\n"
  "ds_read_b128 a[64:67], v9 offset:8192\n"
  "v_mfma_f32_16x16x32_bf16 v[104:107], a[244:247], v[144:147], 0\n"
  "ds_read_b64_tr_b16 a[204:205], v14 offset:16416\n"
  "v_pk_max_i16 v165, v165, 0\n"
  "v_mfma_f32_16x16x32_bf16 v[108:111], a[244:247], v[148:151], 0\n"
  "buffer_load_dwordx4 v6, s[48:51], s31 offen offset:1024 lds\n"
  "s_waitcnt lgkmcnt(9)\n"
  "v_mfma_f32_16x16x32_bf16 v[112:115], a[248:251], v[144:147], 0\n"
  "ds_read_b64_tr_b16 a[206:207], v14 offset:18208\n"
  "ds_read_b128 a[68:71], v9 offset:10240\n"
  "ds_read_b64_tr_b16 a[208:209], v13 offset:16448\n"
  "v_mfma_f32_16x16x32_bf16 v[116:119], a[248:251], v[148:151], 0\n"
  "ds_read_b64_tr_b16 a[210:211], v13 offset:18240\n"
  "ds_read_b128 a[72:75], v10 offset:8192\n"
  "v_pk_max_i16 v166, v166, 0\n"
  "s_waitcnt lgkmcnt(11)\n"
  "v_mfma_f32_16x16x32_bf16 v[120:123], a[252:255], v[144:147], 0\n"
  "buffer_load_dwordx4 v7, s[48:51], s31 offen offset:2048 lds\n"
  "v_mfma_f32_16x16x32_bf16 v[124:127], a[252:255], v[148:151], 0\n"
  "ds_read_b64_tr_b16 a[212:213], v14 offset:16480\n"
  "ds_read_b64_tr_b16 a[214:215], v14 offset:18272\n"
  "ds_read_b128 a[76:79], v10 offset:10240\n"
  "v_mfma_f32_16x16x32_bf16 v[128:131], a[192:195], v[144:147], 0\n"
  "ds_read_b64_tr_b16 a[216:217], v13 offset:16512\n"
  "v_pk_max_i16 v167, v167, 0\n"
  "v_mfma_f32_16x16x32_bf16 v[132:135], a[192:195], v[148:151], 0\n"
  "ds_read_b64_tr_b16 a[218:219], v13 offset:18304\n"
  "ds_read_b128 a[80:83], v9 offset:12288\n"
  "s_waitcnt lgkmcnt(12)\n"
  "v_mfma_f32_16x16x32_bf16 v[136:139], a[196:199], v[144:147], 0\n"
  "s_mov_b32 exec_hi, 0\n"
  "buffer_load_dwordx4 v8, s[48:51], s31 offen offset:3072 lds\n"
  "s_mov_b32 exec_hi, -1\n"
  "v_mfma_f32_16x16x32_bf16 v[140:143], a[196:199], v[148:151], 0\n"
  "ds_read_b64_tr_b16 a[220:221], v14 offset:16544\n"
  "ds_read_b64_tr_b16 a[222:223], v14 offset:18336\n"
  "ds_read_b128 a[84:87], v9 offset:14336\n"
  "v_mfma_f32_16x16x32_bf16 v[32:35], a[200:203], v[160:163], v[32:35]\n"
  "ds_read_b64_tr_b16 a[224:225], v13 offset:16576\n"
  "s_add_u32 m0, s35, 81920\n"
  "s_mov_b32 s31, 143360\n"
  "v_mfma_f32_16x16x32_bf16 v[36:39], a[200:203], v[164:167], v[36:39]\n"
  "buffer_load_dwordx4 v5, s[48:51], s31 offen offset:0 lds\n"
  "s_waitcnt lgkmcnt(10)\n"
  "v_mfma_f32_16x16x32_bf16 v[40:43], a[204:207], v[160:163], v[40:43]\n"
  "ds_read_b64_tr_b16 a[226:227], v13 offset:18368\n"
  "ds_read_b128 a[88:91], v10 offset:12288\n"
  "ds_read_b64_tr_b16 a[228:229], v14 offset:16608\n"
  "v_mfma_f32_16x16x32_bf16 v[44:47], a[204:207], v[164:167], v[44:47]\n"
  "ds_read_b64_tr_b16 a[230:231], v14 offset:18400\n"
  "ds_read_b128 a[92:95], v10 offset:14336\n"
  "v_mfma_f32_16x16x32_bf16 v[48:51], a[208:211], v[160:163], v[48:51]\n"
  "buffer_load_dwordx4 v6, s[48:51], s31 offen offset:1024 lds\n"
  "v_mfma_f32_16x16x32_bf16 v[52:55], a[208:211], v[164:167], v[52:55]\n"
  "ds_read_b64_tr_b16 a[232:233], v13 offset:16640\n"
  "ds_read_b64_tr_b16 a[234:235], v13 offset:18432\n"
  "s_waitcnt lgkmcnt(11)\n"
  "v_mfma_f32_16x16x32_bf16 v[56:59], a[212:215], v[160:163], v[56:59]\n"
  "ds_read_b64_tr_b16 a[236:237], v14 offset:16672\n"
  "v_cvt_pk_bf16_f32 v176, v176, v177\n"
  "v_cvt_pk_bf16_f32 v177, v178, v179\n"
  "v_mfma_f32_16x16x32_bf16 v[60:63], a[212:215], v[164:167], v[60:63]\n"
  "ds_read_b64_tr_b16 a[238:239], v14 offset:18464\n"
  "v_cvt_pk_bf16_f32 v178, v184, v185\n"
  "v_mfma_f32_16x16x32_bf16 v[64:67], a[216:219], v[160:163], v[64:67]\n"
  "buffer_load_dwordx4 v7, s[48:51], s31 offen offset:2048 lds\n"
  "v_mfma_f32_16x16x32_bf16 v[68:71], a[216:219], v[164:167], v[68:71]\n"
  "ds_read_b64_tr_b16 a[240:241], v13 offset:16704\n"
  "ds_read_b64_tr_b16 a[242:243], v13 offset:18496\n"
  "s_waitcnt lgkmcnt(11)\n"
  "v_mfma_f32_16x16x32_bf16 v[72:75], a[220:223], v[160:163], v[72:75]\n"
  "ds_read_b64_tr_b16 a[244:245], v14 offset:16736\n"
  "v_cvt_pk_bf16_f32 v179, v186, v187\n"
  "v_pk_max_i16 v176, v176, 0\n"
  "v_mfma_f32_16x16x32_bf16 v[76:79], a[220:223], v[164:167], v[76:79]\n"
  "s_mov_b32 exec_hi, 0\n"
  "buffer_load_dwordx4 v8, s[48:51], s31 offen offset:3072 lds\n"
  "s_mov_b32 exec_hi, -1\n"
  "s_waitcnt lgkmcnt(7)\n"
  "v_mfma_f32_16x16x32_bf16 v[80:83], a[224:227], v[160:163], v[80:83]\n"
  "ds_read_b64_tr_b16 a[246:247], v14 offset:18528\n"
  "ds_read_b64_tr_b16 a[248:249], v13 offset:16768\n"
  "v_pk_max_i16 v177, v177, 0\n"
  "v_mfma_f32_16x16x32_bf16 v[84:87], a[224:227], v[164:167], v[84:87]\n"
  "ds_read_b64_tr_b16 a[250:251], v13 offset:18560\n"
  "v_pk_max_i16 v178, v178, 0\n"
  "v_mfma_f32_16x16x32_bf16 v[88:91], a[228:231], v[160:163], v[88:91]\n"
  "ds_read_b64_tr_b16 a[252:253], v14 offset:16800\n"
  "v_pk_max_i16 v179, v179, 0\n"
  "v_cvt_pk_bf16_f32 v180, v180, v181\n"
  "v_mfma_f32_16x16x32_bf16 v[92:95], a[228:231], v[164:167], v[92:95]\n"
  "s_waitcnt vmcnt(12)\n"
  "s_barrier\n"
  "ds_read_b64_tr_b16 a[254:255], v14 offset:18592\n"
  "s_waitcnt lgkmcnt(8)\n"
  "v_mfma_f32_16x16x32_bf16 v[96:99], a[232:235], v[160:163], v[96:99]\n"
  "ds_read_b64_tr_b16 a[192:193], v13 offset:32768\n"
  "v_lshrrev_b32 v22, 4, v1\n"
  "v_and_b32 v23, 15, v1\n"
  "v_lshlrev_b32 v23, 2, v23\n"
  "s_lshl_b32 s87, s21, 9\n"
  "v_lshl_add_u32 v22, v22, 7, v23\n"
  "s_mul_i32 s26, s22, 48\n"
  "s_add_u32 s87, s87, s26\n"
  "s_add_u32 s87, s87, 8192\n"
  "buffer_load_dword v21, v22, s[60:63], s87 offen sc1\n"
  "v_mfma_f32_16x16x32_bf16 v[100:103], a[232:235], v[164:167], v[100:103]\n"
  "ds_read_b64_tr_b16 a[194:195], v13 offset:34560\n"
  "v_cvt_pk_bf16_f32 v181, v182, v183\n"
  "v_mfma_f32_16x16x32_bf16 v[104:107], a[236:239], v[160:163], v[104:107]\n"
  "ds_read_b64_tr_b16 a[196:197], v14 offset:32800\n"
  "s_add_u32 m0, s35, 0\n"
  "s_mov_b32 s31, 172032\n"
  "v_cvt_pk_bf16_f32 v182, v188, v189\n"
  "v_cvt_pk_bf16_f32 v183, v190, v191\n"
  "v_mfma_f32_16x16x32_bf16 v[108:111], a[236:239], v[164:167], v[108:111]\n"
  "buffer_load_dwordx4 v5, s[48:51], s31 offen offset:0 lds\n"
  "s_waitcnt lgkmcnt(5)\n"
  "v_mfma_f32_16x16x32_bf16 v[112:115], a[240:243], v[160:163], v[112:115]\n"
  "ds_read_b64_tr_b16 a[198:199], v14 offset:34592\n"
  "ds_read_b64_tr_b16 a[200:201], v13 offset:32832\n"
  "v_pk_max_i16 v180, v180, 0\n"
  "v_mfma_f32_16x16x32_bf16 v[116:119], a[240:243], v[164:167], v[116:119]\n"
  "ds_read_b64_tr_b16 a[202:203], v13 offset:34624\n"
  "v_pk_max_i16 v181, v181, 0\n"
  "v_mfma_f32_16x16x32_bf16 v[120:123], a[244:247], v[160:163], v[120:123]\n"
  "buffer_load_dwordx4 v6, s[48:51], s31 offen offset:1024 lds\n"
  "v_mfma_f32_16x16x32_bf16 v[124:127], a[244:247], v[164:167], v[124:127]\n"
  "ds_read_b64_tr_b16 a[204:205], v14 offset:32864\n"
  "ds_read_b64_tr_b16 a[206:207], v14 offset:34656\n"
  "v_mfma_f32_16x16x32_bf16 v[128:131], a[248:251], v[160:163], v[128:131]\n"
  "ds_read_b64_tr_b16 a[208:209], v13 offset:32896\n"
  "v_pk_max_i16 v182, v182, 0\n"
  "v_pk_max_i16 v183, v183, 0\n"
  "v_mfma_f32_16x16x32_bf16 v[132:135], a[248:251], v[164:167], v[132:135]\n"
  "buffer_load_dwordx4 v7, s[48:51], s31 offen offset:2048 lds\n"
  "s_waitcnt lgkmcnt(7)\n"
  "v_mfma_f32_16x16x32_bf16 v[136:139], a[252:255], v[160:163], v[136:139]\n"
  "ds_read_b64_tr_b16 a[210:211], v13 offset:34688\n"
  "ds_read_b64_tr_b16 a[212:213], v14 offset:32928\n"
  "v_mfma_f32_16x16x32_bf16 v[140:143], a[252:255], v[164:167], v[140:143]\n"
  "ds_read_b64_tr_b16 a[214:215], v14 offset:34720\n"
  "v_mfma_f32_16x16x32_bf16 v[32:35], a[192:195], v[176:179], v[32:35]\n"
  "s_mov_b32 exec_hi, 0\n"
  "buffer_load_dwordx4 v8, s[48:51], s31 offen offset:3072 lds\n"
  "s_mov_b32 exec_hi, -1\n"
  "v_mfma_f32_16x16x32_bf16 v[36:39], a[192:195], v[180:183], v[36:39]\n"
  "ds_read_b64_tr_b16 a[216:217], v13 offset:32960\n"
  "ds_read_b64_tr_b16 a[218:219], v13 offset:34752\n"
  "s_waitcnt lgkmcnt(8)\n"
  "v_mfma_f32_16x16x32_bf16 v[40:43], a[196:199], v[176:179], v[40:43]\n"
  "ds_read_b64_tr_b16 a[220:221], v14 offset:32992\n"
  "s_add_u32 m0, s34, 0\n"
  "s_mov_b32 s30, 0\n"
  "v_cvt_pk_bf16_f32 v192, v192, v193\n"
  "v_cvt_pk_bf16_f32 v193, v194, v195\n"
  "v_mfma_f32_16x16x32_bf16 v[44:47], a[196:199], v[180:183], v[44:47]\n"
  "ds_read_b64_tr_b16 a[222:223], v14 offset:34784\n"
  "v_xor_b32 v3, 0x100, v3\n"
  "buffer_load_dwordx4 v3, s[52:55], s30 offen offset:0 nt lds\n"
  "v_mfma_f32_16x16x32_bf16 v[48:51], a[200:203], v[176:179], v[48:51]\n"
  "ds_read_b64_tr_b16 a[224:225], v13 offset:33024\n"
  "v_cvt_pk_bf16_f32 v194, v200, v201\n"
  "v_mfma_f32_16x16x32_bf16 v[52:55], a[200:203], v[180:183], v[52:55]\n"
  "ds_read_b64_tr_b16 a[226:227], v13 offset:34816\n"
  "s_mov_b32 s30, 64512\n"
  "v_cvt_pk_bf16_f32 v195, v202, v203\n"
  "s_waitcnt lgkmcnt(9)\n"
  "v_mfma_f32_16x16x32_bf16 v[56:59], a[204:207], v[176:179], v[56:59]\n"
  "buffer_load_dwordx4 v3, s[52:55], s30 offen offset:1024 nt lds\n"
  "v_mfma_f32_16x16x32_bf16 v[60:63], a[204:207], v[180:183], v[60:63]\n"
  "ds_read_b64_tr_b16 a[228:229], v14 offset:33056\n"
  "ds_read_b64_tr_b16 a[230:231], v14 offset:34848\n"
  "s_waitcnt lgkmcnt(8)\n"
  "v_mfma_f32_16x16x32_bf16 v[64:67], a[208:211], v[176:179], v[64:67]\n"
  "ds_read_b64_tr_b16 a[232:233], v13 offset:33088\n"
  "s_mov_b32 s30, 14336\n"
  "v_pk_max_i16 v192, v192, 0\n"
  "v_mfma_f32_16x16x32_bf16 v[68:71], a[208:211], v[180:183], v[68:71]\n"
  "buffer_load_dwordx4 v3, s[52:55], s30 offen offset:2048 nt lds\n"
  "v_mfma_f32_16x16x32_bf16 v[72:75], a[212:215], v[176:179], v[72:75]\n"
  "ds_read_b64_tr_b16 a[234:235], v13 offset:34880\n"
  "ds_read_b64_tr_b16 a[236:237], v14 offset:33120\n"
  "v_pk_max_i16 v193, v193, 0\n"
  "v_mfma_f32_16x16x32_bf16 v[76:79], a[212:215], v[180:183], v[76:79]\n"
  "ds_read_b64_tr_b16 a[238:239], v14 offset:34912\n"
  "s_mov_b32 s30, 78848\n"
  "v_pk_max_i16 v194, v194, 0\n"
  "s_waitcnt lgkmcnt(8)\n"
  "v_mfma_f32_16x16x32_bf16 v[80:83], a[216:219], v[176:179], v[80:83]\n"
  "buffer_load_dwordx4 v3, s[52:55], s30 offen offset:3072 nt lds\n"
  "v_mfma_f32_16x16x32_bf16 v[84:87], a[216:219], v[180:183], v[84:87]\n"
  "ds_read_b64_tr_b16 a[240:241], v13 offset:33152\n"
  "ds_read_b64_tr_b16 a[242:243], v13 offset:34944\n"
  "v_mfma_f32_16x16x32_bf16 v[88:91], a[220:223], v[176:179], v[88:91]\n"
  "ds_read_b64_tr_b16 a[244:245], v14 offset:33184\n"
  "v_pk_max_i16 v195, v195, 0\n"
  "v_mfma_f32_16x16x32_bf16 v[92:95], a[220:223], v[180:183], v[92:95]\n"
  "s_waitcnt vmcnt(17)\n"
  "s_barrier\n"
  "ds_read_b64_tr_b16 a[246:247], v14 offset:34976\n"
  "s_add_u32 m0, s34, 4096\n"
  "s_mov_b32 s30, 128\n"
  "s_waitcnt lgkmcnt(8)\n"
  "v_mfma_f32_16x16x32_bf16 v[96:99], a[224:227], v[176:179], v[96:99]\n"
  "ds_read_b64_tr_b16 a[248:249], v13 offset:49152\n"
  "buffer_load_dwordx4 v3, s[52:55], s30 offen offset:0 nt lds\n"
  "v_mfma_f32_16x16x32_bf16 v[100:103], a[224:227], v[180:183], v[100:103]\n"
  "ds_read_b64_tr_b16 a[250:251], v13 offset:50944\n"
  "v_cvt_pk_bf16_f32 v196, v196, v197\n"
  "v_mfma_f32_16x16x32_bf16 v[104:107], a[228:231], v[176:179], v[104:107]\n"
  "ds_read_b64_tr_b16 a[252:253], v14 offset:49184\n"
  "v_cvt_pk_bf16_f32 v197, v198, v199\n"
  "v_mfma_f32_16x16x32_bf16 v[108:111], a[228:231], v[180:183], v[108:111]\n"
  "ds_read_b64_tr_b16 a[254:255], v14 offset:50976\n"
  "v_cvt_pk_bf16_f32 v198, v204, v205\n"
  "s_waitcnt lgkmcnt(8)\n"
  "v_mfma_f32_16x16x32_bf16 v[112:115], a[232:235], v[176:179], v[112:115]\n"
  "ds_read_b64_tr_b16 a[192:193], v13 offset:49216\n"
  "v_cvt_pk_bf16_f32 v199, v206, v207\n"
  "v_mfma_f32_16x16x32_bf16 v[116:119], a[232:235], v[180:183], v[116:119]\n"
  "ds_read_b64_tr_b16 a[194:195], v13 offset:51008\n"
  "s_mov_b32 s30, 64640\n"
  "v_pk_max_i16 v196, v196, 0\n"
  "v_mfma_f32_16x16x32_bf16 v[120:123], a[236:239], v[176:179], v[120:123]\n"
  "buffer_load_dwordx4 v3, s[52:55], s30 offen offset:1024 nt lds\n"
  "v_mfma_f32_16x16x32_bf16 v[124:127], a[236:239], v[180:183], v[124:127]\n"
  "ds_read_b64_tr_b16 a[196:197], v14 offset:49248\n"
  "ds_read_b64_tr_b16 a[198:199], v14 offset:51040\n"
  "s_waitcnt lgkmcnt(8)\n"
  "v_mfma_f32_16x16x32_bf16 v[128:131], a[240:243], v[176:179], v[128:131]\n"
  "ds_read_b64_tr_b16 a[200:201], v13 offset:49280\n"
  "v_pk_max_i16 v197, v197, 0\n"
  "v_mfma_f32_16x16x32_bf16 v[132:135], a[240:243], v[180:183], v[132:135]\n"
  "ds_read_b64_tr_b16 a[202:203], v13 offset:51072\n"
  "v_pk_max_i16 v198, v198, 0\n"
  "v_mfma_f32_16x16x32_bf16 v[136:139], a[244:247], v[176:179], v[136:139]\n"
  "ds_read_b64_tr_b16 a[204:205], v14 offset:49312\n"
  "v_pk_max_i16 v199, v199, 0\n"
  "v_mfma_f32_16x16x32_bf16 v[140:143], a[244:247], v[180:183], v[140:143]\n"
  "ds_read_b64_tr_b16 a[206:207], v14 offset:51104\n"
  "s_waitcnt lgkmcnt(8)\n"
  "v_mfma_f32_16x16x32_bf16 v[32:35], a[248:251], v[192:195], v[32:35]\n"
  "ds_read_b64_tr_b16 a[208:209], v13 offset:49344\n"
  "s_mov_b32 s30, 14464\n"
  "v_mfma_f32_16x16x32_bf16 v[36:39], a[248:251], v[196:199], v[36:39]\n"
  "buffer_load_dwordx4 v3, s[52:55], s30 offen offset:2048 nt lds\n"
  "v_mfma_f32_16x16x32_bf16 v[40:43], a[252:255], v[192:195], v[40:43]\n"
  "ds_read_b64_tr_b16 a[210:211], v13 offset:51136\n"
  "ds_read_b64_tr_b16 a[212:213], v14 offset:49376\n"
  "v_cvt_pk_bf16_f32 v208, v208, v209\n"
  "v_mfma_f32_16x16x32_bf16 v[44:47], a[252:255], v[196:199], v[44:47]\n"
  "ds_read_b64_tr_b16 a[214:215], v14 offset:51168\n"
  "v_cvt_pk_bf16_f32 v209, v210, v211\n"
  "s_waitcnt lgkmcnt(8)\n"
  "v_mfma_f32_16x16x32_bf16 v[48:51], a[192:195], v[192:195], v[48:51]\n"
  "ds_read_b64_tr_b16 a[216:217], v13 offset:49408\n"
  "v_cvt_pk_bf16_f32 v210, v216, v217\n"
  "v_mfma_f32_16x16x32_bf16 v[52:55], a[192:195], v[196:199], v[52:55]\n"
  "ds_read_b64_tr_b16 a[218:219], v13 offset:51200\n"
  "v_cvt_pk_bf16_f32 v211, v218, v219\n"
  "v_mfma_f32_16x16x32_bf16 v[56:59], a[196:199], v[192:195], v[56:59]\n"
  "ds_read_b64_tr_b16 a[220:221], v14 offset:49440\n"
  "v_pk_max_i16 v208, v208, 0\n"
  "v_mfma_f32_16x16x32_bf16 v[60:63], a[196:199], v[196:199], v[60:63]\n"
  "ds_read_b64_tr_b16 a[222:223], v14 offset:51232\n"
  "s_mov_b32 s30, 78976\n"
  "v_pk_max_i16 v209, v209, 0\n"
  "s_waitcnt lgkmcnt(8)\n"
  "v_mfma_f32_16x16x32_bf16 v[64:67], a[200:203], v[192:195], v[64:67]\n"
  "buffer_load_dwordx4 v3, s[52:55], s30 offen offset:3072 nt lds\n"
  "v_mfma_f32_16x16x32_bf16 v[68:71], a[200:203], v[196:199], v[68:71]\n"
  "ds_read_b64_tr_b16 a[224:225], v13 offset:49472\n"
  "ds_read_b64_tr_b16 a[226:227], v13 offset:51264\n"
  "v_mfma_f32_16x16x32_bf16 v[72:75], a[204:207], v[192:195], v[72:75]\n"
  "ds_read_b64_tr_b16 a[228:229], v14 offset:49504\n"
  "v_pk_max_i16 v210, v210, 0\n"
  "v_mfma_f32_16x16x32_bf16 v[76:79], a[204:207], v[196:199], v[76:79]\n"
  "ds_read_b64_tr_b16 a[230:231], v14 offset:51296\n"
  "v_pk_max_i16 v211, v211, 0\n"
  "s_waitcnt lgkmcnt(8)\n"
  "v_mfma_f32_16x16x32_bf16 v[80:83], a[208:211], v[192:195], v[80:83]\n"
  "ds_read_b64_tr_b16 a[232:233], v13 offset:49536\n"
  "v_cvt_pk_bf16_f32 v212, v212, v213\n"
  "v_mfma_f32_16x16x32_bf16 v[84:87], a[208:211], v[196:199], v[84:87]\n"
  "ds_read_b64_tr_b16 a[234:235], v13 offset:51328\n"
  "v_cvt_pk_bf16_f32 v213, v214, v215\n"
  "v_mfma_f32_16x16x32_bf16 v[88:91], a[212:215], v[192:195], v[88:91]\n"
  "ds_read_b64_tr_b16 a[236:237], v14 offset:49568\n"
  "v_cvt_pk_bf16_f32 v214, v220, v221\n"
  "v_mfma_f32_16x16x32_bf16 v[92:95], a[212:215], v[196:199], v[92:95]\n"
  "s_waitcnt vmcnt(17)\n"
  "s_barrier\n"
  "ds_read_b64_tr_b16 a[238:239], v14 offset:51360\n"
  "s_waitcnt lgkmcnt(8)\n"
  "v_mfma_f32_16x16x32_bf16 v[96:99], a[216:219], v[192:195], v[96:99]\n"
  "ds_read_b64_tr_b16 a[240:241], v15 offset:0\n"
  "s_waitcnt vmcnt(12)\n"
  "v_cmp_ne_u32 vcc, v20, v21\n"
  "s_nop 0\n"
  "s_and_b64 s[26:27], vcc, exec\n"
  "s_cbranch_scc1 Lsabn%=\n"
  "Lbabn%=:\n"
  "v_mfma_f32_16x16x32_bf16 v[100:103], a[216:219], v[196:199], v[100:103]\n"
  "ds_read_b64_tr_b16 a[242:243], v15 offset:1792\n"
  "v_cvt_pk_bf16_f32 v215, v222, v223\n"
  "v_mfma_f32_16x16x32_bf16 v[104:107], a[220:223], v[192:195], v[104:107]\n"
  "ds_read_b64_tr_b16 a[244:245], v16 offset:32\n"
  "s_add_u32 m0, s68, 16384\n"
  "s_lshl_b32 s31, s80, 17\n"
  "v_pk_max_i16 v212, v212, 0\n"
  "v_mfma_f32_16x16x32_bf16 v[108:111], a[220:223], v[196:199], v[108:111]\n"
  "buffer_load_dwordx4 v17, s[56:59], s31 offen offset:0 sc1 lds\n"
  "s_waitcnt lgkmcnt(7)\n"
  "v_mfma_f32_16x16x32_bf16 v[112:115], a[224:227], v[192:195], v[112:115]\n"
  "ds_read_b64_tr_b16 a[246:247], v16 offset:1824\n"
  "ds_read_b64_tr_b16 a[248:249], v15 offset:64\n"
  "v_pk_max_i16 v213, v213, 0\n"
  "v_mfma_f32_16x16x32_bf16 v[116:119], a[224:227], v[196:199], v[116:119]\n"
  "ds_read_b64_tr_b16 a[250:251], v15 offset:1856\n"
  "v_mfma_f32_16x16x32_bf16 v[120:123], a[228:231], v[192:195], v[120:123]\n"
  "buffer_load_dwordx4 v17, s[56:59], s31 offen offset:1024 sc1 lds\n"
  "v_mfma_f32_16x16x32_bf16 v[124:127], a[228:231], v[196:199], v[124:127]\n"
  "ds_read_b64_tr_b16 a[252:253], v16 offset:96\n"
  "ds_read_b64_tr_b16 a[254:255], v16 offset:1888\n"
  "s_waitcnt lgkmcnt(8)\n"
  "v_mfma_f32_16x16x32_bf16 v[128:131], a[232:235], v[192:195], v[128:131]\n"
  "ds_read_b64_tr_b16 a[192:193], v15 offset:128\n"
  "v_pk_max_i16 v214, v214, 0\n"
  "v_pk_max_i16 v215, v215, 0\n"
  "v_mfma_f32_16x16x32_bf16 v[132:135], a[232:235], v[196:199], v[132:135]\n"
  "buffer_load_dwordx4 v17, s[56:59], s31 offen offset:2048 sc1 lds\n"
  "v_mfma_f32_16x16x32_bf16 v[136:139], a[236:239], v[192:195], v[136:139]\n"
  "ds_read_b64_tr_b16 a[194:195], v15 offset:1920\n"
  "ds_read_b64_tr_b16 a[196:197], v16 offset:160\n"
  "v_mfma_f32_16x16x32_bf16 v[140:143], a[236:239], v[196:199], v[140:143]\n"
  "ds_read_b64_tr_b16 a[198:199], v16 offset:1952\n"
  "s_waitcnt lgkmcnt(9)\n"
  "v_mfma_f32_16x16x32_bf16 v[32:35], a[240:243], v[208:211], v[32:35]\n"
  "buffer_load_dwordx4 v17, s[56:59], s31 offen offset:3072 sc1 lds\n"
  "v_mfma_f32_16x16x32_bf16 v[36:39], a[240:243], v[212:215], v[36:39]\n"
  "ds_read_b64_tr_b16 a[200:201], v15 offset:192\n"
  "ds_read_b64_tr_b16 a[202:203], v15 offset:1984\n"
  "s_waitcnt lgkmcnt(8)\n"
  "v_mfma_f32_16x16x32_bf16 v[40:43], a[244:247], v[208:211], v[40:43]\n"
  "ds_read_b64_tr_b16 a[204:205], v16 offset:224\n"
  "s_add_u32 m0, s68, 32768\n"
  "s_lshl_b32 s31, s80, 17\n"
  "s_add_u32 s31, s31, 0x10000\n"
  "v_cvt_pk_bf16_f32 v224, v224, v225\n"
  "v_cvt_pk_bf16_f32 v225, v226, v227\n"
  "v_mfma_f32_16x16x32_bf16 v[44:47], a[244:247], v[212:215], v[44:47]\n"
  "buffer_load_dwordx4 v17, s[56:59], s31 offen offset:0 sc1 lds\n"
  "v_mfma_f32_16x16x32_bf16 v[48:51], a[248:251], v[208:211], v[48:51]\n"
  "ds_read_b64_tr_b16 a[206:207], v16 offset:2016\n"
  "ds_read_b64_tr_b16 a[208:209], v15 offset:256\n"
  "v_cvt_pk_bf16_f32 v226, v232, v233\n"
  "v_mfma_f32_16x16x32_bf16 v[52:55], a[248:251], v[212:215], v[52:55]\n"
  "ds_read_b64_tr_b16 a[210:211], v15 offset:2048\n"
  "v_cvt_pk_bf16_f32 v227, v234, v235\n"
  "s_waitcnt lgkmcnt(9)\n"
  "v_mfma_f32_16x16x32_bf16 v[56:59], a[252:255], v[208:211], v[56:59]\n"
  "buffer_load_dwordx4 v17, s[56:59], s31 offen offset:1024 sc1 lds\n"
  "v_mfma_f32_16x16x32_bf16 v[60:63], a[252:255], v[212:215], v[60:63]\n"
  "ds_read_b64_tr_b16 a[212:213], v16 offset:288\n"
  "ds_read_b64_tr_b16 a[214:215], v16 offset:2080\n"
  "s_waitcnt lgkmcnt(8)\n"
  "v_mfma_f32_16x16x32_bf16 v[64:67], a[192:195], v[208:211], v[64:67]\n"
  "ds_read_b64_tr_b16 a[216:217], v15 offset:320\n"
  "v_pk_max_i16 v224, v224, 0\n"
  "v_pk_max_i16 v225, v225, 0\n"
  "v_mfma_f32_16x16x32_bf16 v[68:71], a[192:195], v[212:215], v[68:71]\n"
  "buffer_load_dwordx4 v17, s[56:59], s31 offen offset:2048 sc1 lds\n"
  "v_mfma_f32_16x16x32_bf16 v[72:75], a[196:199], v[208:211], v[72:75]\n"
  "ds_read_b64_tr_b16 a[218:219], v15 offset:2112\n"
  "ds_read_b64_tr_b16 a[220:221], v16 offset:352\n"
  "v_pk_max_i16 v226, v226, 0\n"
  "v_mfma_f32_16x16x32_bf16 v[76:79], a[196:199], v[212:215], v[76:79]\n"
  "ds_read_b64_tr_b16 a[222:223], v16 offset:2144\n"
  "v_pk_max_i16 v227, v227, 0\n"
  "s_waitcnt lgkmcnt(9)\n"
  "v_mfma_f32_16x16x32_bf16 v[80:83], a[200:203], v[208:211], v[80:83]\n"
  "buffer_load_dwordx4 v17, s[56:59], s31 offen offset:3072 sc1 lds\n"
  "v_mfma_f32_16x16x32_bf16 v[84:87], a[200:203], v[212:215], v[84:87]\n"
  "ds_read_b64_tr_b16 a[224:225], v15 offset:384\n"
  "ds_read_b64_tr_b16 a[226:227], v15 offset:2176\n"
  "s_waitcnt lgkmcnt(8)\n"
  "v_mfma_f32_16x16x32_bf16 v[88:91], a[204:207], v[208:211], v[88:91]\n"
  "ds_read_b64_tr_b16 a[228:229], v16 offset:416\n"
  "v_cvt_pk_bf16_f32 v228, v228, v229\n"
  "v_cvt_pk_bf16_f32 v229, v230, v231\n"
  "v_mfma_f32_16x16x32_bf16 v[92:95], a[204:207], v[212:215], v[92:95]\n"
  "s_barrier\n"
  "ds_read_b64_tr_b16 a[230:231], v16 offset:2208\n"
  "s_add_u32 m0, s68, 49152\n"
  "s_lshl_b32 s31, s81, 17\n"
  "v_mfma_f32_16x16x32_bf16 v[96:99], a[208:211], v[208:211], v[96:99]\n"
  "buffer_load_dwordx4 v17, s[56:59], s31 offen offset:0 sc1 lds\n"
  "v_mfma_f32_16x16x32_bf16 v[100:103], a[208:211], v[212:215], v[100:103]\n"
  "ds_read_b64_tr_b16 a[232:233], v15 offset:16384\n"
  "ds_read_b64_tr_b16 a[234:235], v15 offset:18176\n"
  "s_waitcnt vmcnt(13)\n"
  "ds_read_b128 a[96:99], v9 offset:0\n"
  "s_waitcnt lgkmcnt(10)\n"
  "v_mfma_f32_16x16x32_bf16 v[104:107], a[212:215], v[208:211], v[104:107]\n"
  "ds_read_b64_tr_b16 a[236:237], v16 offset:16416\n"
  "v_cvt_pk_bf16_f32 v230, v236, v237\n"
  "v_cvt_pk_bf16_f32 v231, v238, v239\n"
  "v_mfma_f32_16x16x32_bf16 v[108:111], a[212:215], v[212:215], v[108:111]\n"
  "ds_read_b64_tr_b16 a[238:239], v16 offset:18208\n"
  "ds_read_b128 a[100:103], v9 offset:2048\n"
  "s_waitcnt lgkmcnt(10)\n"
  "v_mfma_f32_16x16x32_bf16 v[112:115], a[216:219], v[208:211], v[112:115]\n"
  "buffer_load_dwordx4 v17, s[56:59], s31 offen offset:1024 sc1 lds\n"
  "v_mfma_f32_16x16x32_bf16 v[116:119], a[216:219], v[212:215], v[116:119]\n"
  "ds_read_b64_tr_b16 a[240:241], v15 offset:16448\n"
  "ds_read_b64_tr_b16 a[242:243], v15 offset:18240\n"
  "ds_read_b128 a[104:107], v10 offset:0\n"
  "v_mfma_f32_16x16x32_bf16 v[120:123], a[220:223], v[208:211], v[120:123]\n"
  "ds_read_b64_tr_b16 a[244:245], v16 offset:16480\n"
  "Ltn3%=:\n"
  "s_cbranch_execz Ltnr0%=\n"
  "v_pk_max_i16 v228, v228, 0\n"
  "v_pk_max_i16 v229, v229, 0\n"
  "v_mfma_f32_16x16x32_bf16 v[124:127], a[220:223], v[212:215], v[124:127]\n"
  "ds_read_b64_tr_b16 a[246:247], v16 offset:18272\n"
  "ds_read_b128 a[108:111], v10 offset:2048\n"
  "s_waitcnt lgkmcnt(12)\n"
  "v_mfma_f32_16x16x32_bf16 v[128:131], a[224:227], v[208:211], v[128:131]\n"
  "buffer_load_dwordx4 v17, s[56:59], s31 offen offset:2048 sc1 lds\n"
  "v_mfma_f32_16x16x32_bf16 v[132:135], a[224:227], v[212:215], v[132:135]\n"
  "ds_read_b64_tr_b16 a[248:249], v15 offset:16512\n"
  "ds_read_b64_tr_b16 a[250:251], v15 offset:18304\n"
  "v_mfma_f32_16x16x32_bf16 v[136:139], a[228:231], v[208:211], v[136:139]\n"
  "ds_read_b64_tr_b16 a[252:253], v16 offset:16544\n"
  "v_pk_max_i16 v230, v230, 0\n"
  "v_pk_max_i16 v231, v231, 0\n"
  "v_mfma_f32_16x16x32_bf16 v[140:143], a[228:231], v[212:215], v[140:143]\n"
  "ds_read_b64_tr_b16 a[254:255], v16 offset:18336\n"
  "s_waitcnt lgkmcnt(10)\n"
  "v_mfma_f32_16x16x32_bf16 v[32:35], a[232:235], v[224:227], v[32:35]\n"
  "buffer_load_dwordx4 v17, s[56:59], s31 offen offset:3072 sc1 lds\n"
  "v_mfma_f32_16x16x32_bf16 v[36:39], a[232:235], v[228:231], v[36:39]\n"
  "ds_read_b64_tr_b16 a[192:193], v15 offset:16576\n"
  "ds_read_b64_tr_b16 a[194:195], v15 offset:18368\n"
  "v_mfma_f32_16x16x32_bf16 v[40:43], a[236:239], v[224:227], v[40:43]\n"
  "ds_read_b64_tr_b16 a[196:197], v16 offset:16608\n"
  "v_cvt_pk_bf16_f32 v240, v240, v241\n"
  "v_mfma_f32_16x16x32_bf16 v[44:47], a[236:239], v[228:231], v[44:47]\n"
  "ds_read_b64_tr_b16 a[198:199], v16 offset:18400\n"
  "v_cvt_pk_bf16_f32 v241, v242, v243\n"
  "s_waitcnt lgkmcnt(8)\n"
  "v_mfma_f32_16x16x32_bf16 v[48:51], a[240:243], v[224:227], v[48:51]\n"
  "ds_read_b64_tr_b16 a[200:201], v15 offset:16640\n"
  "v_cvt_pk_bf16_f32 v242, v248, v249\n"
  "v_mfma_f32_16x16x32_bf16 v[52:55], a[240:243], v[228:231], v[52:55]\n"
  "ds_read_b64_tr_b16 a[202:203], v15 offset:18432\n"
  "v_cvt_pk_bf16_f32 v243, v250, v251\n"
  "v_mfma_f32_16x16x32_bf16 v[56:59], a[244:247], v[224:227], v[56:59]\n"
  "ds_read_b64_tr_b16 a[204:205], v16 offset:16672\n"
  "v_pk_max_i16 v240, v240, 0\n"
  "v_mfma_f32_16x16x32_bf16 v[60:63], a[244:247], v[228:231], v[60:63]\n"
  "ds_read_b64_tr_b16 a[206:207], v16 offset:18464\n"
  "v_pk_max_i16 v241, v241, 0\n"
  "s_waitcnt lgkmcnt(8)\n"
  "v_mfma_f32_16x16x32_bf16 v[64:67], a[248:251], v[224:227], v[64:67]\n"
  "ds_read_b64_tr_b16 a[208:209], v15 offset:16704\n"
  "v_pk_max_i16 v242, v242, 0\n"
  "v_mfma_f32_16x16x32_bf16 v[68:71], a[248:251], v[228:231], v[68:71]\n"
  "ds_read_b64_tr_b16 a[210:211], v15 offset:18496\n"
  "v_pk_max_i16 v243, v243, 0\n"
  "v_mfma_f32_16x16x32_bf16 v[72:75], a[252:255], v[224:227], v[72:75]\n"
  "ds_read_b64_tr_b16 a[212:213], v16 offset:16736\n"
  "v_cvt_pk_bf16_f32 v244, v244, v245\n"
  "v_mfma_f32_16x16x32_bf16 v[76:79], a[252:255], v[228:231], v[76:79]\n"
  "ds_read_b64_tr_b16 a[214:215], v16 offset:18528\n"
  "v_cvt_pk_bf16_f32 v245, v246, v247\n"
  "s_waitcnt lgkmcnt(8)\n"
  "v_mfma_f32_16x16x32_bf16 v[80:83], a[192:195], v[224:227], v[80:83]\n"
  "ds_read_b64_tr_b16 a[216:217], v15 offset:16768\n"
  "v_cvt_pk_bf16_f32 v246, v252, v253\n"
  "v_mfma_f32_16x16x32_bf16 v[84:87], a[192:195], v[228:231], v[84:87]\n"
  "ds_read_b64_tr_b16 a[218:219], v15 offset:18560\n"
  "v_cvt_pk_bf16_f32 v247, v254, v255\n"
  "v_mfma_f32_16x16x32_bf16 v[88:91], a[196:199], v[224:227], v[88:91]\n"
  "ds_read_b64_tr_b16 a[220:221], v16 offset:16800\n"
  "v_pk_max_i16 v244, v244, 0\n"
  "v_mfma_f32_16x16x32_bf16 v[92:95], a[196:199], v[228:231], v[92:95]\n"
  "s_barrier\n"
  "ds_read_b64_tr_b16 a[222:223], v16 offset:18592\n"
  "s_add_u32 m0, s68, 65536\n"
  "s_lshl_b32 s31, s81, 17\n"
  "s_add_u32 s31, s31, 0x10000\n"
  "s_waitcnt lgkmcnt(8)\n"
  "v_mfma_f32_16x16x32_bf16 v[96:99], a[200:203], v[224:227], v[96:99]\n"
  "buffer_load_dwordx4 v17, s[56:59], s31 offen offset:0 sc1 lds\n"
  "v_mfma_f32_16x16x32_bf16 v[100:103], a[200:203], v[228:231], v[100:103]\n"
  "ds_read_b64_tr_b16 a[224:225], v13 offset:0\n"
  "ds_read_b64_tr_b16 a[226:227], v13 offset:1792\n"
  "s_waitcnt vmcnt(13)\n"
  "ds_read_b128 a[112:115], v9 offset:4096\n"
  "v_mfma_f32_16x16x32_bf16 v[104:107], a[204:207], v[224:227], v[104:107]\n"
  "ds_read_b64_tr_b16 a[228:229], v14 offset:32\n"
  "v_pk_max_i16 v245, v245, 0\n"
  "v_mfma_f32_16x16x32_bf16 v[108:111], a[204:207], v[228:231], v[108:111]\n"
  "ds_read_b64_tr_b16 a[230:231], v14 offset:1824\n"
  "ds_read_b128 a[116:119], v9 offset:6144\n"
  "s_waitcnt lgkmcnt(10)\n"
  "v_mfma_f32_16x16x32_bf16 v[112:115], a[208:211], v[224:227], v[112:115]\n"
  "buffer_load_dwordx4 v17, s[56:59], s31 offen offset:1024 sc1 lds\n"
  "v_mfma_f32_16x16x32_bf16 v[116:119], a[208:211], v[228:231], v[116:119]\n"
  "ds_read_b64_tr_b16 a[232:233], v13 offset:64\n"
  "ds_read_b64_tr_b16 a[234:235], v13 offset:1856\n"
  "ds_read_b128 a[120:123], v10 offset:4096\n"
  "v_mfma_f32_16x16x32_bf16 v[120:123], a[212:215], v[224:227], v[120:123]\n"
  "ds_read_b64_tr_b16 a[236:237], v14 offset:96\n"
  "v_pk_max_i16 v246, v246, 0\n"
  "v_mfma_f32_16x16x32_bf16 v[124:127], a[212:215], v[228:231], v[124:127]\n"
  "ds_read_b64_tr_b16 a[238:239], v14 offset:1888\n"
  "ds_read_b128 a[124:127], v10 offset:6144\n"
  "s_waitcnt lgkmcnt(12)\n"
  "v_mfma_f32_16x16x32_bf16 v[128:131], a[216:219], v[224:227], v[128:131]\n"
  "buffer_load_dwordx4 v17, s[56:59], s31 offen offset:2048 sc1 lds\n"
  "v_mfma_f32_16x16x32_bf16 v[132:135], a[216:219], v[228:231], v[132:135]\n"
  "ds_read_b64_tr_b16 a[240:241], v13 offset:128\n"
  "ds_read_b64_tr_b16 a[242:243], v13 offset:1920\n"
  "v_mfma_f32_16x16x32_bf16 v[136:139], a[220:223], v[224:227], v[136:139]\n"
  "ds_read_b64_tr_b16 a[244:245], v14 offset:160\n"
  "v_pk_max_i16 v247, v247, 0\n"
  "v_mfma_f32_16x16x32_bf16 v[140:143], a[220:223], v[228:231], v[140:143]\n"
  "ds_read_b64_tr_b16 a[246:247], v14 offset:1952\n"
  "s_waitcnt lgkmcnt(10)\n"
  "v_mfma_f32_16x16x32_bf16 v[32:35], a[224:227], v[240:243], v[32:35]\n"
  "buffer_load_dwordx4 v17, s[56:59], s31 offen offset:3072 sc1 lds\n"
  "v_mfma_f32_16x16x32_bf16 v[36:39], a[224:227], v[244:247], v[36:39]\n"
  "ds_read_b64_tr_b16 a[248:249], v13 offset:192\n"
  "ds_read_b64_tr_b16 a[250:251], v13 offset:1984\n"
  "v_mfma_f32_16x16x32_bf16 v[40:43], a[228:231], v[240:243], v[40:43]\n"
  "ds_read_b64_tr_b16 a[252:253], v14 offset:224\n"
  "v_mfma_f32_16x16x32_bf16 v[44:47], a[228:231], v[244:247], v[44:47]\n"
  "ds_read_b64_tr_b16 a[254:255], v14 offset:2016\n"
  "s_waitcnt lgkmcnt(8)\n"
  "v_mfma_f32_16x16x32_bf16 v[48:51], a[232:235], v[240:243], v[48:51]\n"
  "ds_read_b64_tr_b16 a[192:193], v13 offset:256\n"
  "v_mfma_f32_16x16x32_bf16 v[52:55], a[232:235], v[244:247], v[52:55]\n"
  "ds_read_b64_tr_b16 a[194:195], v13 offset:2048\n"
  "v_mfma_f32_16x16x32_bf16 v[56:59], a[236:239], v[240:243], v[56:59]\n"
  "ds_read_b64_tr_b16 a[196:197], v14 offset:288\n"
  "v_mfma_f32_16x16x32_bf16 v[60:63], a[236:239], v[244:247], v[60:63]\n"
  "ds_read_b64_tr_b16 a[198:199], v14 offset:2080\n"
  "s_waitcnt lgkmcnt(8)\n"
  "v_mfma_f32_16x16x32_bf16 v[64:67], a[240:243], v[240:243], v[64:67]\n"
  "ds_read_b64_tr_b16 a[200:201], v13 offset:320\n"
  "v_mfma_f32_16x16x32_bf16 v[68:71], a[240:243], v[244:247], v[68:71]\n"
  "ds_read_b64_tr_b16 a[202:203], v13 offset:2112\n"
  "v_mfma_f32_16x16x32_bf16 v[72:75], a[244:247], v[240:243], v[72:75]\n"
  "ds_read_b64_tr_b16 a[204:205], v14 offset:352\n"
  "v_mfma_f32_16x16x32_bf16 v[76:79], a[244:247], v[244:247], v[76:79]\n"
  "ds_read_b64_tr_b16 a[206:207], v14 offset:2144\n"
  "s_waitcnt lgkmcnt(8)\n"
  "v_mfma_f32_16x16x32_bf16 v[80:83], a[248:251], v[240:243], v[80:83]\n"
  "ds_read_b64_tr_b16 a[208:209], v13 offset:384\n"
  "v_mul_f32 v32, v32, v11\n"
  "v_mfma_f32_16x16x32_bf16 v[84:87], a[248:251], v[244:247], v[84:87]\n"
  "ds_read_b64_tr_b16 a[210:211], v13 offset:2176\n"
  "v_mul_f32 v33, v33, v11\n"
  "v_mfma_f32_16x16x32_bf16 v[88:91], a[252:255], v[240:243], v[88:91]\n"
  "ds_read_b64_tr_b16 a[212:213], v14 offset:416\n"
  "v_mul_f32 v34, v34, v11\n"
  "v_mfma_f32_16x16x32_bf16 v[92:95], a[252:255], v[244:247], v[92:95]\n"
  "s_waitcnt vmcnt(12)\n"
  "s_barrier\n"
  "ds_read_b64_tr_b16 a[214:215], v14 offset:2208\n"
  "s_add_u32 m0, s68, 81920\n"
  "s_lshl_b32 s31, s82, 17\n"
  "s_waitcnt lgkmcnt(8)\n"
  "v_mfma_f32_16x16x32_bf16 v[96:99], a[192:195], v[240:243], v[96:99]\n"
  "buffer_load_dwordx4 v17, s[56:59], s31 offen offset:0 sc1 lds\n"
  "v_mfma_f32_16x16x32_bf16 v[100:103], a[192:195], v[244:247], v[100:103]\n"
  "ds_read_b128 a[216:219], v18 offset:16384\n"
  "v_mfma_f32_16x16x32_bf16 v[104:107], a[196:199], v[240:243], v[104:107]\n"
  "ds_read_b128 a[220:223], v18 offset:17408\n"
  "v_mul_f32 v35, v35, v11\n"
  "v_mfma_f32_16x16x32_bf16 v[108:111], a[196:199], v[244:247], v[108:111]\n"
  "v_mul_f32 v36, v36, v12\n"
  "v_mul_f32 v37, v37, v12\n"
  "s_waitcnt lgkmcnt(6)\n"
  "v_mfma_f32_16x16x32_bf16 v[112:115], a[200:203], v[240:243], v[112:115]\n"
  "buffer_load_dwordx4 v17, s[56:59], s31 offen offset:1024 sc1 lds\n"
  "v_mfma_f32_16x16x32_bf16 v[116:119], a[200:203], v[244:247], v[116:119]\n"
  "ds_read_b128 a[224:227], v18 offset:18432\n"
  "v_mfma_f32_16x16x32_bf16 v[120:123], a[204:207], v[240:243], v[120:123]\n"
  "ds_read_b128 a[228:231], v18 offset:19456\n"
  "v_mul_f32 v38, v38, v12\n"
  "v_mfma_f32_16x16x32_bf16 v[124:127], a[204:207], v[244:247], v[124:127]\n"
  "v_mul_f32 v39, v39, v12\n"
  "s_waitcnt lgkmcnt(4)\n"
  "v_mfma_f32_16x16x32_bf16 v[128:131], a[208:211], v[240:243], v[128:131]\n"
  "buffer_load_dwordx4 v17, s[56:59], s31 offen offset:2048 sc1 lds\n"
  "v_mfma_f32_16x16x32_bf16 v[132:135], a[208:211], v[244:247], v[132:135]\n"
  "ds_read_b128 a[232:235], v18 offset:20480\n"
  "v_mfma_f32_16x16x32_bf16 v[136:139], a[212:215], v[240:243], v[136:139]\n"
  "ds_read_b128 a[236:239], v18 offset:21504\n"
  "v_mfma_f32_16x16x32_bf16 v[140:143], a[212:215], v[244:247], v[140:143]\n"
  "s_waitcnt lgkmcnt(4)\n"
  "v_mfma_f32_16x16x32_bf16 v[32:35], a[216:219], a[0:3], v[32:35]\n"
  "buffer_load_dwordx4 v17, s[56:59], s31 offen offset:3072 sc1 lds\n"
  "v_mfma_f32_16x16x32_bf16 v[36:39], a[216:219], a[4:7], v[36:39]\n"
  "ds_read_b128 a[240:243], v18 offset:22528\n"
  "v_mfma_f32_16x16x32_bf16 v[32:35], a[220:223], a[8:11], v[32:35]\n"
  "ds_read_b128 a[244:247], v18 offset:23552\n"
  "v_mfma_f32_16x16x32_bf16 v[36:39], a[220:223], a[12:15], v[36:39]\n"
  "s_waitcnt lgkmcnt(4)\n"
  "v_mfma_f32_16x16x32_bf16 v[32:35], a[224:227], a[16:19], v[32:35]\n"
  "ds_read_b128 a[248:251], v18 offset:24576\n"
  "v_mfma_f32_16x16x32_bf16 v[36:39], a[224:227], a[20:23], v[36:39]\n"
  "v_mfma_f32_16x16x32_bf16 v[32:35], a[228:231], a[24:27], v[32:35]\n"
  "ds_read_b128 a[252:255], v18 offset:25600\n"
  "v_mfma_f32_16x16x32_bf16 v[36:39], a[228:231], a[28:31], v[36:39]\n"
  "s_waitcnt lgkmcnt(4)\n"
  "v_mfma_f32_16x16x32_bf16 v[32:35], a[232:235], a[32:35], v[32:35]\n"
  "ds_read_b128 a[192:195], v18 offset:26624\n"
  "v_mfma_f32_16x16x32_bf16 v[36:39], a[232:235], a[36:39], v[36:39]\n"
  "v_mfma_f32_16x16x32_bf16 v[32:35], a[236:239], a[40:43], v[32:35]\n"
  "ds_read_b128 a[196:199], v18 offset:27648\n"
  "v_mfma_f32_16x16x32_bf16 v[36:39], a[236:239], a[44:47], v[36:39]\n"
  "s_waitcnt lgkmcnt(4)\n"
  "v_mfma_f32_16x16x32_bf16 v[32:35], a[240:243], a[48:51], v[32:35]\n"
  "ds_read_b128 a[200:203], v18 offset:28672\n"
  "v_mul_f32 v40, v40, v11\n"
  "v_mfma_f32_16x16x32_bf16 v[36:39], a[240:243], a[52:55], v[36:39]\n"
  "v_mul_f32 v41, v41, v11\n"
  "v_mfma_f32_16x16x32_bf16 v[32:35], a[244:247], a[56:59], v[32:35]\n"
  "ds_read_b128 a[204:207], v18 offset:29696\n"
  "v_mul_f32 v42, v42, v11\n"
  "v_mfma_f32_16x16x32_bf16 v[36:39], a[244:247], a[60:63], v[36:39]\n"
  "v_mul_f32 v43, v43, v11\n"
  "s_waitcnt lgkmcnt(4)\n"
  "v_mfma_f32_16x16x32_bf16 v[32:35], a[248:251], a[64:67], v[32:35]\n"
  "ds_read_b128 a[208:211], v18 offset:30720\n"
  "v_mfma_f32_16x16x32_bf16 v[36:39], a[248:251], a[68:71], v[36:39]\n"
  "v_mul_f32 v44, v44, v12\n"
  "v_mfma_f32_16x16x32_bf16 v[32:35], a[252:255], a[72:75], v[32:35]\n"
  "ds_read_b128 a[212:215], v18 offset:31744\n"
  "v_mul_f32 v45, v45, v12\n"
  "v_mfma_f32_16x16x32_bf16 v[36:39], a[252:255], a[76:79], v[36:39]\n"
  "s_waitcnt vmcnt(12)\n"
  "s_barrier\n"
  "s_add_u32 m0, s68, 0\n"
  "s_lshl_b32 s31, s82, 17\n"
  "s_add_u32 s31, s31, 0x10000\n"
  "s_waitcnt lgkmcnt(4)\n"
  "v_mfma_f32_16x16x32_bf16 v[32:35], a[192:195], a[80:83], v[32:35]\n"
  "buffer_load_dwordx4 v17, s[56:59], s31 offen offset:0 sc1 lds\n"
  "v_mfma_f32_16x16x32_bf16 v[36:39], a[192:195], a[84:87], v[36:39]\n"
  "ds_read_b128 a[216:219], v18 offset:32768\n"
  "v_mfma_f32_16x16x32_bf16 v[32:35], a[196:199], a[88:91], v[32:35]\n"
  "ds_read_b128 a[220:223], v18 offset:33792\n"
  "v_mul_f32 v46, v46, v12\n"
  "v_mfma_f32_16x16x32_bf16 v[36:39], a[196:199], a[92:95], v[36:39]\n"
  "v_mul_f32 v47, v47, v12\n"
  "s_waitcnt lgkmcnt(4)\n"
  "v_mfma_f32_16x16x32_bf16 v[32:35], a[200:203], a[96:99], v[32:35]\n"
  "buffer_load_dwordx4 v17, s[56:59], s31 offen offset:1024 sc1 lds\n"
  "v_mfma_f32_16x16x32_bf16 v[36:39], a[200:203], a[100:103], v[36:39]\n"
  "ds_read_b128 a[224:227], v18 offset:34816\n"
  "v_mfma_f32_16x16x32_bf16 v[32:35], a[204:207], a[104:107], v[32:35]\n"
  "ds_read_b128 a[228:231], v18 offset:35840\n"
  "v_mfma_f32_16x16x32_bf16 v[36:39], a[204:207], a[108:111], v[36:39]\n"
  "s_mul_i32 s29, s80, 1835008\n"
  "s_waitcnt lgkmcnt(4)\n"
  "v_mfma_f32_16x16x32_bf16 v[32:35], a[208:211], a[112:115], v[32:35]\n"
  "buffer_load_dwordx4 v17, s[56:59], s31 offen offset:2048 sc1 lds\n"
  "v_mfma_f32_16x16x32_bf16 v[36:39], a[208:211], a[116:119], v[36:39]\n"
  "ds_read_b128 a[232:235], v18 offset:36864\n"
  "s_add_u32 s29, s29, s73\n"
  "v_mfma_f32_16x16x32_bf16 v[32:35], a[212:215], a[120:123], v[32:35]\n"
  "ds_read_b128 a[236:239], v18 offset:37888\n"
  "v_mfma_f32_16x16x32_bf16 v[36:39], a[212:215], a[124:127], v[36:39]\n"
  "s_add_u32 s29, s29, 49152\n"
  "s_waitcnt lgkmcnt(4)\n"
  "v_mfma_f32_16x16x32_bf16 v[40:43], a[216:219], a[0:3], v[40:43]\n"
  "buffer_load_dwordx4 v17, s[56:59], s31 offen offset:3072 sc1 lds\n"
  "v_mfma_f32_16x16x32_bf16 v[44:47], a[216:219], a[4:7], v[44:47]\n"
  "ds_read_b128 a[240:243], v18 offset:38912\n"
  "v_mfma_f32_16x16x32_bf16 v[40:43], a[220:223], a[8:11], v[40:43]\n"
  "ds_read_b128 a[244:247], v18 offset:39936\n"
  "v_cvt_pk_bf16_f32 v144, v32, v33\n"
  "v_cvt_pk_bf16_f32 v145, v34, v35\n"
  "v_mfma_f32_16x16x32_bf16 v[44:47], a[220:223], a[12:15], v[44:47]\n"
  "v_cvt_pk_bf16_f32 v146, v36, v37\n"
  "v_cvt_pk_bf16_f32 v147, v38, v39\n"
  "s_waitcnt lgkmcnt(4)\n"
  "v_mfma_f32_16x16x32_bf16 v[40:43], a[224:227], a[16:19], v[40:43]\n"
  "ds_read_b128 a[248:251], v18 offset:40960\n"
  "v_mfma_f32_16x16x32_bf16 v[44:47], a[224:227], a[20:23], v[44:47]\n"
  "buffer_store_dwordx4 v[144:147], v17, s[60:63], s29 offen offset:0 sc1\n"
  "v_mfma_f32_16x16x32_bf16 v[40:43], a[228:231], a[24:27], v[40:43]\n"
  "ds_read_b128 a[252:255], v18 offset:41984\n"
  "v_mfma_f32_16x16x32_bf16 v[44:47], a[228:231], a[28:31], v[44:47]\n"
  "s_waitcnt lgkmcnt(4)\n"
  "v_mfma_f32_16x16x32_bf16 v[40:43], a[232:235], a[32:35], v[40:43]\n"
  "ds_read_b128 a[192:195], v18 offset:43008\n"
  "v_mfma_f32_16x16x32_bf16 v[44:47], a[232:235], a[36:39], v[44:47]\n"
  "v_mfma_f32_16x16x32_bf16 v[40:43], a[236:239], a[40:43], v[40:43]\n"
  "ds_read_b128 a[196:199], v18 offset:44032\n"
  "v_mfma_f32_16x16x32_bf16 v[44:47], a[236:239], a[44:47], v[44:47]\n"
  "s_waitcnt lgkmcnt(4)\n"
  "v_mfma_f32_16x16x32_bf16 v[40:43], a[240:243], a[48:51], v[40:43]\n"
  "ds_read_b128 a[200:203], v18 offset:45056\n"
  "v_mul_f32 v48, v48, v11\n"
  "v_mfma_f32_16x16x32_bf16 v[44:47], a[240:243], a[52:55], v[44:47]\n"
  "v_mul_f32 v49, v49, v11\n"
  "v_mfma_f32_16x16x32_bf16 v[40:43], a[244:247], a[56:59], v[40:43]\n"
  "ds_read_b128 a[204:207], v18 offset:46080\n"
  "v_mul_f32 v50, v50, v11\n"
  "v_mfma_f32_16x16x32_bf16 v[44:47], a[244:247], a[60:63], v[44:47]\n"
  "s_waitcnt lgkmcnt(4)\n"
  "v_mfma_f32_16x16x32_bf16 v[40:43], a[248:251], a[64:67], v[40:43]\n"
  "ds_read_b128 a[208:211], v18 offset:47104\n"
  "v_mul_f32 v51, v51, v11\n"
  "v_mfma_f32_16x16x32_bf16 v[44:47], a[248:251], a[68:71], v[44:47]\n"
  "v_mul_f32 v52, v52, v12\n"
  "v_mfma_f32_16x16x32_bf16 v[40:43], a[252:255], a[72:75], v[40:43]\n"
  "ds_read_b128 a[212:215], v18 offset:48128\n"
  "v_mul_f32 v53, v53, v12\n"
  "v_mfma_f32_16x16x32_bf16 v[44:47], a[252:255], a[76:79], v[44:47]\n"
  "s_waitcnt vmcnt(13)\n"
  "s_barrier\n"
  "s_add_u32 m0, s68, 16384\n"
  "s_lshl_b32 s31, s83, 17\n"
  "s_waitcnt lgkmcnt(4)\n"
  "v_mfma_f32_16x16x32_bf16 v[40:43], a[192:195], a[80:83], v[40:43]\n"
  "buffer_load_dwordx4 v17, s[56:59], s31 offen offset:0 sc1 lds\n"
  "v_mfma_f32_16x16x32_bf16 v[44:47], a[192:195], a[84:87], v[44:47]\n"
  "ds_read_b128 a[216:219], v18 offset:49152\n"
  "v_mfma_f32_16x16x32_bf16 v[40:43], a[196:199], a[88:91], v[40:43]\n"
  "ds_read_b128 a[220:223], v18 offset:50176\n"
  "v_mfma_f32_16x16x32_bf16 v[44:47], a[196:199], a[92:95], v[44:47]\n"
  "v_mul_f32 v54, v54, v12\n"
  "s_waitcnt lgkmcnt(4)\n"
  "v_mfma_f32_16x16x32_bf16 v[40:43], a[200:203], a[96:99], v[40:43]\n"
  "buffer_load_dwordx4 v17, s[56:59], s31 offen offset:1024 sc1 lds\n"
  "v_mfma_f32_16x16x32_bf16 v[44:47], a[200:203], a[100:103], v[44:47]\n"
  "ds_read_b128 a[224:227], v18 offset:51200\n"
  "v_mfma_f32_16x16x32_bf16 v[40:43], a[204:207], a[104:107], v[40:43]\n"
  "ds_read_b128 a[228:231], v18 offset:52224\n"
  "v_mul_f32 v55, v55, v12\n"
  "v_mfma_f32_16x16x32_bf16 v[44:47], a[204:207], a[108:111], v[44:47]\n"
  "s_waitcnt lgkmcnt(4)\n"
  "v_mfma_f32_16x16x32_bf16 v[40:43], a[208:211], a[112:115], v[40:43]\n"
  "buffer_load_dwordx4 v17, s[56:59], s31 offen offset:2048 sc1 lds\n"
  "v_mfma_f32_16x16x32_bf16 v[44:47], a[208:211], a[116:119], v[44:47]\n"
  "ds_read_b128 a[232:235], v18 offset:53248\n"
  "v_mfma_f32_16x16x32_bf16 v[40:43], a[212:215], a[120:123], v[40:43]\n"
  "ds_read_b128 a[236:239], v18 offset:54272\n"
  "v_mfma_f32_16x16x32_bf16 v[44:47], a[212:215], a[124:127], v[44:47]\n"
  "s_waitcnt lgkmcnt(4)\n"
  "v_mfma_f32_16x16x32_bf16 v[48:51], a[216:219], a[0:3], v[48:51]\n"
  "buffer_load_dwordx4 v17, s[56:59], s31 offen offset:3072 sc1 lds\n"
  "v_mfma_f32_16x16x32_bf16 v[52:55], a[216:219], a[4:7], v[52:55]\n"
  "ds_read_b128 a[240:243], v18 offset:55296\n"
  "v_mfma_f32_16x16x32_bf16 v[48:51], a[220:223], a[8:11], v[48:51]\n"
  "ds_read_b128 a[244:247], v18 offset:56320\n"
  "v_cvt_pk_bf16_f32 v148, v40, v41\n"
  "v_cvt_pk_bf16_f32 v149, v42, v43\n"
  "v_mfma_f32_16x16x32_bf16 v[52:55], a[220:223], a[12:15], v[52:55]\n"
  "v_cvt_pk_bf16_f32 v150, v44, v45\n"
  "v_cvt_pk_bf16_f32 v151, v46, v47\n"
  "s_waitcnt lgkmcnt(4)\n"
  "v_mfma_f32_16x16x32_bf16 v[48:51], a[224:227], a[16:19], v[48:51]\n"
  "ds_read_b128 a[248:251], v18 offset:57344\n"
  "v_mfma_f32_16x16x32_bf16 v[52:55], a[224:227], a[20:23], v[52:55]\n"
  "buffer_store_dwordx4 v[148:151], v17, s[60:63], s29 offen offset:1024 sc1\n"
  "v_mfma_f32_16x16x32_bf16 v[48:51], a[228:231], a[24:27], v[48:51]\n"
  "ds_read_b128 a[252:255], v18 offset:58368\n"
  "v_mfma_f32_16x16x32_bf16 v[52:55], a[228:231], a[28:31], v[52:55]\n"
  "s_waitcnt lgkmcnt(4)\n"
  "v_mfma_f32_16x16x32_bf16 v[48:51], a[232:235], a[32:35], v[48:51]\n"
  "ds_read_b128 a[192:195], v18 offset:59392\n"
  "v_mfma_f32_16x16x32_bf16 v[52:55], a[232:235], a[36:39], v[52:55]\n"
  "v_mfma_f32_16x16x32_bf16 v[48:51], a[236:239], a[40:43], v[48:51]\n"
  "ds_read_b128 a[196:199], v18 offset:60416\n"
  "v_mfma_f32_16x16x32_bf16 v[52:55], a[236:239], a[44:47], v[52:55]\n"
  "s_waitcnt lgkmcnt(4)\n"
  "v_mfma_f32_16x16x32_bf16 v[48:51], a[240:243], a[48:51], v[48:51]\n"
  "ds_read_b128 a[200:203], v18 offset:61440\n"
  "v_mul_f32 v56, v56, v11\n"
  "v_mfma_f32_16x16x32_bf16 v[52:55], a[240:243], a[52:55], v[52:55]\n"
  "v_mul_f32 v57, v57, v11\n"
  "v_mfma_f32_16x16x32_bf16 v[48:51], a[244:247], a[56:59], v[48:51]\n"
  "ds_read_b128 a[204:207], v18 offset:62464\n"
  "v_mul_f32 v58, v58, v11\n"
  "v_mfma_f32_16x16x32_bf16 v[52:55], a[244:247], a[60:63], v[52:55]\n"
  "v_mul_f32 v59, v59, v11\n"
  "s_waitcnt lgkmcnt(4)\n"
  "v_mfma_f32_16x16x32_bf16 v[48:51], a[248:251], a[64:67], v[48:51]\n"
  "ds_read_b128 a[208:211], v18 offset:63488\n"
  "v_mfma_f32_16x16x32_bf16 v[52:55], a[248:251], a[68:71], v[52:55]\n"
  "v_mul_f32 v60, v60, v12\n"
  "v_mfma_f32_16x16x32_bf16 v[48:51], a[252:255], a[72:75], v[48:51]\n"
  "ds_read_b128 a[212:215], v18 offset:64512\n"
  "v_mul_f32 v61, v61, v12\n"
  "v_mfma_f32_16x16x32_bf16 v[52:55], a[252:255], a[76:79], v[52:55]\n"
  "s_waitcnt vmcnt(14)\n"
  "s_barrier\n"
  "s_add_u32 m0, s68, 32768\n"
  "s_lshl_b32 s31, s83, 17\n"
  "s_add_u32 s31, s31, 0x10000\n"
  "s_waitcnt lgkmcnt(4)\n"
  "v_mfma_f32_16x16x32_bf16 v[48:51], a[192:195], a[80:83], v[48:51]\n"
  "buffer_load_dwordx4 v17, s[56:59], s31 offen offset:0 sc1 lds\n"
  "v_mfma_f32_16x16x32_bf16 v[52:55], a[192:195], a[84:87], v[52:55]\n"
  "ds_read_b128 a[216:219], v19 offset:0\n"
  "v_mfma_f32_16x16x32_bf16 v[48:51], a[196:199], a[88:91], v[48:51]\n"
  "ds_read_b128 a[220:223], v19 offset:1024\n"
  "v_mul_f32 v62, v62, v12\n"
  "v_mfma_f32_16x16x32_bf16 v[52:55], a[196:199], a[92:95], v[52:55]\n"
  "v_mul_f32 v63, v63, v12\n"
  "s_waitcnt lgkmcnt(4)\n"
  "v_mfma_f32_16x16x32_bf16 v[48:51], a[200:203], a[96:99], v[48:51]\n"
  "buffer_load_dwordx4 v17, s[56:59], s31 offen offset:1024 sc1 lds\n"
  "v_mfma_f32_16x16x32_bf16 v[52:55], a[200:203], a[100:103], v[52:55]\n"
  "ds_read_b128 a[224:227], v19 offset:2048\n"
  "v_mfma_f32_16x16x32_bf16 v[48:51], a[204:207], a[104:107], v[48:51]\n"
  "ds_read_b128 a[228:231], v19 offset:3072\n"
  "v_mfma_f32_16x16x32_bf16 v[52:55], a[204:207], a[108:111], v[52:55]\n"
  "s_mul_i32 s29, s81, 1835008\n"
  "s_waitcnt lgkmcnt(4)\n"
  "v_mfma_f32_16x16x32_bf16 v[48:51], a[208:211], a[112:115], v[48:51]\n"
  "buffer_load_dwordx4 v17, s[56:59], s31 offen offset:2048 sc1 lds\n"
  "v_mfma_f32_16x16x32_bf16 v[52:55], a[208:211], a[116:119], v[52:55]\n"
  "ds_read_b128 a[232:235], v19 offset:4096\n"
  "s_add_u32 s29, s29, s73\n"
  "v_mfma_f32_16x16x32_bf16 v[48:51], a[212:215], a[120:123], v[48:51]\n"
  "ds_read_b128 a[236:239], v19 offset:5120\n"
  "v_mfma_f32_16x16x32_bf16 v[52:55], a[212:215], a[124:127], v[52:55]\n"
  "s_add_u32 s29, s29, 49152\n"
  "s_waitcnt lgkmcnt(4)\n"
  "v_mfma_f32_16x16x32_bf16 v[56:59], a[216:219], a[0:3], v[56:59]\n"
  "buffer_load_dwordx4 v17, s[56:59], s31 offen offset:3072 sc1 lds\n"
  "v_mfma_f32_16x16x32_bf16 v[60:63], a[216:219], a[4:7], v[60:63]\n"
  "ds_read_b128 a[240:243], v19 offset:6144\n"
  "v_mfma_f32_16x16x32_bf16 v[56:59], a[220:223], a[8:11], v[56:59]\n"
  "ds_read_b128 a[244:247], v19 offset:7168\n"
  "v_cvt_pk_bf16_f32 v144, v48, v49\n"
  "v_cvt_pk_bf16_f32 v145, v50, v51\n"
  "v_mfma_f32_16x16x32_bf16 v[60:63], a[220:223], a[12:15], v[60:63]\n"
  "v_cvt_pk_bf16_f32 v146, v52, v53\n"
  "v_cvt_pk_bf16_f32 v147, v54, v55\n"
  "s_waitcnt lgkmcnt(4)\n"
  "v_mfma_f32_16x16x32_bf16 v[56:59], a[224:227], a[16:19], v[56:59]\n"
  "ds_read_b128 a[248:251], v19 offset:8192\n"
  "v_mfma_f32_16x16x32_bf16 v[60:63], a[224:227], a[20:23], v[60:63]\n"
  "buffer_store_dwordx4 v[144:147], v17, s[60:63], s29 offen offset:0 sc1\n"
  "v_mfma_f32_16x16x32_bf16 v[56:59], a[228:231], a[24:27], v[56:59]\n"
  "ds_read_b128 a[252:255], v19 offset:9216\n"
  "v_mfma_f32_16x16x32_bf16 v[60:63], a[228:231], a[28:31], v[60:63]\n"
  "s_waitcnt lgkmcnt(4)\n"
  "v_mfma_f32_16x16x32_bf16 v[56:59], a[232:235], a[32:35], v[56:59]\n"
  "ds_read_b128 a[192:195], v19 offset:10240\n"
  "v_mfma_f32_16x16x32_bf16 v[60:63], a[232:235], a[36:39], v[60:63]\n"
  "v_mfma_f32_16x16x32_bf16 v[56:59], a[236:239], a[40:43], v[56:59]\n"
  "ds_read_b128 a[196:199], v19 offset:11264\n"
  "v_mfma_f32_16x16x32_bf16 v[60:63], a[236:239], a[44:47], v[60:63]\n"
  "s_waitcnt lgkmcnt(4)\n"
  "v_mfma_f32_16x16x32_bf16 v[56:59], a[240:243], a[48:51], v[56:59]\n"
  "ds_read_b128 a[200:203], v19 offset:12288\n"
  "v_mul_f32 v64, v64, v11\n"
  "v_mfma_f32_16x16x32_bf16 v[60:63], a[240:243], a[52:55], v[60:63]\n"
  "v_mul_f32 v65, v65, v11\n"
  "v_mfma_f32_16x16x32_bf16 v[56:59], a[244:247], a[56:59], v[56:59]\n"
  "ds_read_b128 a[204:207], v19 offset:13312\n"
  "v_mul_f32 v66, v66, v11\n"
  "v_mfma_f32_16x16x32_bf16 v[60:63], a[244:247], a[60:63], v[60:63]\n"
  "v_mul_f32 v67, v67, v11\n"
  "s_waitcnt lgkmcnt(4)\n"
  "v_mfma_f32_16x16x32_bf16 v[56:59], a[248:251], a[64:67], v[56:59]\n"
  "ds_read_b128 a[208:211], v19 offset:14336\n"
  "v_mfma_f32_16x16x32_bf16 v[60:63], a[248:251], a[68:71], v[60:63]\n"
  "v_mul_f32 v68, v68, v12\n"
  "v_mfma_f32_16x16x32_bf16 v[56:59], a[252:255], a[72:75], v[56:59]\n"
  "ds_read_b128 a[212:215], v19 offset:15360\n"
  "v_mul_f32 v69, v69, v12\n"
  "v_mfma_f32_16x16x32_bf16 v[60:63], a[252:255], a[76:79], v[60:63]\n"
  "s_waitcnt vmcnt(15)\n"
  "s_barrier\n"
  "s_add_u32 m0, s68, 49152\n"
  "s_lshl_b32 s31, s84, 17\n"
  "s_waitcnt lgkmcnt(4)\n"
  "v_mfma_f32_16x16x32_bf16 v[56:59], a[192:195], a[80:83], v[56:59]\n"
  "buffer_load_dwordx4 v17, s[56:59], s31 offen offset:0 sc1 lds\n"
  "v_mfma_f32_16x16x32_bf16 v[60:63], a[192:195], a[84:87], v[60:63]\n"
  "ds_read_b128 a[216:219], v19 offset:16384\n"
  "v_mfma_f32_16x16x32_bf16 v[56:59], a[196:199], a[88:91], v[56:59]\n"
  "ds_read_b128 a[220:223], v19 offset:17408\n"
  "v_mul_f32 v70, v70, v12\n"
  "v_mfma_f32_16x16x32_bf16 v[60:63], a[196:199], a[92:95], v[60:63]\n"
  "v_mul_f32 v71, v71, v12\n"
  "s_waitcnt lgkmcnt(4)\n"
  "v_mfma_f32_16x16x32_bf16 v[56:59], a[200:203], a[96:99], v[56:59]\n"
  "buffer_load_dwordx4 v17, s[56:59], s31 offen offset:1024 sc1 lds\n"
  "s_lshl_b32 s32, s80, 14\n"
  "v_mfma_f32_16x16x32_bf16 v[60:63], a[200:203], a[100:103], v[60:63]\n"
  "ds_read_b128 a[224:227], v19 offset:18432\n"
  "s_add_u32 s32, s32, s33\n"
  "v_mfma_f32_16x16x32_bf16 v[56:59], a[204:207], a[104:107], v[56:59]\n"
  "ds_read_b128 a[228:231], v19 offset:19456\n"
  "s_waitcnt vmcnt(7)\n"
  "s_mov_b64 exec, 1\n"
  "buffer_store_dword v20, off, s[60:63], s32 offset:24 sc1\n"
  "s_mov_b64 exec, -1\n"
  "v_mfma_f32_16x16x32_bf16 v[60:63], a[204:207], a[108:111], v[60:63]\n"
  "s_waitcnt lgkmcnt(4)\n"
  "v_mfma_f32_16x16x32_bf16 v[56:59], a[208:211], a[112:115], v[56:59]\n"
  "buffer_load_dwordx4 v17, s[56:59], s31 offen offset:2048 sc1 lds\n"
  "v_mfma_f32_16x16x32_bf16 v[60:63], a[208:211], a[116:119], v[60:63]\n"
  "ds_read_b128 a[232:235], v19 offset:20480\n"
  "v_mfma_f32_16x16x32_bf16 v[56:59], a[212:215], a[120:123], v[56:59]\n"
  "ds_read_b128 a[236:239], v19 offset:21504\n"
  "v_mfma_f32_16x16x32_bf16 v[60:63], a[212:215], a[124:127], v[60:63]\n"
  "s_waitcnt lgkmcnt(4)\n"
  "v_mfma_f32_16x16x32_bf16 v[64:67], a[216:219], a[0:3], v[64:67]\n"
  "buffer_load_dwordx4 v17, s[56:59], s31 offen offset:3072 sc1 lds\n"
  "v_mfma_f32_16x16x32_bf16 v[68:71], a[216:219], a[4:7], v[68:71]\n"
  "ds_read_b128 a[240:243], v19 offset:22528\n"
  "v_mfma_f32_16x16x32_bf16 v[64:67], a[220:223], a[8:11], v[64:67]\n"
  "ds_read_b128 a[244:247], v19 offset:23552\n"
  "v_cvt_pk_bf16_f32 v148, v56, v57\n"
  "v_cvt_pk_bf16_f32 v149, v58, v59\n"
  "v_mfma_f32_16x16x32_bf16 v[68:71], a[220:223], a[12:15], v[68:71]\n"
  "v_cvt_pk_bf16_f32 v150, v60, v61\n"
  "v_cvt_pk_bf16_f32 v151, v62, v63\n"
  "s_waitcnt lgkmcnt(4)\n"
  "v_mfma_f32_16x16x32_bf16 v[64:67], a[224:227], a[16:19], v[64:67]\n"
  "ds_read_b128 a[248:251], v19 offset:24576\n"
  "v_mfma_f32_16x16x32_bf16 v[68:71], a[224:227], a[20:23], v[68:71]\n"
  "buffer_store_dwordx4 v[148:151], v17, s[60:63], s29 offen offset:1024 sc1\n"
  "v_mfma_f32_16x16x32_bf16 v[64:67], a[228:231], a[24:27], v[64:67]\n"
  "ds_read_b128 a[252:255], v19 offset:25600\n"
  "v_mfma_f32_16x16x32_bf16 v[68:71], a[228:231], a[28:31], v[68:71]\n"
  "s_waitcnt lgkmcnt(4)\n"
  "v_mfma_f32_16x16x32_bf16 v[64:67], a[232:235], a[32:35], v[64:67]\n"
  "ds_read_b128 a[192:195], v19 offset:26624\n"
  "v_mfma_f32_16x16x32_bf16 v[68:71], a[232:235], a[36:39], v[68:71]\n"
  "v_mfma_f32_16x16x32_bf16 v[64:67], a[236:239], a[40:43], v[64:67]\n"
  "ds_read_b128 a[196:199], v19 offset:27648\n"
  "v_mfma_f32_16x16x32_bf16 v[68:71], a[236:239], a[44:47], v[68:71]\n"
  "s_waitcnt lgkmcnt(4)\n"
  "v_mfma_f32_16x16x32_bf16 v[64:67], a[240:243], a[48:51], v[64:67]\n"
  "ds_read_b128 a[200:203], v19 offset:28672\n"
  "v_mul_f32 v72, v72, v11\n"
  "v_mfma_f32_16x16x32_bf16 v[68:71], a[240:243], a[52:55], v[68:71]\n"
  "v_mul_f32 v73, v73, v11\n"
  "v_mfma_f32_16x16x32_bf16 v[64:67], a[244:247], a[56:59], v[64:67]\n"
  "ds_read_b128 a[204:207], v19 offset:29696\n"
  "v_mul_f32 v74, v74, v11\n"
  "v_mfma_f32_16x16x32_bf16 v[68:71], a[244:247], a[60:63], v[68:71]\n"
  "v_mul_f32 v75, v75, v11\n"
  "s_waitcnt lgkmcnt(4)\n"
  "v_mfma_f32_16x16x32_bf16 v[64:67], a[248:251], a[64:67], v[64:67]\n"
  "ds_read_b128 a[208:211], v19 offset:30720\n"
  "v_mfma_f32_16x16x32_bf16 v[68:71], a[248:251], a[68:71], v[68:71]\n"
  "v_mul_f32 v76, v76, v12\n"
  "v_mfma_f32_16x16x32_bf16 v[64:67], a[252:255], a[72:75], v[64:67]\n"
  "ds_read_b128 a[212:215], v19 offset:31744\n"
  "v_mul_f32 v77, v77, v12\n"
  "v_mfma_f32_16x16x32_bf16 v[68:71], a[252:255], a[76:79], v[68:71]\n"
  "s_barrier\n"
  "s_add_u32 m0, s68, 65536\n"
  "s_lshl_b32 s31, s84, 17\n"
  "s_add_u32 s31, s31, 0x10000\n"
  "s_waitcnt lgkmcnt(4)\n"
  "v_mfma_f32_16x16x32_bf16 v[64:67], a[192:195], a[80:83], v[64:67]\n"
  "buffer_load_dwordx4 v17, s[56:59], s31 offen offset:0 sc1 lds\n"
  "v_mfma_f32_16x16x32_bf16 v[68:71], a[192:195], a[84:87], v[68:71]\n"
  "ds_read_b128 a[216:219], v18 offset:0\n"
  "v_mfma_f32_16x16x32_bf16 v[64:67], a[196:199], a[88:91], v[64:67]\n"
  "ds_read_b128 a[220:223], v18 offset:1024\n"
  "v_mul_f32 v78, v78, v12\n"
  "v_mfma_f32_16x16x32_bf16 v[68:71], a[196:199], a[92:95], v[68:71]\n"
  "v_mul_f32 v79, v79, v12\n"
  "s_waitcnt lgkmcnt(4)\n"
  "v_mfma_f32_16x16x32_bf16 v[64:67], a[200:203], a[96:99], v[64:67]\n"
  "buffer_load_dwordx4 v17, s[56:59], s31 offen offset:1024 sc1 lds\n"
  "v_mfma_f32_16x16x32_bf16 v[68:71], a[200:203], a[100:103], v[68:71]\n"
  "ds_read_b128 a[224:227], v18 offset:2048\n"
  "v_mfma_f32_16x16x32_bf16 v[64:67], a[204:207], a[104:107], v[64:67]\n"
  "ds_read_b128 a[228:231], v18 offset:3072\n"
  "v_mfma_f32_16x16x32_bf16 v[68:71], a[204:207], a[108:111], v[68:71]\n"
  "s_mul_i32 s29, s82, 1835008\n"
  "s_waitcnt lgkmcnt(4)\n"
  "v_mfma_f32_16x16x32_bf16 v[64:67], a[208:211], a[112:115], v[64:67]\n"
  "buffer_load_dwordx4 v17, s[56:59], s31 offen offset:2048 sc1 lds\n"
  "v_mfma_f32_16x16x32_bf16 v[68:71], a[208:211], a[116:119], v[68:71]\n"
  "ds_read_b128 a[232:235], v18 offset:4096\n"
  "s_add_u32 s29, s29, s73\n"
  "v_mfma_f32_16x16x32_bf16 v[64:67], a[212:215], a[120:123], v[64:67]\n"
  "ds_read_b128 a[236:239], v18 offset:5120\n"
  "v_mfma_f32_16x16x32_bf16 v[68:71], a[212:215], a[124:127], v[68:71]\n"
  "s_add_u32 s29, s29, 49152\n"
  "s_waitcnt lgkmcnt(4)\n"
  "v_mfma_f32_16x16x32_bf16 v[72:75], a[216:219], a[0:3], v[72:75]\n"
  "buffer_load_dwordx4 v17, s[56:59], s31 offen offset:3072 sc1 lds\n"
  "v_mfma_f32_16x16x32_bf16 v[76:79], a[216:219], a[4:7], v[76:79]\n"
  "ds_read_b128 a[240:243], v18 offset:6144\n"
  "v_mfma_f32_16x16x32_bf16 v[72:75], a[220:223], a[8:11], v[72:75]\n"
  "ds_read_b128 a[244:247], v18 offset:7168\n"
  "v_cvt_pk_bf16_f32 v144, v64, v65\n"
  "v_cvt_pk_bf16_f32 v145, v66, v67\n"
  "v_mfma_f32_16x16x32_bf16 v[76:79], a[220:223], a[12:15], v[76:79]\n"
  "v_cvt_pk_bf16_f32 v146, v68, v69\n"
  "v_cvt_pk_bf16_f32 v147, v70, v71\n"
  "s_waitcnt lgkmcnt(4)\n"
  "v_mfma_f32_16x16x32_bf16 v[72:75], a[224:227], a[16:19], v[72:75]\n"
  "ds_read_b128 a[248:251], v18 offset:8192\n"
  "v_mfma_f32_16x16x32_bf16 v[76:79], a[224:227], a[20:23], v[76:79]\n"
  "buffer_store_dwordx4 v[144:147], v17, s[60:63], s29 offen offset:0 sc1\n"
  "v_mfma_f32_16x16x32_bf16 v[72:75], a[228:231], a[24:27], v[72:75]\n"
  "ds_read_b128 a[252:255], v18 offset:9216\n"
  "v_mfma_f32_16x16x32_bf16 v[76:79], a[228:231], a[28:31], v[76:79]\n"
  "s_waitcnt lgkmcnt(4)\n"
  "v_mfma_f32_16x16x32_bf16 v[72:75], a[232:235], a[32:35], v[72:75]\n"
  "ds_read_b128 a[192:195], v18 offset:10240\n"
  "v_mfma_f32_16x16x32_bf16 v[76:79], a[232:235], a[36:39], v[76:79]\n"
  "v_mfma_f32_16x16x32_bf16 v[72:75], a[236:239], a[40:43], v[72:75]\n"
  "ds_read_b128 a[196:199], v18 offset:11264\n"
  "v_mfma_f32_16x16x32_bf16 v[76:79], a[236:239], a[44:47], v[76:79]\n"
  "s_waitcnt lgkmcnt(4)\n"
  "v_mfma_f32_16x16x32_bf16 v[72:75], a[240:243], a[48:51], v[72:75]\n"
  "ds_read_b128 a[200:203], v18 offset:12288\n"
  "v_mul_f32 v80, v80, v11\n"
  "v_mfma_f32_16x16x32_bf16 v[76:79], a[240:243], a[52:55], v[76:79]\n"
  "v_mul_f32 v81, v81, v11\n"
  "v_mfma_f32_16x16x32_bf16 v[72:75], a[244:247], a[56:59], v[72:75]\n"
  "ds_read_b128 a[204:207], v18 offset:13312\n"
  "v_mul_f32 v82, v82, v11\n"
  "v_mfma_f32_16x16x32_bf16 v[76:79], a[244:247], a[60:63], v[76:79]\n"
  "v_mul_f32 v83, v83, v11\n"
  "s_waitcnt lgkmcnt(4)\n"
  "v_mfma_f32_16x16x32_bf16 v[72:75], a[248:251], a[64:67], v[72:75]\n"
  "ds_read_b128 a[208:211], v18 offset:14336\n"
  "v_mfma_f32_16x16x32_bf16 v[76:79], a[248:251], a[68:71], v[76:79]\n"
  "v_mul_f32 v84, v84, v12\n"
  "v_mfma_f32_16x16x32_bf16 v[72:75], a[252:255], a[72:75], v[72:75]\n"
  "ds_read_b128 a[212:215], v18 offset:15360\n"
  "v_mul_f32 v85, v85, v12\n"
  "v_mfma_f32_16x16x32_bf16 v[76:79], a[252:255], a[76:79], v[76:79]\n"
  "s_barrier\n"
  "s_add_u32 m0, s68, 81920\n"
  "s_lshl_b32 s31, s85, 17\n"
  "s_waitcnt lgkmcnt(4)\n"
  "v_mfma_f32_16x16x32_bf16 v[72:75], a[192:195], a[80:83], v[72:75]\n"
  "buffer_load_dwordx4 v17, s[56:59], s31 offen offset:0 sc1 lds\n"
  "v_mfma_f32_16x16x32_bf16 v[76:79], a[192:195], a[84:87], v[76:79]\n"
  "ds_read_b128 a[216:219], v18 offset:16384\n"
  "v_mfma_f32_16x16x32_bf16 v[72:75], a[196:199], a[88:91], v[72:75]\n"
  "ds_read_b128 a[220:223], v18 offset:17408\n"
  "v_mul_f32 v86, v86, v12\n"
  "v_mfma_f32_16x16x32_bf16 v[76:79], a[196:199], a[92:95], v[76:79]\n"
  "v_mul_f32 v87, v87, v12\n"
  "s_waitcnt lgkmcnt(4)\n"
  "v_mfma_f32_16x16x32_bf16 v[72:75], a[200:203], a[96:99], v[72:75]\n"
  "buffer_load_dwordx4 v17, s[56:59], s31 offen offset:1024 sc1 lds\n"
  "s_lshl_b32 s32, s81, 14\n"
  "v_mfma_f32_16x16x32_bf16 v[76:79], a[200:203], a[100:103], v[76:79]\n"
  "ds_read_b128 a[224:227], v18 offset:18432\n"
  "s_add_u32 s32, s32, s33\n"
  "v_mfma_f32_16x16x32_bf16 v[72:75], a[204:207], a[104:107], v[72:75]\n"
  "ds_read_b128 a[228:231], v18 offset:19456\n"
  "s_waitcnt vmcnt(7)\n"
  "s_mov_b64 exec, 1\n"
  "buffer_store_dword v20, off, s[60:63], s32 offset:24 sc1\n"
  "s_mov_b64 exec, -1\n"
  "v_mfma_f32_16x16x32_bf16 v[76:79], a[204:207], a[108:111], v[76:79]\n"
  "s_waitcnt lgkmcnt(4)\n"
  "v_mfma_f32_16x16x32_bf16 v[72:75], a[208:211], a[112:115], v[72:75]\n"
  "buffer_load_dwordx4 v17, s[56:59], s31 offen offset:2048 sc1 lds\n"
  "v_mfma_f32_16x16x32_bf16 v[76:79], a[208:211], a[116:119], v[76:79]\n"
  "ds_read_b128 a[232:235], v18 offset:20480\n"
  "v_mfma_f32_16x16x32_bf16 v[72:75], a[212:215], a[120:123], v[72:75]\n"
  "ds_read_b128 a[236:239], v18 offset:21504\n"
  "v_mfma_f32_16x16x32_bf16 v[76:79], a[212:215], a[124:127], v[76:79]\n"
  "s_waitcnt lgkmcnt(4)\n"
  "v_mfma_f32_16x16x32_bf16 v[80:83], a[216:219], a[0:3], v[80:83]\n"
  "buffer_load_dwordx4 v17, s[56:59], s31 offen offset:3072 sc1 lds\n"
  "v_mfma_f32_16x16x32_bf16 v[84:87], a[216:219], a[4:7], v[84:87]\n"
  "ds_read_b128 a[240:243], v18 offset:22528\n"
  "v_mfma_f32_16x16x32_bf16 v[80:83], a[220:223], a[8:11], v[80:83]\n"
  "ds_read_b128 a[244:247], v18 offset:23552\n"
  "v_cvt_pk_bf16_f32 v148, v72, v73\n"
  "v_cvt_pk_bf16_f32 v149, v74, v75\n"
  "v_mfma_f32_16x16x32_bf16 v[84:87], a[220:223], a[12:15], v[84:87]\n"
  "v_cvt_pk_bf16_f32 v150, v76, v77\n"
  "v_cvt_pk_bf16_f32 v151, v78, v79\n"
  "s_waitcnt lgkmcnt(4)\n"
  "v_mfma_f32_16x16x32_bf16 v[80:83], a[224:227], a[16:19], v[80:83]\n"
  "ds_read_b128 a[248:251], v18 offset:24576\n"
  "v_mfma_f32_16x16x32_bf16 v[84:87], a[224:227], a[20:23], v[84:87]\n"
  "buffer_store_dwordx4 v[148:151], v17, s[60:63], s29 offen offset:1024 sc1\n"
  "v_mfma_f32_16x16x32_bf16 v[80:83], a[228:231], a[24:27], v[80:83]\n"
  "ds_read_b128 a[252:255], v18 offset:25600\n"
  "v_mfma_f32_16x16x32_bf16 v[84:87], a[228:231], a[28:31], v[84:87]\n"
  "s_waitcnt lgkmcnt(4)\n"
  "v_mfma_f32_16x16x32_bf16 v[80:83], a[232:235], a[32:35], v[80:83]\n"
  "ds_read_b128 a[192:195], v18 offset:26624\n"
  "v_mfma_f32_16x16x32_bf16 v[84:87], a[232:235], a[36:39], v[84:87]\n"
  "v_mfma_f32_16x16x32_bf16 v[80:83], a[236:239], a[40:43], v[80:83]\n"
  "ds_read_b128 a[196:199], v18 offset:27648\n"
  "v_mfma_f32_16x16x32_bf16 v[84:87], a[236:239], a[44:47], v[84:87]\n"
  "s_waitcnt lgkmcnt(4)\n"
  "v_mfma_f32_16x16x32_bf16 v[80:83], a[240:243], a[48:51], v[80:83]\n"
  "ds_read_b128 a[200:203], v18 offset:28672\n"
  "v_mul_f32 v88, v88, v11\n"
  "v_mfma_f32_16x16x32_bf16 v[84:87], a[240:243], a[52:55], v[84:87]\n"
  "v_mul_f32 v89, v89, v11\n"
  "v_mfma_f32_16x16x32_bf16 v[80:83], a[244:247], a[56:59], v[80:83]\n"
  "ds_read_b128 a[204:207], v18 offset:29696\n"
  "v_mul_f32 v90, v90, v11\n"
  "v_mfma_f32_16x16x32_bf16 v[84:87], a[244:247], a[60:63], v[84:87]\n"
  "v_mul_f32 v91, v91, v11\n"
  "s_waitcnt lgkmcnt(4)\n"
  "v_mfma_f32_16x16x32_bf16 v[80:83], a[248:251], a[64:67], v[80:83]\n"
  "ds_read_b128 a[208:211], v18 offset:30720\n"
  "v_mfma_f32_16x16x32_bf16 v[84:87], a[248:251], a[68:71], v[84:87]\n"
  "v_mul_f32 v92, v92, v12\n"
  "v_mfma_f32_16x16x32_bf16 v[80:83], a[252:255], a[72:75], v[80:83]\n"
  "ds_read_b128 a[212:215], v18 offset:31744\n"
  "v_mul_f32 v93, v93, v12\n"
  "v_mfma_f32_16x16x32_bf16 v[84:87], a[252:255], a[76:79], v[84:87]\n"
  "s_barrier\n"
  "s_add_u32 m0, s68, 0\n"
  "s_lshl_b32 s31, s85, 17\n"
  "s_add_u32 s31, s31, 0x10000\n"
  "s_waitcnt lgkmcnt(4)\n"
  "v_mfma_f32_16x16x32_bf16 v[80:83], a[192:195], a[80:83], v[80:83]\n"
  "buffer_load_dwordx4 v17, s[56:59], s31 offen offset:0 sc1 lds\n"
  "v_mfma_f32_16x16x32_bf16 v[84:87], a[192:195], a[84:87], v[84:87]\n"
  "ds_read_b128 a[216:219], v18 offset:32768\n"
  "v_mfma_f32_16x16x32_bf16 v[80:83], a[196:199], a[88:91], v[80:83]\n"
  "ds_read_b128 a[220:223], v18 offset:33792\n"
  "v_mul_f32 v94, v94, v12\n"
  "v_mfma_f32_16x16x32_bf16 v[84:87], a[196:199], a[92:95], v[84:87]\n"
  "v_mul_f32 v95, v95, v12\n"
  "s_waitcnt lgkmcnt(4)\n"
  "v_mfma_f32_16x16x32_bf16 v[80:83], a[200:203], a[96:99], v[80:83]\n"
  "buffer_load_dwordx4 v17, s[56:59], s31 offen offset:1024 sc1 lds\n"
  "v_mfma_f32_16x16x32_bf16 v[84:87], a[200:203], a[100:103], v[84:87]\n"
  "ds_read_b128 a[224:227], v18 offset:34816\n"
  "v_mfma_f32_16x16x32_bf16 v[80:83], a[204:207], a[104:107], v[80:83]\n"
  "ds_read_b128 a[228:231], v18 offset:35840\n"
  "v_mfma_f32_16x16x32_bf16 v[84:87], a[204:207], a[108:111], v[84:87]\n"
  "s_mul_i32 s29, s83, 1835008\n"
  "s_waitcnt lgkmcnt(4)\n"
  "v_mfma_f32_16x16x32_bf16 v[80:83], a[208:211], a[112:115], v[80:83]\n"
  "buffer_load_dwordx4 v17, s[56:59], s31 offen offset:2048 sc1 lds\n"
  "v_mfma_f32_16x16x32_bf16 v[84:87], a[208:211], a[116:119], v[84:87]\n"
  "ds_read_b128 a[232:235], v18 offset:36864\n"
  "s_add_u32 s29, s29, s73\n"
  "v_mfma_f32_16x16x32_bf16 v[80:83], a[212:215], a[120:123], v[80:83]\n"
  "ds_read_b128 a[236:239], v18 offset:37888\n"
  "v_mfma_f32_16x16x32_bf16 v[84:87], a[212:215], a[124:127], v[84:87]\n"
  "s_add_u32 s29, s29, 49152\n"
  "s_waitcnt lgkmcnt(4)\n"
  "v_mfma_f32_16x16x32_bf16 v[88:91], a[216:219], a[0:3], v[88:91]\n"
  "buffer_load_dwordx4 v17, s[56:59], s31 offen offset:3072 sc1 lds\n"
  "v_mfma_f32_16x16x32_bf16 v[92:95], a[216:219], a[4:7], v[92:95]\n"
  "ds_read_b128 a[240:243], v18 offset:38912\n"
  "v_mfma_f32_16x16x32_bf16 v[88:91], a[220:223], a[8:11], v[88:91]\n"
  "ds_read_b128 a[244:247], v18 offset:39936\n"
  "v_cvt_pk_bf16_f32 v144, v80, v81\n"
  "v_cvt_pk_bf16_f32 v145, v82, v83\n"
  "v_mfma_f32_16x16x32_bf16 v[92:95], a[220:223], a[12:15], v[92:95]\n"
  "v_cvt_pk_bf16_f32 v146, v84, v85\n"
  "v_cvt_pk_bf16_f32 v147, v86, v87\n"
  "s_waitcnt lgkmcnt(4)\n"
  "v_mfma_f32_16x16x32_bf16 v[88:91], a[224:227], a[16:19], v[88:91]\n"
  "ds_read_b128 a[248:251], v18 offset:40960\n"
  "v_mfma_f32_16x16x32_bf16 v[92:95], a[224:227], a[20:23], v[92:95]\n"
  "buffer_store_dwordx4 v[144:147], v17, s[60:63], s29 offen offset:0 sc1\n"
  "v_mfma_f32_16x16x32_bf16 v[88:91], a[228:231], a[24:27], v[88:91]\n"
  "ds_read_b128 a[252:255], v18 offset:41984\n"
  "v_mfma_f32_16x16x32_bf16 v[92:95], a[228:231], a[28:31], v[92:95]\n"
  "s_waitcnt lgkmcnt(4)\n"
  "v_mfma_f32_16x16x32_bf16 v[88:91], a[232:235], a[32:35], v[88:91]\n"
  "ds_read_b128 a[192:195], v18 offset:43008\n"
  "v_mfma_f32_16x16x32_bf16 v[92:95], a[232:235], a[36:39], v[92:95]\n"
  "v_mfma_f32_16x16x32_bf16 v[88:91], a[236:239], a[40:43], v[88:91]\n"
  "ds_read_b128 a[196:199], v18 offset:44032\n"
  "v_mfma_f32_16x16x32_bf16 v[92:95], a[236:239], a[44:47], v[92:95]\n"
  "s_waitcnt lgkmcnt(4)\n"
  "v_mfma_f32_16x16x32_bf16 v[88:91], a[240:243], a[48:51], v[88:91]\n"
  "ds_read_b128 a[200:203], v18 offset:45056\n"
  "v_mul_f32 v96, v96, v11\n"
  "v_mfma_f32_16x16x32_bf16 v[92:95], a[240:243], a[52:55], v[92:95]\n"
  "v_mul_f32 v97, v97, v11\n"
  "v_mfma_f32_16x16x32_bf16 v[88:91], a[244:247], a[56:59], v[88:91]\n"
  "ds_read_b128 a[204:207], v18 offset:46080\n"
  "v_mul_f32 v98, v98, v11\n"
  "v_mfma_f32_16x16x32_bf16 v[92:95], a[244:247], a[60:63], v[92:95]\n"
  "v_mul_f32 v99, v99, v11\n"
  "s_waitcnt lgkmcnt(4)\n"
  "v_mfma_f32_16x16x32_bf16 v[88:91], a[248:251], a[64:67], v[88:91]\n"
  "ds_read_b128 a[208:211], v18 offset:47104\n"
  "v_mfma_f32_16x16x32_bf16 v[92:95], a[248:251], a[68:71], v[92:95]\n"
  "v_mul_f32 v100, v100, v12\n"
  "v_mfma_f32_16x16x32_bf16 v[88:91], a[252:255], a[72:75], v[88:91]\n"
  "ds_read_b128 a[212:215], v18 offset:48128\n"
  "v_mul_f32 v101, v101, v12\n"
  "v_mfma_f32_16x16x32_bf16 v[92:95], a[252:255], a[76:79], v[92:95]\n"
  "s_barrier\n"
  "s_add_u32 m0, s68, 16384\n"
  "s_lshl_b32 s31, s86, 17\n"
  "s_waitcnt lgkmcnt(4)\n"
  "v_mfma_f32_16x16x32_bf16 v[88:91], a[192:195], a[80:83], v[88:91]\n"
  "buffer_load_dwordx4 v17, s[56:59], s31 offen offset:0 sc1 lds\n"
  "v_mfma_f32_16x16x32_bf16 v[92:95], a[192:195], a[84:87], v[92:95]\n"
  "ds_read_b128 a[216:219], v18 offset:49152\n"
  "v_mfma_f32_16x16x32_bf16 v[88:91], a[196:199], a[88:91], v[88:91]\n"
  "ds_read_b128 a[220:223], v18 offset:50176\n"
  "v_mul_f32 v102, v102, v12\n"
  "v_mfma_f32_16x16x32_bf16 v[92:95], a[196:199], a[92:95], v[92:95]\n"
  "v_mul_f32 v103, v103, v12\n"
  "s_waitcnt lgkmcnt(4)\n"
  "v_mfma_f32_16x16x32_bf16 v[88:91], a[200:203], a[96:99], v[88:91]\n"
  "buffer_load_dwordx4 v17, s[56:59], s31 offen offset:1024 sc1 lds\n"
  "s_lshl_b32 s32, s82, 14\n"
  "v_mfma_f32_16x16x32_bf16 v[92:95], a[200:203], a[100:103], v[92:95]\n"
  "ds_read_b128 a[224:227], v18 offset:51200\n"
  "s_add_u32 s32, s32, s33\n"
  "v_mfma_f32_16x16x32_bf16 v[88:91], a[204:207], a[104:107], v[88:91]\n"
  "ds_read_b128 a[228:231], v18 offset:52224\n"
  "s_waitcnt vmcnt(7)\n"
  "s_mov_b64 exec, 1\n"
  "buffer_store_dword v20, off, s[60:63], s32 offset:24 sc1\n"
  "s_mov_b64 exec, -1\n"
  "v_mfma_f32_16x16x32_bf16 v[92:95], a[204:207], a[108:111], v[92:95]\n"
  "s_waitcnt lgkmcnt(4)\n"
  "v_mfma_f32_16x16x32_bf16 v[88:91], a[208:211], a[112:115], v[88:91]\n"
  "buffer_load_dwordx4 v17, s[56:59], s31 offen offset:2048 sc1 lds\n"
  "v_mfma_f32_16x16x32_bf16 v[92:95], a[208:211], a[116:119], v[92:95]\n"
  "ds_read_b128 a[232:235], v18 offset:53248\n"
  "v_mfma_f32_16x16x32_bf16 v[88:91], a[212:215], a[120:123], v[88:91]\n"
  "ds_read_b128 a[236:239], v18 offset:54272\n"
  "v_mfma_f32_16x16x32_bf16 v[92:95], a[212:215], a[124:127], v[92:95]\n"
  "s_waitcnt lgkmcnt(4)\n"
  "v_mfma_f32_16x16x32_bf16 v[96:99], a[216:219], a[0:3], v[96:99]\n"
  "buffer_load_dwordx4 v17, s[56:59], s31 offen offset:3072 sc1 lds\n"
  "v_mfma_f32_16x16x32_bf16 v[100:103], a[216:219], a[4:7], v[100:103]\n"
  "ds_read_b128 a[240:243], v18 offset:55296\n"
  "v_mfma_f32_16x16x32_bf16 v[96:99], a[220:223], a[8:11], v[96:99]\n"
  "ds_read_b128 a[244:247], v18 offset:56320\n"
  "v_cvt_pk_bf16_f32 v148, v88, v89\n"
  "v_cvt_pk_bf16_f32 v149, v90, v91\n"
  "v_mfma_f32_16x16x32_bf16 v[100:103], a[220:223], a[12:15], v[100:103]\n"
  "v_cvt_pk_bf16_f32 v150, v92, v93\n"
  "v_cvt_pk_bf16_f32 v151, v94, v95\n"
  "s_waitcnt lgkmcnt(4)\n"
  "v_mfma_f32_16x16x32_bf16 v[96:99], a[224:227], a[16:19], v[96:99]\n"
  "ds_read_b128 a[248:251], v18 offset:57344\n"
  "v_mfma_f32_16x16x32_bf16 v[100:103], a[224:227], a[20:23], v[100:103]\n"
  "buffer_store_dwordx4 v[148:151], v17, s[60:63], s29 offen offset:1024 sc1\n"
  "v_mfma_f32_16x16x32_bf16 v[96:99], a[228:231], a[24:27], v[96:99]\n"
  "ds_read_b128 a[252:255], v18 offset:58368\n"
  "v_mfma_f32_16x16x32_bf16 v[100:103], a[228:231], a[28:31], v[100:103]\n"
  "s_waitcnt lgkmcnt(4)\n"
  "v_mfma_f32_16x16x32_bf16 v[96:99], a[232:235], a[32:35], v[96:99]\n"
  "ds_read_b128 a[192:195], v18 offset:59392\n"
  "v_mfma_f32_16x16x32_bf16 v[100:103], a[232:235], a[36:39], v[100:103]\n"
  "v_mfma_f32_16x16x32_bf16 v[96:99], a[236:239], a[40:43], v[96:99]\n"
  "ds_read_b128 a[196:199], v18 offset:60416\n"
  "v_mfma_f32_16x16x32_bf16 v[100:103], a[236:239], a[44:47], v[100:103]\n"
  "s_waitcnt lgkmcnt(4)\n"
  "v_mfma_f32_16x16x32_bf16 v[96:99], a[240:243], a[48:51], v[96:99]\n"
  "ds_read_b128 a[200:203], v18 offset:61440\n"
  "v_mul_f32 v104, v104, v11\n"
  "v_mfma_f32_16x16x32_bf16 v[100:103], a[240:243], a[52:55], v[100:103]\n"
  "v_mul_f32 v105, v105, v11\n"
  "v_mfma_f32_16x16x32_bf16 v[96:99], a[244:247], a[56:59], v[96:99]\n"
  "ds_read_b128 a[204:207], v18 offset:62464\n"
  "v_mul_f32 v106, v106, v11\n"
  "v_mfma_f32_16x16x32_bf16 v[100:103], a[244:247], a[60:63], v[100:103]\n"
  "v_mul_f32 v107, v107, v11\n"
  "s_waitcnt lgkmcnt(4)\n"
  "v_mfma_f32_16x16x32_bf16 v[96:99], a[248:251], a[64:67], v[96:99]\n"
  "ds_read_b128 a[208:211], v18 offset:63488\n"
  "v_mfma_f32_16x16x32_bf16 v[100:103], a[248:251], a[68:71], v[100:103]\n"
  "v_mul_f32 v108, v108, v12\n"
  "v_mfma_f32_16x16x32_bf16 v[96:99], a[252:255], a[72:75], v[96:99]\n"
  "ds_read_b128 a[212:215], v18 offset:64512\n"
  "v_mul_f32 v109, v109, v12\n"
  "v_mfma_f32_16x16x32_bf16 v[100:103], a[252:255], a[76:79], v[100:103]\n"
  "s_barrier\n"
  "s_add_u32 m0, s68, 32768\n"
  "s_lshl_b32 s31, s86, 17\n"
  "s_add_u32 s31, s31, 0x10000\n"
  "s_waitcnt lgkmcnt(4)\n"
  "v_mfma_f32_16x16x32_bf16 v[96:99], a[192:195], a[80:83], v[96:99]\n"
  "buffer_load_dwordx4 v17, s[56:59], s31 offen offset:0 sc1 lds\n"
  "v_mfma_f32_16x16x32_bf16 v[100:103], a[192:195], a[84:87], v[100:103]\n"
  "ds_read_b128 a[216:219], v19 offset:0\n"
  "v_mfma_f32_16x16x32_bf16 v[96:99], a[196:199], a[88:91], v[96:99]\n"
  "ds_read_b128 a[220:223], v19 offset:1024\n"
  "v_mul_f32 v110, v110, v12\n"
  "v_mfma_f32_16x16x32_bf16 v[100:103], a[196:199], a[92:95], v[100:103]\n"
  "v_mul_f32 v111, v111, v12\n"
  "s_waitcnt lgkmcnt(4)\n"
  "v_mfma_f32_16x16x32_bf16 v[96:99], a[200:203], a[96:99], v[96:99]\n"
  "buffer_load_dwordx4 v17, s[56:59], s31 offen offset:1024 sc1 lds\n"
  "v_mfma_f32_16x16x32_bf16 v[100:103], a[200:203], a[100:103], v[100:103]\n"
  "ds_read_b128 a[224:227], v19 offset:2048\n"
  "v_mfma_f32_16x16x32_bf16 v[96:99], a[204:207], a[104:107], v[96:99]\n"
  "ds_read_b128 a[228:231], v19 offset:3072\n"
  "v_mfma_f32_16x16x32_bf16 v[100:103], a[204:207], a[108:111], v[100:103]\n"
  "s_mul_i32 s29, s84, 1835008\n"
  "s_waitcnt lgkmcnt(4)\n"
  "v_mfma_f32_16x16x32_bf16 v[96:99], a[208:211], a[112:115], v[96:99]\n"
  "buffer_load_dwordx4 v17, s[56:59], s31 offen offset:2048 sc1 lds\n"
  "v_mfma_f32_16x16x32_bf16 v[100:103], a[208:211], a[116:119], v[100:103]\n"
  "ds_read_b128 a[232:235], v19 offset:4096\n"
  "s_add_u32 s29, s29, s73\n"
  "v_mfma_f32_16x16x32_bf16 v[96:99], a[212:215], a[120:123], v[96:99]\n"
  "ds_read_b128 a[236:239], v19 offset:5120\n"
  "v_mfma_f32_16x16x32_bf16 v[100:103], a[212:215], a[124:127], v[100:103]\n"
  "s_add_u32 s29, s29, 49152\n"
  "s_waitcnt lgkmcnt(4)\n"
  "v_mfma_f32_16x16x32_bf16 v[104:107], a[216:219], a[0:3], v[104:107]\n"
  "buffer_load_dwordx4 v17, s[56:59], s31 offen offset:3072 sc1 lds\n"
  "v_mfma_f32_16x16x32_bf16 v[108:111], a[216:219], a[4:7], v[108:111]\n"
  "ds_read_b128 a[240:243], v19 offset:6144\n"
  "v_mfma_f32_16x16x32_bf16 v[104:107], a[220:223], a[8:11], v[104:107]\n"
  "ds_read_b128 a[244:247], v19 offset:7168\n"
  "v_cvt_pk_bf16_f32 v144, v96, v97\n"
  "v_cvt_pk_bf16_f32 v145, v98, v99\n"
  "v_mfma_f32_16x16x32_bf16 v[108:111], a[220:223], a[12:15], v[108:111]\n"
  "v_cvt_pk_bf16_f32 v146, v100, v101\n"
  "v_cvt_pk_bf16_f32 v147, v102, v103\n"
  "s_waitcnt lgkmcnt(4)\n"
  "v_mfma_f32_16x16x32_bf16 v[104:107], a[224:227], a[16:19], v[104:107]\n"
  "ds_read_b128 a[248:251], v19 offset:8192\n"
  "v_mfma_f32_16x16x32_bf16 v[108:111], a[224:227], a[20:23], v[108:111]\n"
  "buffer_store_dwordx4 v[144:147], v17, s[60:63], s29 offen offset:0 sc1\n"
  "v_mfma_f32_16x16x32_bf16 v[104:107], a[228:231], a[24:27], v[104:107]\n"
  "ds_read_b128 a[252:255], v19 offset:9216\n"
  "v_mfma_f32_16x16x32_bf16 v[108:111], a[228:231], a[28:31], v[108:111]\n"
  "s_waitcnt lgkmcnt(4)\n"
  "v_mfma_f32_16x16x32_bf16 v[104:107], a[232:235], a[32:35], v[104:107]\n"
  "ds_read_b128 a[192:195], v19 offset:10240\n"
  "v_mfma_f32_16x16x32_bf16 v[108:111], a[232:235], a[36:39], v[108:111]\n"
  "v_mfma_f32_16x16x32_bf16 v[104:107], a[236:239], a[40:43], v[104:107]\n"
  "ds_read_b128 a[196:199], v19 offset:11264\n"
  "v_mfma_f32_16x16x32_bf16 v[108:111], a[236:239], a[44:47], v[108:111]\n"
  "s_waitcnt lgkmcnt(4)\n"
  "v_mfma_f32_16x16x32_bf16 v[104:107], a[240:243], a[48:51], v[104:107]\n"
  "ds_read_b128 a[200:203], v19 offset:12288\n"
  "v_mul_f32 v112, v112, v11\n"
  "v_mfma_f32_16x16x32_bf16 v[108:111], a[240:243], a[52:55], v[108:111]\n"
  "v_mul_f32 v113, v113, v11\n"
  "v_mfma_f32_16x16x32_bf16 v[104:107], a[244:247], a[56:59], v[104:107]\n"
  "ds_read_b128 a[204:207], v19 offset:13312\n"
  "v_mfma_f32_16x16x32_bf16 v[108:111], a[244:247], a[60:63], v[108:111]\n"
  "v_mul_f32 v114, v114, v11\n"
  "s_waitcnt lgkmcnt(4)\n"
  "v_mfma_f32_16x16x32_bf16 v[104:107], a[248:251], a[64:67], v[104:107]\n"
  "ds_read_b128 a[208:211], v19 offset:14336\n"
  "v_mul_f32 v115, v115, v11\n"
  "v_mfma_f32_16x16x32_bf16 v[108:111], a[248:251], a[68:71], v[108:111]\n"
  "v_mfma_f32_16x16x32_bf16 v[104:107], a[252:255], a[72:75], v[104:107]\n"
  "ds_read_b128 a[212:215], v19 offset:15360\n"
  "v_mul_f32 v116, v116, v12\n"
  "v_mfma_f32_16x16x32_bf16 v[108:111], a[252:255], a[76:79], v[108:111]\n"
  "s_barrier\n"
  "s_waitcnt lgkmcnt(4)\n"
  "v_mfma_f32_16x16x32_bf16 v[104:107], a[192:195], a[80:83], v[104:107]\n"
  "ds_read_b128 a[216:219], v19 offset:16384\n"
  "v_mfma_f32_16x16x32_bf16 v[108:111], a[192:195], a[84:87], v[108:111]\n"
  "v_mul_f32 v117, v117, v12\n"
  "v_mfma_f32_16x16x32_bf16 v[104:107], a[196:199], a[88:91], v[104:107]\n"
  "ds_read_b128 a[220:223], v19 offset:17408\n"
  "v_mul_f32 v118, v118, v12\n"
  "v_mfma_f32_16x16x32_bf16 v[108:111], a[196:199], a[92:95], v[108:111]\n"
  "s_waitcnt lgkmcnt(4)\n"
  "v_mfma_f32_16x16x32_bf16 v[104:107], a[200:203], a[96:99], v[104:107]\n"
  "ds_read_b128 a[224:227], v19 offset:18432\n"
  "s_lshl_b32 s32, s83, 14\n"
  "v_mfma_f32_16x16x32_bf16 v[108:111], a[200:203], a[100:103], v[108:111]\n"
  "s_add_u32 s32, s32, s33\n"
  "v_mfma_f32_16x16x32_bf16 v[104:107], a[204:207], a[104:107], v[104:107]\n"
  "ds_read_b128 a[228:231], v19 offset:19456\n"
  "s_waitcnt vmcnt(5)\n"
  "s_mov_b64 exec, 1\n"
  "buffer_store_dword v20, off, s[60:63], s32 offset:24 sc1\n"
  "s_mov_b64 exec, -1\n"
  "v_mfma_f32_16x16x32_bf16 v[108:111], a[204:207], a[108:111], v[108:111]\n"
  "v_mul_f32 v119, v119, v12\n"
  "s_waitcnt lgkmcnt(4)\n"
  "v_mfma_f32_16x16x32_bf16 v[104:107], a[208:211], a[112:115], v[104:107]\n"
  "ds_read_b128 a[232:235], v19 offset:20480\n"
  "v_mfma_f32_16x16x32_bf16 v[108:111], a[208:211], a[116:119], v[108:111]\n"
  "v_mfma_f32_16x16x32_bf16 v[104:107], a[212:215], a[120:123], v[104:107]\n"
  "ds_read_b128 a[236:239], v19 offset:21504\n"
  "v_mfma_f32_16x16x32_bf16 v[108:111], a[212:215], a[124:127], v[108:111]\n"
  "s_waitcnt lgkmcnt(4)\n"
  "v_mfma_f32_16x16x32_bf16 v[112:115], a[216:219], a[0:3], v[112:115]\n"
  "ds_read_b128 a[240:243], v19 offset:22528\n"
  "v_mfma_f32_16x16x32_bf16 v[116:119], a[216:219], a[4:7], v[116:119]\n"
  "v_mfma_f32_16x16x32_bf16 v[112:115], a[220:223], a[8:11], v[112:115]\n"
  "ds_read_b128 a[244:247], v19 offset:23552\n"
  "v_cvt_pk_bf16_f32 v148, v104, v105\n"
  "v_cvt_pk_bf16_f32 v149, v106, v107\n"
  "v_mfma_f32_16x16x32_bf16 v[116:119], a[220:223], a[12:15], v[116:119]\n"
  "v_cvt_pk_bf16_f32 v150, v108, v109\n"
  "v_cvt_pk_bf16_f32 v151, v110, v111\n"
  "s_waitcnt lgkmcnt(4)\n"
  "v_mfma_f32_16x16x32_bf16 v[112:115], a[224:227], a[16:19], v[112:115]\n"
  "ds_read_b128 a[248:251], v19 offset:24576\n"
  "v_mfma_f32_16x16x32_bf16 v[116:119], a[224:227], a[20:23], v[116:119]\n"
  "buffer_store_dwordx4 v[148:151], v17, s[60:63], s29 offen offset:1024 sc1\n"
  "v_mfma_f32_16x16x32_bf16 v[112:115], a[228:231], a[24:27], v[112:115]\n"
  "ds_read_b128 a[252:255], v19 offset:25600\n"
  "v_mfma_f32_16x16x32_bf16 v[116:119], a[228:231], a[28:31], v[116:119]\n"
  "s_waitcnt lgkmcnt(4)\n"
  "v_mfma_f32_16x16x32_bf16 v[112:115], a[232:235], a[32:35], v[112:115]\n"
  "ds_read_b128 a[192:195], v19 offset:26624\n"
  "v_mfma_f32_16x16x32_bf16 v[116:119], a[232:235], a[36:39], v[116:119]\n"
  "v_mfma_f32_16x16x32_bf16 v[112:115], a[236:239], a[40:43], v[112:115]\n"
  "ds_read_b128 a[196:199], v19 offset:27648\n"
  "v_mfma_f32_16x16x32_bf16 v[116:119], a[236:239], a[44:47], v[116:119]\n"
  "s_waitcnt lgkmcnt(4)\n"
  "v_mfma_f32_16x16x32_bf16 v[112:115], a[240:243], a[48:51], v[112:115]\n"
  "ds_read_b128 a[200:203], v19 offset:28672\n"
  "v_mul_f32 v120, v120, v11\n"
  "v_mfma_f32_16x16x32_bf16 v[116:119], a[240:243], a[52:55], v[116:119]\n"
  "v_mul_f32 v121, v121, v11\n"
  "v_mfma_f32_16x16x32_bf16 v[112:115], a[244:247], a[56:59], v[112:115]\n"
  "ds_read_b128 a[204:207], v19 offset:29696\n"
  "v_mfma_f32_16x16x32_bf16 v[116:119], a[244:247], a[60:63], v[116:119]\n"
  "v_mul_f32 v122, v122, v11\n"
  "s_waitcnt lgkmcnt(4)\n"
  "v_mfma_f32_16x16x32_bf16 v[112:115], a[248:251], a[64:67], v[112:115]\n"
  "ds_read_b128 a[208:211], v19 offset:30720\n"
  "v_mfma_f32_16x16x32_bf16 v[116:119], a[248:251], a[68:71], v[116:119]\n"
  "v_mul_f32 v123, v123, v11\n"
  "v_mfma_f32_16x16x32_bf16 v[112:115], a[252:255], a[72:75], v[112:115]\n"
  "ds_read_b128 a[212:215], v19 offset:31744\n"
  "v_mfma_f32_16x16x32_bf16 v[116:119], a[252:255], a[76:79], v[116:119]\n"
  "s_barrier\n"
  "s_waitcnt lgkmcnt(4)\n"
  "v_mfma_f32_16x16x32_bf16 v[112:115], a[192:195], a[80:83], v[112:115]\n"
  "ds_read_b128 a[216:219], v18 offset:0\n"
  "v_mul_f32 v124, v124, v12\n"
  "v_mfma_f32_16x16x32_bf16 v[116:119], a[192:195], a[84:87], v[116:119]\n"
  "v_mfma_f32_16x16x32_bf16 v[112:115], a[196:199], a[88:91], v[112:115]\n"
  "ds_read_b128 a[220:223], v18 offset:1024\n"
  "v_mul_f32 v125, v125, v12\n"
  "v_mfma_f32_16x16x32_bf16 v[116:119], a[196:199], a[92:95], v[116:119]\n"
  "s_waitcnt lgkmcnt(4)\n"
  "v_mfma_f32_16x16x32_bf16 v[112:115], a[200:203], a[96:99], v[112:115]\n"
  "ds_read_b128 a[224:227], v18 offset:2048\n"
  "v_mul_f32 v126, v126, v12\n"
  "v_mfma_f32_16x16x32_bf16 v[116:119], a[200:203], a[100:103], v[116:119]\n"
  "v_mfma_f32_16x16x32_bf16 v[112:115], a[204:207], a[104:107], v[112:115]\n"
  "ds_read_b128 a[228:231], v18 offset:3072\n"
  "v_mul_f32 v127, v127, v12\n"
  "v_mfma_f32_16x16x32_bf16 v[116:119], a[204:207], a[108:111], v[116:119]\n"
  "s_mul_i32 s29, s85, 1835008\n"
  "s_waitcnt lgkmcnt(4)\n"
  "v_mfma_f32_16x16x32_bf16 v[112:115], a[208:211], a[112:115], v[112:115]\n"
  "ds_read_b128 a[232:235], v18 offset:4096\n"
  "v_mfma_f32_16x16x32_bf16 v[116:119], a[208:211], a[116:119], v[116:119]\n"
  "s_add_u32 s29, s29, s73\n"
  "v_mfma_f32_16x16x32_bf16 v[112:115], a[212:215], a[120:123], v[112:115]\n"
  "ds_read_b128 a[236:239], v18 offset:5120\n"
  "v_mfma_f32_16x16x32_bf16 v[116:119], a[212:215], a[124:127], v[116:119]\n"
  "s_add_u32 s29, s29, 49152\n"
  "s_waitcnt lgkmcnt(4)\n"
  "v_mfma_f32_16x16x32_bf16 v[120:123], a[216:219], a[0:3], v[120:123]\n"
  "ds_read_b128 a[240:243], v18 offset:6144\n"
  "v_mfma_f32_16x16x32_bf16 v[124:127], a[216:219], a[4:7], v[124:127]\n"
  "v_mfma_f32_16x16x32_bf16 v[120:123], a[220:223], a[8:11], v[120:123]\n"
  "ds_read_b128 a[244:247], v18 offset:7168\n"
  "v_cvt_pk_bf16_f32 v144, v112, v113\n"
  "v_cvt_pk_bf16_f32 v145, v114, v115\n"
  "v_mfma_f32_16x16x32_bf16 v[124:127], a[220:223], a[12:15], v[124:127]\n"
  "v_cvt_pk_bf16_f32 v146, v116, v117\n"
  "v_cvt_pk_bf16_f32 v147, v118, v119\n"
  "s_waitcnt lgkmcnt(4)\n"
  "v_mfma_f32_16x16x32_bf16 v[120:123], a[224:227], a[16:19], v[120:123]\n"
  "ds_read_b128 a[248:251], v18 offset:8192\n"
  "v_mfma_f32_16x16x32_bf16 v[124:127], a[224:227], a[20:23], v[124:127]\n"
  "buffer_store_dwordx4 v[144:147], v17, s[60:63], s29 offen offset:0 sc1\n"
  "v_mfma_f32_16x16x32_bf16 v[120:123], a[228:231], a[24:27], v[120:123]\n"
  "ds_read_b128 a[252:255], v18 offset:9216\n"
  "v_mfma_f32_16x16x32_bf16 v[124:127], a[228:231], a[28:31], v[124:127]\n"
  "s_waitcnt lgkmcnt(4)\n"
  "v_mfma_f32_16x16x32_bf16 v[120:123], a[232:235], a[32:35], v[120:123]\n"
  "ds_read_b128 a[192:195], v18 offset:10240\n"
  "v_mfma_f32_16x16x32_bf16 v[124:127], a[232:235], a[36:39], v[124:127]\n"
  "v_mfma_f32_16x16x32_bf16 v[120:123], a[236:239], a[40:43], v[120:123]\n"
  "ds_read_b128 a[196:199], v18 offset:11264\n"
  "v_mfma_f32_16x16x32_bf16 v[124:127], a[236:239], a[44:47], v[124:127]\n"
  "s_waitcnt lgkmcnt(4)\n"
  "v_mfma_f32_16x16x32_bf16 v[120:123], a[240:243], a[48:51], v[120:123]\n"
  "ds_read_b128 a[200:203], v18 offset:12288\n"
  "v_mul_f32 v128, v128, v11\n"
  "v_mfma_f32_16x16x32_bf16 v[124:127], a[240:243], a[52:55], v[124:127]\n"
  "v_mul_f32 v129, v129, v11\n"
  "v_mfma_f32_16x16x32_bf16 v[120:123], a[244:247], a[56:59], v[120:123]\n"
  "ds_read_b128 a[204:207], v18 offset:13312\n"
  "v_mfma_f32_16x16x32_bf16 v[124:127], a[244:247], a[60:63], v[124:127]\n"
  "v_mul_f32 v130, v130, v11\n"
  "s_waitcnt lgkmcnt(4)\n"
  "v_mfma_f32_16x16x32_bf16 v[120:123], a[248:251], a[64:67], v[120:123]\n"
  "ds_read_b128 a[208:211], v18 offset:14336\n"
  "v_mul_f32 v131, v131, v11\n"
  "v_mfma_f32_16x16x32_bf16 v[124:127], a[248:251], a[68:71], v[124:127]\n"
  "v_mfma_f32_16x16x32_bf16 v[120:123], a[252:255], a[72:75], v[120:123]\n"
  "ds_read_b128 a[212:215], v18 offset:15360\n"
  "v_mul_f32 v132, v132, v12\n"
  "v_mfma_f32_16x16x32_bf16 v[124:127], a[252:255], a[76:79], v[124:127]\n"
  "s_barrier\n"
  "s_waitcnt lgkmcnt(4)\n"
  "v_mfma_f32_16x16x32_bf16 v[120:123], a[192:195], a[80:83], v[120:123]\n"
  "ds_read_b128 a[216:219], v18 offset:16384\n"
  "v_mfma_f32_16x16x32_bf16 v[124:127], a[192:195], a[84:87], v[124:127]\n"
  "v_mul_f32 v133, v133, v12\n"
  "v_mfma_f32_16x16x32_bf16 v[120:123], a[196:199], a[88:91], v[120:123]\n"
  "ds_read_b128 a[220:223], v18 offset:17408\n"
  "v_mul_f32 v134, v134, v12\n"
  "v_mfma_f32_16x16x32_bf16 v[124:127], a[196:199], a[92:95], v[124:127]\n"
  "s_waitcnt lgkmcnt(4)\n"
  "v_mfma_f32_16x16x32_bf16 v[120:123], a[200:203], a[96:99], v[120:123]\n"
  "ds_read_b128 a[224:227], v18 offset:18432\n"
  "s_lshl_b32 s32, s84, 14\n"
  "v_mfma_f32_16x16x32_bf16 v[124:127], a[200:203], a[100:103], v[124:127]\n"
  "s_add_u32 s32, s32, s33\n"
  "v_mfma_f32_16x16x32_bf16 v[120:123], a[204:207], a[104:107], v[120:123]\n"
  "ds_read_b128 a[228:231], v18 offset:19456\n"
  "s_waitcnt vmcnt(1)\n"
  "s_mov_b64 exec, 1\n"
  "buffer_store_dword v20, off, s[60:63], s32 offset:24 sc1\n"
  "s_mov_b64 exec, -1\n"
  "v_mfma_f32_16x16x32_bf16 v[124:127], a[204:207], a[108:111], v[124:127]\n"
  "v_mul_f32 v135, v135, v12\n"
  "s_waitcnt lgkmcnt(4)\n"
  "v_mfma_f32_16x16x32_bf16 v[120:123], a[208:211], a[112:115], v[120:123]\n"
  "ds_read_b128 a[232:235], v18 offset:20480\n"
  "v_mfma_f32_16x16x32_bf16 v[124:127], a[208:211], a[116:119], v[124:127]\n"
  "v_mfma_f32_16x16x32_bf16 v[120:123], a[212:215], a[120:123], v[120:123]\n"
  "ds_read_b128 a[236:239], v18 offset:21504\n"
  "v_mfma_f32_16x16x32_bf16 v[124:127], a[212:215], a[124:127], v[124:127]\n"
  "s_waitcnt lgkmcnt(4)\n"
  "v_mfma_f32_16x16x32_bf16 v[128:131], a[216:219], a[0:3], v[128:131]\n"
  "ds_read_b128 a[240:243], v18 offset:22528\n"
  "v_mfma_f32_16x16x32_bf16 v[132:135], a[216:219], a[4:7], v[132:135]\n"
  "v_mfma_f32_16x16x32_bf16 v[128:131], a[220:223], a[8:11], v[128:131]\n"
  "ds_read_b128 a[244:247], v18 offset:23552\n"
  "v_cvt_pk_bf16_f32 v148, v120, v121\n"
  "v_cvt_pk_bf16_f32 v149, v122, v123\n"
  "v_mfma_f32_16x16x32_bf16 v[132:135], a[220:223], a[12:15], v[132:135]\n"
  "v_cvt_pk_bf16_f32 v150, v124, v125\n"
  "v_cvt_pk_bf16_f32 v151, v126, v127\n"
  "s_waitcnt lgkmcnt(4)\n"
  "v_mfma_f32_16x16x32_bf16 v[128:131], a[224:227], a[16:19], v[128:131]\n"
  "ds_read_b128 a[248:251], v18 offset:24576\n"
  "v_mfma_f32_16x16x32_bf16 v[132:135], a[224:227], a[20:23], v[132:135]\n"
  "buffer_store_dwordx4 v[148:151], v17, s[60:63], s29 offen offset:1024 sc1\n"
  "v_mfma_f32_16x16x32_bf16 v[128:131], a[228:231], a[24:27], v[128:131]\n"
  "ds_read_b128 a[252:255], v18 offset:25600\n"
  "v_mfma_f32_16x16x32_bf16 v[132:135], a[228:231], a[28:31], v[132:135]\n"
  "s_waitcnt lgkmcnt(4)\n"
  "v_mfma_f32_16x16x32_bf16 v[128:131], a[232:235], a[32:35], v[128:131]\n"
  "ds_read_b128 a[192:195], v18 offset:26624\n"
  "v_mfma_f32_16x16x32_bf16 v[132:135], a[232:235], a[36:39], v[132:135]\n"
  "v_mfma_f32_16x16x32_bf16 v[128:131], a[236:239], a[40:43], v[128:131]\n"
  "ds_read_b128 a[196:199], v18 offset:27648\n"
  "v_mfma_f32_16x16x32_bf16 v[132:135], a[236:239], a[44:47], v[132:135]\n"
  "s_waitcnt lgkmcnt(4)\n"
  "v_mfma_f32_16x16x32_bf16 v[128:131], a[240:243], a[48:51], v[128:131]\n"
  "ds_read_b128 a[200:203], v18 offset:28672\n"
  "v_mul_f32 v136, v136, v11\n"
  "v_mfma_f32_16x16x32_bf16 v[132:135], a[240:243], a[52:55], v[132:135]\n"
  "v_mul_f32 v137, v137, v11\n"
  "v_mfma_f32_16x16x32_bf16 v[128:131], a[244:247], a[56:59], v[128:131]\n"
  "ds_read_b128 a[204:207], v18 offset:29696\n"
  "v_mfma_f32_16x16x32_bf16 v[132:135], a[244:247], a[60:63], v[132:135]\n"
  "v_mul_f32 v138, v138, v11\n"
  "s_waitcnt lgkmcnt(4)\n"
  "v_mfma_f32_16x16x32_bf16 v[128:131], a[248:251], a[64:67], v[128:131]\n"
  "ds_read_b128 a[208:211], v18 offset:30720\n"
  "v_mfma_f32_16x16x32_bf16 v[132:135], a[248:251], a[68:71], v[132:135]\n"
  "v_mul_f32 v139, v139, v11\n"
  "v_mfma_f32_16x16x32_bf16 v[128:131], a[252:255], a[72:75], v[128:131]\n"
  "ds_read_b128 a[212:215], v18 offset:31744\n"
  "v_mfma_f32_16x16x32_bf16 v[132:135], a[252:255], a[76:79], v[132:135]\n"
  "s_barrier\n"
  "s_waitcnt lgkmcnt(4)\n"
  "v_mfma_f32_16x16x32_bf16 v[128:131], a[192:195], a[80:83], v[128:131]\n"
  "ds_read_b128 a[216:219], v18 offset:32768\n"
  "v_mul_f32 v140, v140, v12\n"
  "v_mfma_f32_16x16x32_bf16 v[132:135], a[192:195], a[84:87], v[132:135]\n"
  "v_mfma_f32_16x16x32_bf16 v[128:131], a[196:199], a[88:91], v[128:131]\n"
  "ds_read_b128 a[220:223], v18 offset:33792\n"
  "v_mul_f32 v141, v141, v12\n"
  "v_mfma_f32_16x16x32_bf16 v[132:135], a[196:199], a[92:95], v[132:135]\n"
  "s_waitcnt lgkmcnt(4)\n"
  "v_mfma_f32_16x16x32_bf16 v[128:131], a[200:203], a[96:99], v[128:131]\n"
  "ds_read_b128 a[224:227], v18 offset:34816\n"
  "v_mul_f32 v142, v142, v12\n"
  "v_mfma_f32_16x16x32_bf16 v[132:135], a[200:203], a[100:103], v[132:135]\n"
  "v_mfma_f32_16x16x32_bf16 v[128:131], a[204:207], a[104:107], v[128:131]\n"
  "ds_read_b128 a[228:231], v18 offset:35840\n"
  "v_mul_f32 v143, v143, v12\n"
  "v_mfma_f32_16x16x32_bf16 v[132:135], a[204:207], a[108:111], v[132:135]\n"
  "s_mul_i32 s29, s86, 1835008\n"
  "s_waitcnt lgkmcnt(4)\n"
  "v_mfma_f32_16x16x32_bf16 v[128:131], a[208:211], a[112:115], v[128:131]\n"
  "ds_read_b128 a[232:235], v18 offset:36864\n"
  "v_mfma_f32_16x16x32_bf16 v[132:135], a[208:211], a[116:119], v[132:135]\n"
  "s_add_u32 s29, s29, s73\n"
  "v_mfma_f32_16x16x32_bf16 v[128:131], a[212:215], a[120:123], v[128:131]\n"
  "ds_read_b128 a[236:239], v18 offset:37888\n"
  "s_lshl_b32 s32, s85, 14\n"
  "v_mfma_f32_16x16x32_bf16 v[132:135], a[212:215], a[124:127], v[132:135]\n"
  "s_add_u32 s32, s32, s33\n"
  "s_add_u32 s29, s29, 49152\n"
  "s_waitcnt lgkmcnt(4)\n"
  "v_mfma_f32_16x16x32_bf16 v[136:139], a[216:219], a[0:3], v[136:139]\n"
  "ds_read_b128 a[240:243], v18 offset:38912\n"
  "s_waitcnt vmcnt(0)\n"
  "s_mov_b64 exec, 1\n"
  "buffer_store_dword v20, off, s[60:63], s32 offset:24 sc1\n"
  "s_mov_b64 exec, -1\n"
  "v_mfma_f32_16x16x32_bf16 v[140:143], a[216:219], a[4:7], v[140:143]\n"
  "v_mfma_f32_16x16x32_bf16 v[136:139], a[220:223], a[8:11], v[136:139]\n"
  "ds_read_b128 a[244:247], v18 offset:39936\n"
  "v_cvt_pk_bf16_f32 v144, v128, v129\n"
  "v_cvt_pk_bf16_f32 v145, v130, v131\n"
  "v_mfma_f32_16x16x32_bf16 v[140:143], a[220:223], a[12:15], v[140:143]\n"
  "v_cvt_pk_bf16_f32 v146, v132, v133\n"
  "v_cvt_pk_bf16_f32 v147, v134, v135\n"
  "s_waitcnt lgkmcnt(4)\n"
  "v_mfma_f32_16x16x32_bf16 v[136:139], a[224:227], a[16:19], v[136:139]\n"
  "ds_read_b128 a[248:251], v18 offset:40960\n"
  "v_mfma_f32_16x16x32_bf16 v[140:143], a[224:227], a[20:23], v[140:143]\n"
  "buffer_store_dwordx4 v[144:147], v17, s[60:63], s29 offen offset:0 sc1\n"
  "v_mfma_f32_16x16x32_bf16 v[136:139], a[228:231], a[24:27], v[136:139]\n"
  "ds_read_b128 a[252:255], v18 offset:41984\n"
  "v_mfma_f32_16x16x32_bf16 v[140:143], a[228:231], a[28:31], v[140:143]\n"
  "s_waitcnt lgkmcnt(4)\n"
  "v_mfma_f32_16x16x32_bf16 v[136:139], a[232:235], a[32:35], v[136:139]\n"
  "ds_read_b128 a[192:195], v18 offset:43008\n"
  "v_mfma_f32_16x16x32_bf16 v[140:143], a[232:235], a[36:39], v[140:143]\n"
  "v_mfma_f32_16x16x32_bf16 v[136:139], a[236:239], a[40:43], v[136:139]\n"
  "ds_read_b128 a[196:199], v18 offset:44032\n"
  "v_mfma_f32_16x16x32_bf16 v[140:143], a[236:239], a[44:47], v[140:143]\n"
  "s_waitcnt lgkmcnt(4)\n"
  "v_mfma_f32_16x16x32_bf16 v[136:139], a[240:243], a[48:51], v[136:139]\n"
  "ds_read_b128 a[200:203], v18 offset:45056\n"
  "v_mfma_f32_16x16x32_bf16 v[140:143], a[240:243], a[52:55], v[140:143]\n"
  "v_mfma_f32_16x16x32_bf16 v[136:139], a[244:247], a[56:59], v[136:139]\n"
  "ds_read_b128 a[204:207], v18 offset:46080\n"
  "v_mfma_f32_16x16x32_bf16 v[140:143], a[244:247], a[60:63], v[140:143]\n"
  "s_waitcnt lgkmcnt(4)\n"
  "v_mfma_f32_16x16x32_bf16 v[136:139], a[248:251], a[64:67], v[136:139]\n"
  "ds_read_b128 a[208:211], v18 offset:47104\n"
  "v_mfma_f32_16x16x32_bf16 v[140:143], a[248:251], a[68:71], v[140:143]\n"
  "v_mfma_f32_16x16x32_bf16 v[136:139], a[252:255], a[72:75], v[136:139]\n"
  "ds_read_b128 a[212:215], v18 offset:48128\n"
  "v_mfma_f32_16x16x32_bf16 v[140:143], a[252:255], a[76:79], v[140:143]\n"
  "s_waitcnt lgkmcnt(4)\n"
  "v_mfma_f32_16x16x32_bf16 v[136:139], a[192:195], a[80:83], v[136:139]\n"
  "v_mfma_f32_16x16x32_bf16 v[140:143], a[192:195], a[84:87], v[140:143]\n"
  "v_mfma_f32_16x16x32_bf16 v[136:139], a[196:199], a[88:91], v[136:139]\n"
  "v_mfma_f32_16x16x32_bf16 v[140:143], a[196:199], a[92:95], v[140:143]\n"
  "s_waitcnt lgkmcnt(2)\n"
  "v_mfma_f32_16x16x32_bf16 v[136:139], a[200:203], a[96:99], v[136:139]\n"
  "v_mfma_f32_16x16x32_bf16 v[140:143], a[200:203], a[100:103], v[140:143]\n"
  "v_mfma_f32_16x16x32_bf16 v[136:139], a[204:207], a[104:107], v[136:139]\n"
  "v_mfma_f32_16x16x32_bf16 v[140:143], a[204:207], a[108:111], v[140:143]\n"
  "s_waitcnt lgkmcnt(0)\n"
  "v_mfma_f32_16x16x32_bf16 v[136:139], a[208:211], a[112:115], v[136:139]\n"
  "v_mfma_f32_16x16x32_bf16 v[140:143], a[208:211], a[116:119], v[140:143]\n"
  "v_mfma_f32_16x16x32_bf16 v[136:139], a[212:215], a[120:123], v[136:139]\n"
  "v_mfma_f32_16x16x32_bf16 v[140:143], a[212:215], a[124:127], v[140:143]\n"
  "s_nop 6\n"
  "v_cvt_pk_bf16_f32 v148, v136, v137\n"
  "v_cvt_pk_bf16_f32 v149, v138, v139\n"
  "v_cvt_pk_bf16_f32 v150, v140, v141\n"
  "v_cvt_pk_bf16_f32 v151, v142, v143\n"
  "buffer_store_dwordx4 v[148:151], v17, s[60:63], s29 offen offset:1024 sc1\n"
  "s_lshl_b32 s32, s86, 14\n"
  "s_add_u32 s32, s32, s33\n"
  "s_waitcnt vmcnt(0)\n"
  "s_mov_b64 exec, 1\n"
  "buffer_store_dword v20, off, s[60:63], s32 offset:24 sc1\n"
  "s_mov_b64 exec, -1\n"
  "s_waitcnt vmcnt(0)\n"
  "s_cmp_eq_u32 s20, 7\n"
  "s_cbranch_scc0 Lnb%=\n"
  "s_cmp_eq_u32 s24, 0\n"
  "s_cbranch_scc0 Lnb%=\n"
  "s_mov_b32 s75, 0\n"
  "s_movk_i32 s28, 0x2000\n"
  "Lbumpl%=:\n"
  "buffer_load_dwordx4 v[32:35], v17, s[60:63], s28 offen offset:0 sc1\n"
  "buffer_load_dwordx4 v[36:39], v17, s[60:63], s28 offen offset:1024 sc1\n"
  "buffer_load_dwordx4 v[40:43], v17, s[60:63], s28 offen offset:2048 sc1\n"
  "buffer_load_dwordx4 v[44:47], v17, s[60:63], s28 offen offset:3072 sc1\n"
  "s_waitcnt vmcnt(0)\n"
  "v_xor_b32 v32, v32, v20\n"
  "v_xor_b32 v33, v33, v20\n"
  "v_xor_b32 v34, v34, v20\n"
  "v_xor_b32 v35, v35, v20\n"
  "v_xor_b32 v36, v36, v20\n"
  "v_xor_b32 v37, v37, v20\n"
  "v_xor_b32 v38, v38, v20\n"
  "v_xor_b32 v39, v39, v20\n"
  "v_xor_b32 v40, v40, v20\n"
  "v_xor_b32 v41, v41, v20\n"
  "v_xor_b32 v42, v42, v20\n"
  "v_xor_b32 v43, v43, v20\n"
  "v_xor_b32 v44, v44, v20\n"
  "v_xor_b32 v45, v45, v20\n"
  "v_xor_b32 v46, v46, v20\n"
  "v_xor_b32 v47, v47, v20\n"
  "v_or_b32 v32, v32, v33\n"
  "v_or_b32 v32, v32, v34\n"
  "v_or_b32 v32, v32, v35\n"
  "v_or_b32 v32, v32, v36\n"
  "v_or_b32 v32, v32, v37\n"
  "v_or_b32 v32, v32, v38\n"
  "v_or_b32 v32, v32, v39\n"
  "v_or_b32 v32, v32, v40\n"
  "v_or_b32 v32, v32, v41\n"
  "v_or_b32 v32, v32, v42\n"
  "v_or_b32 v32, v32, v43\n"
  "v_or_b32 v32, v32, v44\n"
  "v_or_b32 v32, v32, v45\n"
  "v_or_b32 v32, v32, v46\n"
  "v_or_b32 v32, v32, v47\n"
  "v_cmp_ne_u32 vcc, 0, v32\n"
  "s_nop 1\n"
  "s_and_b64 s[26:27], vcc, exec\n"
  "s_cbranch_scc0 Lbumpd%=\n"
  "s_add_u32 s75, s75, 1\n"
  "s_cmp_lt_u32 s75, 0x8000\n"
  "s_cbranch_scc1 Lbumpl%=\n"
  "Lbumpd%=:\n"
  "s_mov_b32 s29, 0\n"
  "s_mov_b64 exec, 1\n"
  "buffer_store_dword v20, off, s[60:63], s29 sc1\n"
  "s_mov_b64 exec, -1\n"
  "Lnb%=:\n"
  "Lend%=:\n"
  :: "s"(__builtin_amdgcn_kernarg_segment_ptr()),"s"(blockIdx.x),"v"(threadIdx.x) : "v1","v2","v3","v4","v5","v6","v7","v8","v9","v10","v11","v12","v13","v14","v15","v16","v17","v18","v19","v20","v21","v22","v23","v24","v25","v26","v27","v28","v29","v30","v31","v32","v33","v34","v35","v36","v37","v38","v39","v40","v41","v42","v43","v44","v45","v46","v47","v48","v49","v50","v51","v52","v53","v54","v55","v56","v57","v58","v59","v60","v61","v62","v63","v64","v65","v66","v67","v68","v69","v70","v71","v72","v73","v74","v75","v76","v77","v78","v79","v80","v81","v82","v83","v84","v85","v86","v87","v88","v89","v90","v91","v92","v93","v94","v95","v96","v97","v98","v99","v100","v101","v102","v103","v104","v105","v106","v107","v108","v109","v110","v111","v112","v113","v114","v115","v116","v117","v118","v119","v120","v121","v122","v123","v124","v125","v126","v127","v128","v129","v130","v131","v132","v133","v134","v135","v136","v137","v138","v139","v140","v141","v142","v143","v144","v145","v146","v147","v148","v149","v150","v151","v152","v153","v154","v155","v156","v157","v158","v159","v160","v161","v162","v163","v164","v165","v166","v167","v168","v169","v170","v171","v172","v173","v174","v175","v176","v177","v178","v179","v180","v181","v182","v183","v184","v185","v186","v187","v188","v189","v190","v191","v192","v193","v194","v195","v196","v197","v198","v199","v200","v201","v202","v203","v204","v205","v206","v207","v208","v209","v210","v211","v212","v213","v214","v215","v216","v217","v218","v219","v220","v221","v222","v223","v224","v225","v226","v227","v228","v229","v230","v231","v232","v233","v234","v235","v236","v237","v238","v239","v240","v241","v242","v243","v244","v245","v246","v247","v248","v249","v250","v251","v252","v253","v254","v255","a0","a1","a2","a3","a4","a5","a6","a7","a8","a9","a10","a11","a12","a13","a14","a15","a16","a17","a18","a19","a20","a21","a22","a23","a24","a25","a26","a27","a28","a29","a30","a31","a32","a33","a34","a35","a36","a37","a38","a39","a40","a41","a42","a43","a44","a45","a46","a47","a48","a49","a50","a51","a52","a53","a54","a55","a56","a57","a58","a59","a60","a61","a62","a63","a64","a65","a66","a67","a68","a69","a70","a71","a72","a73","a74","a75","a76","a77","a78","a79","a80","a81","a82","a83","a84","a85","a86","a87","a88","a89","a90","a91","a92","a93","a94","a95","a96","a97","a98","a99","a100","a101","a102","a103","a104","a105","a106","a107","a108","a109","a110","a111","a112","a113","a114","a115","a116","a117","a118","a119","a120","a121","a122","a123","a124","a125","a126","a127","a128","a129","a130","a131","a132","a133","a134","a135","a136","a137","a138","a139","a140","a141","a142","a143","a144","a145","a146","a147","a148","a149","a150","a151","a152","a153","a154","a155","a156","a157","a158","a159","a160","a161","a162","a163","a164","a165","a166","a167","a168","a169","a170","a171","a172","a173","a174","a175","a176","a177","a178","a179","a180","a181","a182","a183","a184","a185","a186","a187","a188","a189","a190","a191","a192","a193","a194","a195","a196","a197","a198","a199","a200","a201","a202","a203","a204","a205","a206","a207","a208","a209","a210","a211","a212","a213","a214","a215","a216","a217","a218","a219","a220","a221","a222","a223","a224","a225","a226","a227","a228","a229","a230","a231","a232","a233","a234","a235","a236","a237","a238","a239","a240","a241","a242","a243","a244","a245","a246","a247","a248","a249","a250","a251","a252","a253","a254","a255","s20","s21","s22","s23","s24","s25","s26","s27","s28","s29","s30","s31","s34","s35","s36","s37","s38","s39","s40","s41","s42","s43","s44","s45","s46","s47","s48","s49","s50","s51","s52","s53","s54","s55","s56","s57","s58","s59","s60","s61","s62","s63","s64","s65","s66","s67","s68","s69","s70","s71","s72","s73","s74","s75","s76","s77","s78","s79","s80","s81","s82","s83","s84","s85","s86","s87","s88","s89","s90","s91","s92","s93","s94","s95","s96","s97","s98","s99","vcc","scc","memory");
}
extern "C" void kernel_launch(void* const* d_in, const int* in_sizes, int n_in, void* d_out, int out_size, void* d_ws, size_t ws_size, hipStream_t stream) {
  (void)hipFuncSetAttribute((const void*)k_fused, hipFuncAttributeMaxDynamicSharedMemorySize, 163840);
  k_fused<<<256,256,163840,stream>>>(d_in[0],d_in[1],d_in[2],d_in[3],d_in[4],d_in[6],d_in[7],d_out,d_ws);
}
```
